```python
import torch

def _softmax(x, axis=-1):
    return torch.softmax(x, dim=axis)
def _gelu(x):
    return torch.nn.functional.gelu(x, approximate="tanh")
def _silu(x):
    return torch.nn.functional.silu(x)
def _sigmoid(x):
    return torch.sigmoid(x)
def _layernorm(x, axis=-1, eps=1e-5):
    m = x.mean(dim=axis, keepdim=True)
    v = x.var(dim=axis, keepdim=True, unbiased=False)
    return (x - m) / torch.sqrt(v + eps)
def _scatter_add(tgt, idx, src, axis=0):
    return tgt.clone().index_add_(axis, idx, src)
def _segment_sum(src, seg, n):
    out = torch.zeros((n,) + src.shape[1:], dtype=src.dtype)
    return out.index_add_(0, seg, src)
def _pad(x, pw):
    flat = tuple(v for pair in reversed(pw) for v in pair)
    return torch.nn.functional.pad(x, flat)

kernel_name = 'sequential_preLN__ssm_conv_h3__glu_gated_reglu__prompt'

def setup_inputs():

    _w = (lambda a: (lambda t: t.to(torch.bfloat16) if t.is_floating_point() else t)(torch.as_tensor(a)))
    def _A_setup():
        B, S, D = 1, 2048, 640
        ks = 4
        return {
            "q": _w((lambda *s: torch.randn(*s, dtype=torch.bfloat16) * 0.05)(B, S, D)),
            "k": _w((lambda *s: torch.randn(*s, dtype=torch.bfloat16) * 0.2)(B, S, D)),
            "v": _w((lambda *s: torch.randn(*s, dtype=torch.bfloat16) * 0.2)(B, S, D)),
            "shift_k": _w((lambda *s: torch.randn(*s, dtype=torch.bfloat16) * 0.2)(D, ks) * 0.5),
            "ssm_k": _w((lambda *s: torch.randn(*s, dtype=torch.bfloat16) * 0.4)(D, S) * (1.0 / S ** 0.5)),
            "ssm_D": _w((lambda *s: torch.randn(*s, dtype=torch.bfloat16) * 0.2)(D)),
        }
    _A_in = _A_setup()

    def _B_setup():
        B, S, D, F = 1, 2048, 640, 1728
        return {
            "x": _w((lambda *s: torch.randn(*s, dtype=torch.bfloat16) * 0.05)(B, S, D)),
            "W_gate": _w((lambda *s: torch.randn(*s, dtype=torch.bfloat16) * 0.2)(D, F) * (2.0 / D) ** 0.5),
            "W_up": _w((lambda *s: torch.randn(*s, dtype=torch.bfloat16) * 0.2)(D, F) * (2.0 / D) ** 0.5),
            "W_down": _w((lambda *s: torch.randn(*s, dtype=torch.bfloat16) * 0.2)(F, D) * (1.0 / F) ** 0.5),
        }
    _B_in = _B_setup()

    return {
        "A_q": _A_in["q"],
        "A_k": _A_in["k"],
        "A_v": _A_in["v"],
        "A_shift_k": _A_in["shift_k"],
        "A_ssm_k": _A_in["ssm_k"],
        "A_ssm_D": _A_in["ssm_D"],
        "B_x": _B_in["x"],
        "B_W_gate": _B_in["W_gate"],
        "B_W_up": _B_in["W_up"],
        "B_W_down": _B_in["W_down"],
    }

def reference(A_q, A_k, A_v, A_shift_k, A_ssm_k, A_ssm_D, B_x, B_W_gate, B_W_up, B_W_down):
    def _A_ref(q, k, v, shift_k, ssm_k, ssm_D):
        B, S, D = q.shape
        ks = shift_k.shape[-1]
        k_pad = _pad(k, ((0, 0), (ks - 1, 0), (0, 0)))
        k_shift = torch.zeros_like(k)
        for t in range(ks):
            k_shift = k_shift + k_pad[:, t:t + S, :] * shift_k[:, ks - 1 - t][None, None, :]
        kv = k_shift * v
        L = 2 * S
        kv_pad = _pad(kv, ((0, 0), (0, S), (0, 0)))
        kf_pad = _pad(ssm_k, ((0, 0), (0, S)))
        kv_f = (lambda x, axes=(-2,-1): torch.fft.rfft2(x.float(), dim=axes))(kv_pad[:, None, :, :], axes=(1, 2))
        kf_f = (lambda x, axes=(-2,-1): torch.fft.rfft2(x.float(), dim=axes))(kf_pad[:, None, :], axes=(1, 2))
        kf_f = (lambda x, *d: x.permute(*d))(kf_f, 1, 2, 0)
        y_f = kv_f * kf_f[None, :, :, :]
        y = (lambda x, s, axes=(-2,-1): torch.fft.irfft2(x, s=s, dim=axes).to(torch.bfloat16))(y_f, (1, L), axes=(1, 2))[:, 0, :S, :]
        y = y + kv * ssm_D[None, None, :]
        return q * y

    def _B_ref(x, W_gate, W_up, W_down):
        gate = torch.einsum("bsd,df->bsf", x, W_gate)
        up = torch.einsum("bsd,df->bsf", x, W_up)
        gate = gate * (lambda x, dt: x.to(dt))(gate > 0, gate.dtype)
        h = gate * up
        return torch.einsum("bsf,fd->bsd", h, W_down)

    x = A_q
    h = x + _A_ref(_layernorm(x, axis=-1), A_k, A_v, A_shift_k, A_ssm_k, A_ssm_D)
    h2 = h + _B_ref(_layernorm(h, axis=-1), B_W_gate, B_W_up, B_W_down)
    return h2
```

```cpp
#include <hip/hip_runtime.h>
#include <hip/hip_bf16.h>
typedef unsigned short u16;
static constexpr int S=2048, D=640, F=1728;
__device__ __forceinline__ float b2f(u16 x){ return __uint_as_float(((unsigned)x)<<16); }
__device__ __forceinline__ u16 f2b(float f){ unsigned u=__float_as_uint(f); u = (u + 0x7FFF + ((u>>16)&1))>>16; return (u16)u; }
__device__ __forceinline__ float rb(float f){ return b2f(f2b(f)); }

__global__ void k_kv(const u16* k,const u16* v,const u16* sk,u16* kv){
  int i=blockIdx.x*256+threadIdx.x; if(i>=S*D) return; int s=i/D,d=i%D;
  float acc=0;
  for(int t=0;t<4;++t){ int ss=s+t-3; float kk= ss>=0? b2f(k[ss*D+d]):0.f; acc=rb(acc+rb(kk*b2f(sk[d*4+3-t]))); }
  kv[i]=f2b(acc*b2f(v[i]));
}
__device__ void ln_row(const float* xs  , float& m, float& sd, float* red){
  float s=0; for(int d=threadIdx.x;d<D;d+=256) s+=xs[d];
  red[threadIdx.x]=s; __syncthreads();
  for(int o=128;o>0;o>>=1){ if(threadIdx.x<o) red[threadIdx.x]+=red[threadIdx.x+o]; __syncthreads(); }
  float mean=red[0]/D; __syncthreads();
  float q=0; for(int d=threadIdx.x;d<D;d+=256){ float t=xs[d]-mean; q+=t*t; }
  red[threadIdx.x]=q; __syncthreads();
  for(int o=128;o>0;o>>=1){ if(threadIdx.x<o) red[threadIdx.x]+=red[threadIdx.x+o]; __syncthreads(); }
  float var=red[0]/D; __syncthreads();
  m=rb(mean); sd=rb(sqrtf(rb(rb(var)+1e-5f)));
}
__global__ void k_conv(const u16* kv,const u16* f,u16* y){
  int d=blockIdx.x; __shared__ float u[S]; __shared__ float w[S];
  for(int s=threadIdx.x;s<S;s+=256){ u[s]=b2f(kv[s*D+d]); w[s]=b2f(f[d*S+s]); }
  __syncthreads();
  for(int s=threadIdx.x;s<S;s+=256){ float a=0; for(int j=0;j<=s;++j) a+=w[j]*u[s-j]; y[s*D+d]=f2b(a); }
}
__global__ void k_mid(const u16* x,const u16* kv,const u16* y,const u16* sD,u16* h,u16* hn){
  int s=blockIdx.x; __shared__ float xs[D]; __shared__ float red[256];
  for(int d=threadIdx.x;d<D;d+=256) xs[d]=b2f(x[s*D+d]);
  __syncthreads();
  float m,sd; ln_row(xs,m,sd,red);
  for(int d=threadIdx.x;d<D;d+=256){
    float q=rb(rb(xs[d]-m)/sd);
    float yy=rb(b2f(y[s*D+d])+rb(b2f(kv[s*D+d])*b2f(sD[d])));
    float hh=rb(xs[d]+rb(q*yy));
    h[s*D+d]=f2b(hh); xs[d]=hh;
  }
  __syncthreads();
  ln_row(xs,m,sd,red);
  for(int d=threadIdx.x;d<D;d+=256) hn[s*D+d]=f2b(rb(xs[d]-m)/sd);
}
__global__ void k_g1(const u16* hn,const u16* wg,const u16* wu,u16* hf){
  int s=blockIdx.y, f=blockIdx.x*256+threadIdx.x; if(f>=F) return;
  float g=0,u=0; for(int d=0;d<D;++d){ float a=b2f(hn[s*D+d]); g+=a*b2f(wg[d*F+f]); u+=a*b2f(wu[d*F+f]); }
  g=rb(g); u=rb(u); g = g>0? g:0.f*g; hf[s*F+f]=f2b(g*u);
}
__global__ void k_g2(const u16* hf,const u16* wd,const u16* h,u16* out){
  int s=blockIdx.y, d=blockIdx.x*128+threadIdx.x; if(d>=D) return;
  float a=0; for(int f=0;f<F;++f) a+=b2f(hf[s*F+f])*b2f(wd[f*D+d]);
  out[s*D+d]=f2b(b2f(h[s*D+d])+rb(a));
}
extern "C" void kernel_launch(void* const* d_in, const int* in_sizes, int n_in, void* d_out, int out_size, void* d_ws, size_t ws_size, hipStream_t stream){
  const u16* q=(const u16*)d_in[0]; const u16* k=(const u16*)d_in[1]; const u16* v=(const u16*)d_in[2];
  const u16* sk=(const u16*)d_in[3]; const u16* fk=(const u16*)d_in[4]; const u16* sD=(const u16*)d_in[5];
  const u16* wg=(const u16*)d_in[7]; const u16* wu=(const u16*)d_in[8]; const u16* wd=(const u16*)d_in[9];
  u16* ws=(u16*)d_ws; u16* kv=ws; u16* y=kv+S*D; u16* h=y+S*D; u16* hn=h+S*D; u16* hf=hn+S*D;
  k_kv<<<S*D/256,256,0,stream>>>(k,v,sk,kv);
  k_conv<<<D,256,0,stream>>>(kv,fk,y);
  k_mid<<<S,256,0,stream>>>(q,kv,y,sD,h,hn);
  k_g1<<<dim3((F+255)/256,S),256,0,stream>>>(hn,wg,wu,hf);
  k_g2<<<dim3(5,S),128,0,stream>>>(hf,wd,h,(u16*)d_out);
}
```

```cpp
#include <hip/hip_runtime.h>
#include <hip/hip_bf16.h>

typedef unsigned short u16;
typedef unsigned int u32;
typedef __attribute__((ext_vector_type(2))) unsigned int u32x2;
typedef __attribute__((ext_vector_type(4))) unsigned int u32x4;
typedef __attribute__((ext_vector_type(8))) short bf16x8;
typedef __attribute__((ext_vector_type(2))) __bf16 bf16x2;
typedef __attribute__((ext_vector_type(2))) float f32x2;
typedef __attribute__((ext_vector_type(4))) float f32x4;
typedef __attribute__((ext_vector_type(4))) unsigned short u16x4;
#define LAS __attribute__((address_space(3)))

static constexpr int S = 2048, D = 640, F = 1728;
static constexpr int NKS1 = D / 32;
static constexpr int NKS2 = F / 32;

static constexpr size_t WS_UR1 = 0;
static constexpr size_t WS_UR0 = WS_UR1 + (size_t)D * S * 2;
static constexpr size_t WS_XT  = WS_UR0 + (size_t)D * S * 2;
static constexpr size_t WS_HT  = WS_XT  + (size_t)D * S * 2;
static constexpr size_t WS_ST1 = WS_HT  + (size_t)D * S * 2;
static constexpr size_t WS_CS  = WS_ST1 + (size_t)S * 8;
static constexpr size_t WS_W1T = WS_CS  + (size_t)16 * 14 * 16 * 4;
static constexpr size_t WS_W2T = WS_W1T + (size_t)16 * NKS1 * 14 * 1024;
static constexpr size_t WS_HF  = WS_W2T + (size_t)8 * NKS2 * 5 * 1024;
static constexpr size_t WS_END = WS_HF  + (size_t)128 * NKS2 * 1024;

__device__ __forceinline__ float bflo(u32 w) { return __uint_as_float(w << 16); }
__device__ __forceinline__ float bfhi(u32 w) { return __uint_as_float(w & 0xffff0000u); }
__device__ __forceinline__ u32 pack2(float lo, float hi) { f32x2 v = {lo, hi}; bf16x2 b = __builtin_convertvector(v, bf16x2); return __builtin_bit_cast(u32, b); }
__device__ __forceinline__ float rbf(float x) { return bflo(pack2(x, 0.f)); }
__device__ __forceinline__ float dot2(u32 a, u32 b, float c) { return __builtin_amdgcn_fdot2_f32_bf16(__builtin_bit_cast(bf16x2, a), __builtin_bit_cast(bf16x2, b), c, false); }
__device__ __forceinline__ f32x4 mfma16(u32x4 a, u32x4 b, f32x4 c) { return __builtin_amdgcn_mfma_f32_16x16x32_bf16(__builtin_bit_cast(bf16x8, a), __builtin_bit_cast(bf16x8, b), c, 0, 0, 0); }
__device__ __forceinline__ void dma16(const void* g, LAS void* l) { __builtin_amdgcn_global_load_lds((const u32*)g, (LAS u32*)l, 16, 0, 0); }
#define WAIT_VM(n) asm volatile("s_waitcnt vmcnt(%0)" ::"n"(n) : "memory")
#define WAIT_LGKM0() asm volatile("s_waitcnt lgkmcnt(0)" ::: "memory")
#define BAR() do { asm volatile("" ::: "memory"); __builtin_amdgcn_s_barrier(); asm volatile("" ::: "memory"); } while (0)

#define STAMP(kid, i) do {} while (0)
__host__ __device__ __forceinline__ int first_block(int ct) { return ct < 12 ? 7 * ct : 84 + 6 * (ct - 12); }
__host__ __device__ __forceinline__ int tile_of_block(int b) { return b < 84 ? b / 7 : 12 + (b - 84) / 6; }

static constexpr int K0_TILES = 32 * 10, K0_STATS = 128;
__global__ __launch_bounds__(256) void k0_prep(const u16* __restrict__ x, const u16* __restrict__ k, const u16* __restrict__ v,
                                               const u16* __restrict__ shk, char* __restrict__ ws) {
  __shared__ __attribute__((aligned(16))) char lds[3 * 64 * 144];
  const int t = threadIdx.x, b = blockIdx.x;
  STAMP(0, 0);
  if (b < K0_TILES) {
    const int st = b / 10, ct = b % 10, s0 = st * 64, c0 = ct * 64;
    const int cp = t & 31, rg = t >> 5, ch = c0 + 2 * cp, r0 = s0 + 8 * rg;
    u32 kk[12], vv[9], xx[8];
#pragma unroll
    for (int i = 0; i < 12; ++i) { int r = r0 - 4 + i; kk[i] = r >= 0 ? *(const u32*)(k + (size_t)r * D + ch) : 0u; }
#pragma unroll
    for (int i = 0; i < 9; ++i) { int r = r0 - 1 + i; vv[i] = r >= 0 ? *(const u32*)(v + (size_t)r * D + ch) : 0u; }
#pragma unroll
    for (int i = 0; i < 8; ++i) xx[i] = *(const u32*)(x + (size_t)(r0 + i) * D + ch);
    const u32x4 sh = *(const u32x4*)(shk + ch * 4);
    const float a0 = bflo(sh.x), a1 = bfhi(sh.x), a2 = bflo(sh.y), a3 = bfhi(sh.y);
    const float b0 = bflo(sh.z), b1 = bfhi(sh.z), b2 = bflo(sh.w), b3 = bfhi(sh.w);
    float ua[9], ub[9];
#pragma unroll
    for (int i = 0; i < 9; ++i) {
      float sa = bflo(kk[i]) * a3; sa = rbf(sa); sa = rbf(sa + rbf(bflo(kk[i + 1]) * a2)); sa = rbf(sa + rbf(bflo(kk[i + 2]) * a1)); sa = rbf(sa + rbf(bflo(kk[i + 3]) * a0));
      float sb = bfhi(kk[i]) * b3; sb = rbf(sb); sb = rbf(sb + rbf(bfhi(kk[i + 1]) * b2)); sb = rbf(sb + rbf(bfhi(kk[i + 2]) * b1)); sb = rbf(sb + rbf(bfhi(kk[i + 3]) * b0));
      ua[i] = sa * bflo(vv[i]); ub[i] = sb * bfhi(vv[i]);
    }
    STAMP(0, 1);
    u32x4 w;
    char* L1 = lds; char* L0 = lds + 64 * 144; char* LX = lds + 2 * 64 * 144;
    const int o = (7 - rg) * 16;
    w = (u32x4){pack2(ua[8], ua[7]), pack2(ua[6], ua[5]), pack2(ua[4], ua[3]), pack2(ua[2], ua[1])}; *(u32x4*)(L1 + (2 * cp) * 144 + o) = w;
    w = (u32x4){pack2(ub[8], ub[7]), pack2(ub[6], ub[5]), pack2(ub[4], ub[3]), pack2(ub[2], ub[1])}; *(u32x4*)(L1 + (2 * cp + 1) * 144 + o) = w;
    w = (u32x4){pack2(ua[7], ua[6]), pack2(ua[5], ua[4]), pack2(ua[3], ua[2]), pack2(ua[1], ua[0])}; *(u32x4*)(L0 + (2 * cp) * 144 + o) = w;
    w = (u32x4){pack2(ub[7], ub[6]), pack2(ub[5], ub[4]), pack2(ub[3], ub[2]), pack2(ub[1], ub[0])}; *(u32x4*)(L0 + (2 * cp + 1) * 144 + o) = w;
    u32x4 xa, xb;
#pragma unroll
    for (int i = 0; i < 4; ++i) { xa[i] = (xx[2 * i] & 0xffffu) | (xx[2 * i + 1] << 16); xb[i] = (xx[2 * i] >> 16) | (xx[2 * i + 1] & 0xffff0000u); }
    *(u32x4*)(LX + (2 * cp) * 144 + rg * 16) = xa; *(u32x4*)(LX + (2 * cp + 1) * 144 + rg * 16) = xb;
    __syncthreads();
#pragma unroll
    for (int i = 0; i < 2; ++i) {
      const int row = (t >> 3) + 32 * i, c = t & 7, d = c0 + row;
      *(u32x4*)(ws + WS_UR1 + ((size_t)d * S + (1984 - s0)) * 2 + c * 16) = *(const u32x4*)(L1 + row * 144 + c * 16);
      *(u32x4*)(ws + WS_UR0 + ((size_t)d * S + (1984 - s0)) * 2 + c * 16) = *(const u32x4*)(L0 + row * 144 + c * 16);
      *(u32x4*)(ws + WS_XT + ((size_t)d * S + s0) * 2 + c * 16) = *(const u32x4*)(LX + row * 144 + c * 16);
    }
    STAMP(0, 2);
  } else {
    const int w = t >> 6, lane = t & 63, rbase = (b - K0_TILES) * 16 + w * 4;
    u32 xv[4][5];
#pragma unroll
    for (int r = 0; r < 4; ++r)
#pragma unroll
      for (int j = 0; j < 5; ++j) xv[r][j] = *(const u32*)(x + (size_t)(rbase + r) * D + j * 128 + lane * 2);
#pragma unroll
    for (int r = 0; r < 4; ++r) {
      float s = 0.f;
#pragma unroll
      for (int j = 0; j < 5; ++j) s += bflo(xv[r][j]) + bfhi(xv[r][j]);
#pragma unroll
      for (int o = 32; o > 0; o >>= 1) s += __shfl_xor(s, o);
      const float mean = s * (1.0f / D);
      float q = 0.f;
#pragma unroll
      for (int j = 0; j < 5; ++j) { float a = bflo(xv[r][j]) - mean, c = bfhi(xv[r][j]) - mean; q += a * a + c * c; }
#pragma unroll
      for (int o = 32; o > 0; o >>= 1) q += __shfl_xor(q, o);
      const float var = q * (1.0f / D);
      const float sd = rbf(sqrtf(rbf(rbf(var) + 1e-5f)));
      if (lane == 0) *(f32x2*)(ws + WS_ST1 + (size_t)(rbase + r) * 8) = (f32x2){rbf(mean), 1.0f / sd};
    }
    STAMP(0, 2);
  }
}

static constexpr int CH_BYTES = 13568, CH_F = 0, CH_U1 = 4224, CH_U0 = 4224 + 4608 + 32;
static constexpr int K1_XCH = 4 * CH_BYTES;
static constexpr int K1_LDS = K1_XCH + 8 * 4096;
static constexpr int K1_CONV = 160, K1_W1 = 54, K1_W2 = 30;

__device__ __forceinline__ u16x4 tr_read(u32 addr) { u16x4 r; asm volatile("ds_read_b64_tr_b16 %0, %1" : "=v"(r) : "v"(addr) : "memory"); return r; }

__global__ __launch_bounds__(512, 2) void k1_conv(const u16* __restrict__ fk, const u16* __restrict__ sD, const u16* __restrict__ wg, const u16* __restrict__ wu,
                                                  const u16* __restrict__ wd, char* __restrict__ ws) {
  extern __shared__ __attribute__((aligned(16))) char shm[];
  LAS char* lds = (LAS char*)shm;
  const int t = threadIdx.x, lane = t & 63, w = __builtin_amdgcn_readfirstlane(t >> 6), b = blockIdx.x;
  STAMP(1, 0);
  if (b < K1_CONV) {
    const int c = w & 3, hf = w >> 2, d = b * 4 + c;
    LAS char* cb = lds + c * CH_BYTES;
    {
      const char* gF = (const char*)fk + (size_t)d * S * 2 + lane * 16;
      const char* g1 = ws + WS_UR1 + (size_t)d * S * 2 + lane * 16;
      const char* g0 = ws + WS_UR0 + (size_t)d * S * 2 + lane * 16;
#pragma unroll
      for (int i = 0; i < 2; ++i) {
        const int pc = hf * 2 + i;
        dma16(g1 + pc * 1024, cb + CH_U1 + pc * 1024);
        dma16(g0 + pc * 1024, cb + CH_U0 + pc * 1024);
        dma16(gF + pc * 1024, cb + CH_F + 64 + pc * 1024);
      }
      if (hf == 0) { if (lane < 32) *(LAS u32x4*)(cb + CH_U1 + 4096 + lane * 16) = (u32x4){0, 0, 0, 0}; if (lane < 4) *(LAS u32x4*)(cb + CH_F + lane * 16) = (u32x4){0, 0, 0, 0}; }
      else { if (lane < 32) *(LAS u32x4*)(cb + CH_U0 + 4096 + lane * 16) = (u32x4){0, 0, 0, 0}; }
    }
    const int r = lane & 15, g = lane >> 4;
    u32 xw[8]; f32x4 stv[8];
#pragma unroll
    for (int j = 0; j < 8; ++j) {
      const int s = 256 * (4 * hf + (j >> 1)) + 32 * (2 * g + (j & 1)) + 2 * r;
      xw[j] = *(const u32*)(ws + WS_XT + ((size_t)d * S + s) * 2);
      stv[j] = *(const f32x4*)(ws + WS_ST1 + (size_t)s * 8);
    }
    const float Dd = bflo((u32)sD[d]);
    WAIT_VM(0); WAIT_LGKM0(); BAR();
    STAMP(1, 1);
    f32x4 acc[8];
#pragma unroll
    for (int i = 0; i < 8; ++i) acc[i] = (f32x4){0.f, 0.f, 0.f, 0.f};
    const int a = r >> 1, e = r & 1;
    LAS char* ub = cb + (e ? CH_U1 : CH_U0) + 448 - 64 * a + 16 * g + 256 * hf;
    LAS char* fb = cb + CH_F + 16 * g + 4 * r + 4 + 256 * hf;
#pragma unroll
    for (int cc = 0; cc < 4; ++cc) {
      u32x4 U[8], Fr[8];
#pragma unroll
      for (int i = 0; i < 8; ++i) {
        U[i] = *(LAS const u32x4*)(ub + 512 * (7 - i) + 64 * cc);
        LAS const u32* p = (LAS const u32*)(fb + 512 * i + 64 * cc);
        Fr[i] = (u32x4){p[0], p[1], p[2], p[3]};
      }
#pragma unroll
      for (int c1 = 0; c1 < 8; ++c1)
#pragma unroll
        for (int i = c1; i < 8; ++i) acc[i] = mfma16(U[i - c1], Fr[c1], acc[i]);
    }
    STAMP(1, 2);
    LAS char* xo = lds + K1_XCH + w * 4096 + lane * 16;
    LAS char* xi = lds + K1_XCH + (w ^ 4) * 4096 + lane * 16;
#pragma unroll
    for (int j = 0; j < 4; ++j) *(LAS f32x4*)(xo + j * 1024) = hf ? acc[j] : acc[4 + j];
    WAIT_LGKM0(); BAR();
    LAS char* u1 = cb + CH_U1;
#pragma unroll
    for (int j = 0; j < 4; ++j) {
      f32x4 y = *(LAS const f32x4*)(xi + j * 1024);
      y += hf ? acc[4 + j] : acc[j];
#pragma unroll
      for (int hh = 0; hh < 2; ++hh) {
        const int s = 256 * (4 * hf + j) + 32 * (2 * g + hh) + 2 * r;
        const u32 uw = *(LAS const u32*)(u1 + (2046 - s) * 2);
        const u32 xx = xw[2 * j + hh]; const f32x4 st = stv[2 * j + hh];
        const float x0 = bflo(xx), x1 = bfhi(xx);
        const float y0 = y[2 * hh] + bfhi(uw) * Dd, y1 = y[2 * hh + 1] + bflo(uw) * Dd;
        const float h0 = x0 + (x0 - st[0]) * st[1] * y0, h1 = x1 + (x1 - st[2]) * st[3] * y1;
        *(u32*)(ws + WS_HT + ((size_t)d * S + s) * 2) = pack2(h0, h1);
      }
    }
    STAMP(1, 3);
  } else {
    const int jb = b - K1_CONV;
    if (jb >= K1_W1 + K1_W2) return;
    const bool isW1 = jb < K1_W1;
    const u16* src; int pitch, ks0, nks;
    int cg;
    if (isW1) { cg = jb >> 1; src = ((jb & 1) ? wu : wg) + cg * 64; pitch = F; ks0 = 0; nks = NKS1; }
    else { const int j2 = jb - K1_W1; cg = j2 / 3; src = wd + cg * 64; pitch = D; ks0 = (j2 % 3) * 18; nks = 18; }
    LAS char* my = lds + w * 4096;
    const u32 myaddr = (u32)(size_t)my;
    float cs[4] = {0.f, 0.f, 0.f, 0.f};
    const int g = lane >> 4, q = (lane & 15) >> 2, p = lane & 3;
    u32x4 ld[3][4];
#pragma unroll
    for (int it = 0; it < 3; ++it) {
      const int ks = w + 8 * it;
      if (ks < nks) {
#pragma unroll
        for (int i = 0; i < 4; ++i) ld[it][i] = *(const u32x4*)(src + (size_t)((ks0 + ks) * 32 + 8 * i + (lane >> 3)) * pitch + (lane & 7) * 8);
      }
    }
#pragma unroll
    for (int it = 0; it < 3; ++it) {
      const int ks = w + 8 * it;
      if (ks < nks) {
#pragma unroll
        for (int i = 0; i < 4; ++i) { const int row = 8 * i + (lane >> 3); *(LAS u32x4*)(my + row * 128 + (((lane & 7) ^ (row & 7)) * 16)) = ld[it][i]; }
        WAIT_LGKM0();
#pragma unroll
        for (int fb = 0; fb < 4; ++fb) {
          const int row0 = isW1 ? 8 * g + q : 4 * g + q, row1 = isW1 ? 8 * g + 4 + q : 16 + 4 * g + q;
          const int chk = 2 * fb + (p >> 1);
          u16x4 lo = tr_read(myaddr + row0 * 128 + ((chk ^ (row0 & 7)) * 16) + 8 * (p & 1));
          u16x4 hi = tr_read(myaddr + row1 * 128 + ((chk ^ (row1 & 7)) * 16) + 8 * (p & 1));
          asm volatile("s_waitcnt lgkmcnt(0)" : "+v"(lo), "+v"(hi)::"memory");
          const u32x2 l2 = __builtin_bit_cast(u32x2, lo), h2 = __builtin_bit_cast(u32x2, hi);
          const u32x4 fr = (u32x4){l2.x, l2.y, h2.x, h2.y};
          if (isW1) {
            const int blk = cg * 4 + fb, ct = tile_of_block(blk), j = 2 * (blk - first_block(ct)) + (jb & 1);
            *(u32x4*)(ws + WS_W1T + ((size_t)(ct * NKS1 + ks) * 14 + j) * 1024 + lane * 16) = fr;
            cs[fb] = dot2(fr.x, 0x3f803f80u, dot2(fr.y, 0x3f803f80u, dot2(fr.z, 0x3f803f80u, dot2(fr.w, 0x3f803f80u, cs[fb]))));
          } else {
            const int df = cg * 4 + fb, dt = df / 5, j = df % 5;
            *(u32x4*)(ws + WS_W2T + ((size_t)(dt * NKS2 + ks0 + ks) * 5 + j) * 1024 + lane * 16) = fr;
          }
        }
      }
    }
    STAMP(1, 3);
    if (isW1) {
      LAS float* red = (LAS float*)(lds + 8 * 4096);
#pragma unroll
      for (int fb = 0; fb < 4; ++fb) { float s = cs[fb]; s += __shfl_xor(s, 16); s += __shfl_xor(s, 32); if (lane < 16) red[w * 64 + fb * 16 + lane] = s; }
      __syncthreads();
      if (t < 64) {
        float s = 0.f;
#pragma unroll
        for (int i = 0; i < 8; ++i) s += red[i * 64 + t];
        const int blk = cg * 4 + (t >> 4), ct = tile_of_block(blk), j = 2 * (blk - first_block(ct)) + (jb & 1);
        *(float*)(ws + WS_CS + ((size_t)(ct * 14 + j) * 16 + (t & 15)) * 4) = s;
      }
      if (cg == 26) {
        for (int i = t; i < 4 * NKS1 * 64; i += 512) { const int ct = 12 + i / (NKS1 * 64), ks = (i / 64) % NKS1; *(u32x4*)(ws + WS_W1T + ((size_t)(ct * NKS1 + ks) * 14 + 12 + (jb & 1)) * 1024 + (i & 63) * 16) = (u32x4){0, 0, 0, 0}; }
        if (t < 64) *(float*)(ws + WS_CS + ((size_t)((12 + (t >> 4)) * 14 + 12 + (jb & 1)) * 16 + (t & 15)) * 4) = 0.f;
      }
    }
  }
}

static constexpr int G1_SLOT = 22 * 1024, G1_NS = 6, G1_LDS = G1_SLOT * G1_NS;
typedef __attribute__((ext_vector_type(4))) short s16x4;
__device__ __forceinline__ u32x2 tr_rd(LAS char* p) { return __builtin_bit_cast(u32x2, __builtin_amdgcn_ds_read_tr16_b64_v4i16((LAS s16x4*)p)); }

__global__ __launch_bounds__(512, 2) void k2_gemm1(char* __restrict__ ws) {
  extern __shared__ __attribute__((aligned(16))) char shm[];
  LAS char* lds = (LAS char*)shm;
  const int t = threadIdx.x, lane = t & 63, w = __builtin_amdgcn_readfirstlane(t >> 6);
  STAMP(2, 0);
  const int xg = blockIdx.x & 7, ix = blockIdx.x >> 3;
  const int rt = (xg & 1) * 8 + (ix & 7), ct = (xg >> 1) * 4 + (ix >> 3);
  const int s0 = rt * 128;
  const int wsn = w & 3, wn = w >> 2;
  const int hrow = 4 * w + (lane >> 4);
  const int hsw = ((hrow & 3) << 2) | ((hrow >> 2) & 3);
  const char* hsrc = ws + WS_HT + ((size_t)hrow * S + s0) * 2 + (((lane & 15) ^ hsw) * 16);
  const char* wsrc = ws + WS_W1T + (size_t)ct * NKS1 * 14 * 1024 + w * 1792 + lane * 16;
  int islot = 0;
  auto issue = [&]() {
    LAS char* sl = lds + islot;
    dma16(hsrc, sl + w * 1024);
    dma16(wsrc, sl + 8192 + w * 1792);
    if (lane < 48) dma16(wsrc + 1024, sl + 8192 + w * 1792 + 1024);
    hsrc += 32 * S * 2; wsrc += 14 * 1024;
    islot = islot == (G1_NS - 1) * G1_SLOT ? 0 : islot + G1_SLOT;
  };
#pragma unroll
  for (int ks = 0; ks < G1_NS - 1; ++ks) issue();
  const int g = lane >> 4, q = (lane & 15) >> 2, p = lane & 3;
  int hoff[2][2];
#pragma unroll
  for (int sf = 0; sf < 2; ++sf)
#pragma unroll
    for (int tt = 0; tt < 2; ++tt) {
      const int row = 8 * g + 4 * tt + q, chk = 2 * (2 * wsn + sf) + (p >> 1), sw = ((row & 3) << 2) | ((row >> 2) & 3);
      hoff[sf][tt] = 256 * row + 16 * (chk ^ sw) + 8 * (p & 1);
    }
  const int woff = 8192 + wn * 7 * 1024 + lane * 16;
  f32x4 acc[2][7];
#pragma unroll
  for (int i = 0; i < 2; ++i)
#pragma unroll
    for (int j = 0; j < 7; ++j) acc[i][j] = (f32x4){0.f, 0.f, 0.f, 0.f};
  float ssum = 0.f, ssq = 0.f;
  u32x4 hA[2], wA[7], hB[2], wB[7];
  int rslot = 0;
  auto readfr = [&](u32x4 (&h)[2], u32x4 (&wf)[7]) {
    LAS char* sl = lds + rslot;
#pragma unroll
    for (int sf = 0; sf < 2; ++sf) { const u32x2 l2 = tr_rd(sl + hoff[sf][0]), h2 = tr_rd(sl + hoff[sf][1]); h[sf] = (u32x4){l2.x, l2.y, h2.x, h2.y}; }
#pragma unroll
    for (int j = 0; j < 7; ++j) wf[j] = *(LAS const u32x4*)(sl + woff + j * 1024);
    rslot = rslot == (G1_NS - 1) * G1_SLOT ? 0 : rslot + G1_SLOT;
  };
  auto compute = [&](const u32x4 (&h)[2], const u32x4 (&wf)[7]) {
#pragma unroll
    for (int j = 0; j < 7; ++j)
#pragma unroll
      for (int sf = 0; sf < 2; ++sf) acc[sf][j] = mfma16(wf[j], h[sf], acc[sf][j]);
    const u32x4 hs = wn ? h[1] : h[0];
    ssum = dot2(hs.x, 0x3f803f80u, dot2(hs.y, 0x3f803f80u, dot2(hs.z, 0x3f803f80u, dot2(hs.w, 0x3f803f80u, ssum))));
    ssq = dot2(hs.x, hs.x, dot2(hs.y, hs.y, dot2(hs.z, hs.z, dot2(hs.w, hs.w, ssq))));
  };
#define G1_STEP(PEND, ISSUE, READ, hc, wc, hn, wn_) do { \
    WAIT_VM(3 * (PEND)); WAIT_LGKM0(); BAR(); \
    if (ISSUE) issue(); \
    if (READ) readfr(hn, wn_); \
    compute(hc, wc); } while (0)
  WAIT_VM(4 * 3); BAR();
  STAMP(2, 1);
  readfr(hA, wA);
  for (int it = 0; it < 7; ++it) {
    G1_STEP(3, true, true, hA, wA, hB, wB);
    G1_STEP(3, true, true, hB, wB, hA, wA);
  }
  G1_STEP(3, true, true, hA, wA, hB, wB);
  G1_STEP(3, false, true, hB, wB, hA, wA);
  G1_STEP(2, false, true, hA, wA, hB, wB);
  G1_STEP(1, false, true, hB, wB, hA, wA);
  G1_STEP(0, false, true, hA, wA, hB, wB);
  compute(hB, wB);
  STAMP(2, 2);
  WAIT_LGKM0(); BAR();
  ssum += __shfl_xor(ssum, 16); ssum += __shfl_xor(ssum, 32);
  ssq += __shfl_xor(ssq, 16); ssq += __shfl_xor(ssq, 32);
  LAS f32x2* stl = (LAS f32x2*)lds;
  LAS f32x4* xch = (LAS f32x4*)(lds + 1024);
  {
    const float mean = ssum * (1.0f / D);
    const float var = fmaxf(ssq * (1.0f / D) - mean * mean, 0.f);
    const float sd = rbf(sqrtf(rbf(rbf(var) + 1e-5f)));
    if (lane < 16) stl[(2 * wsn + wn) * 16 + lane] = (f32x2){rbf(mean), 1.0f / sd};
    xch[w * 64 + lane] = wn ? acc[0][0] : acc[1][6];
  }
  WAIT_LGKM0(); BAR();
  const int fbk = first_block(ct), nblk = ct < 12 ? 7 : 6;
  const float* csb = (const float*)(ws + WS_CS) + (size_t)ct * 14 * 16 + 4 * g;
  char* hf_out = ws + WS_HF;
  const f32x4 other = xch[(w ^ 4) * 64 + lane];
#pragma unroll
  for (int sf = 0; sf < 2; ++sf) {
    const f32x2 st = stl[(2 * wsn + sf) * 16 + (lane & 15)];
    const size_t sfrag = (size_t)(rt * 8 + 2 * wsn + sf);
#pragma unroll
    for (int pb = 0; pb < 4; ++pb) {
      int bb; f32x4 ga, ua;
      if (pb < 3) { bb = wn ? 4 + pb : pb; ga = wn ? acc[sf][1 + 2 * pb] : acc[sf][2 * pb]; ua = wn ? acc[sf][2 + 2 * pb] : acc[sf][2 * pb + 1]; }
      else { if (sf != wn) continue; bb = 3; ga = wn ? other : acc[0][6]; ua = wn ? acc[1][0] : other; }
      if (bb >= nblk) continue;
      const f32x4 cg4 = *(const f32x4*)(csb + (2 * bb) * 16), cu4 = *(const f32x4*)(csb + (2 * bb + 1) * 16);
      float hv[4];
#pragma unroll
      for (int i = 0; i < 4; ++i) {
        const float gt = rbf((ga[i] - st[0] * cg4[i]) * st[1]), up = rbf((ua[i] - st[0] * cu4[i]) * st[1]);
        hv[i] = gt > 0.f ? gt * up : 0.f * gt * up;
      }
      const int blk = fbk + bb;
      *(u32x2*)(hf_out + (sfrag * NKS2 + (blk >> 1)) * 1024 + lane * 16 + (blk & 1) * 8) = (u32x2){pack2(hv[0], hv[1]), pack2(hv[2], hv[3])};
    }
  }
  STAMP(2, 3);
}

static constexpr int G2_STAGE = 15 * 1024, G2_NS = 4, G2_LDS = 2 * G2_NS * G2_STAGE;
__global__ __launch_bounds__(512, 2) void k3_gemm2(char* __restrict__ ws, u16* __restrict__ out) {
  extern __shared__ __attribute__((aligned(16))) char shm[];
  LAS char* lds = (LAS char*)shm;
  const int t = threadIdx.x, lane = t & 63, w = __builtin_amdgcn_readfirstlane(t >> 6);
  const int xg = blockIdx.x & 7, ix = blockIdx.x >> 3;
  STAMP(3, 0);
  const int rt = (xg & 3) * 8 + (ix & 7), dt = (xg >> 2) * 4 + (ix >> 3);
  const int sf = w & 3, kh = w >> 2;
  const int g = lane >> 4;
  const int srow = rt * 64 + sf * 16 + (lane & 15);
  u16 res[3][4];
#pragma unroll
  for (int j = 0; j < 3; ++j)
#pragma unroll
    for (int i = 0; i < 4; ++i) { const int df = kh ? (j < 2 ? 3 + j : 3) : j; res[j][i] = *(const u16*)(ws + WS_HT + ((size_t)(dt * 80 + df * 16 + 4 * g + i) * S + srow) * 2); }
  const char* wsrc = ws + WS_W2T + ((size_t)dt * NKS2 + kh * 27) * 5 * 1024 + sf * 3840 + lane * 16;
  const char* hsrc = ws + WS_HF + ((size_t)(rt * 4 + sf) * NKS2 + kh * 27) * 1024 + lane * 16;
  LAS char* ring = lds + kh * G2_NS * G2_STAGE;
  u32x4 hq[G2_NS][3];
  auto issue = [&](int st) {
    LAS char* sl = ring + (st % G2_NS) * G2_STAGE + sf * 3840;
#pragma unroll
    for (int i = 0; i < 3; ++i) asm volatile("global_load_dwordx4 %0, %1, off offset:%2" : "=v"(hq[st % G2_NS][i]) : "v"(hsrc), "n"(i * 1024) : "memory");
#pragma unroll
    for (int i = 0; i < 3; ++i) dma16(wsrc + i * 1024, sl + i * 1024);
    if (lane < 48) dma16(wsrc + 3072, sl + 3072);
    hsrc += 3 * 1024; wsrc += G2_STAGE;
  };
  issue(0); issue(1); issue(2);
  f32x4 acc[5];
#pragma unroll
  for (int j = 0; j < 5; ++j) acc[j] = (f32x4){0.f, 0.f, 0.f, 0.f};
#pragma unroll
  for (int st = 0; st < 9; ++st) {
    const int pend = (st + 2 <= 8) ? 2 : 8 - st;
    if (pend == 2) WAIT_VM(14); else if (pend == 1) WAIT_VM(7); else WAIT_VM(0);
    asm volatile("" : "+v"(hq[st % G2_NS][0]), "+v"(hq[st % G2_NS][1]), "+v"(hq[st % G2_NS][2])::"memory");
    WAIT_LGKM0();
    BAR();
    if (st == 0) STAMP(3, 1);
    if (st + 3 <= 8) issue(st + 3);
    LAS char* sl = ring + (st % G2_NS) * G2_STAGE + lane * 16;
#pragma unroll
    for (int i = 0; i < 3; ++i)
#pragma unroll
      for (int j = 0; j < 5; ++j) acc[j] = mfma16(*(LAS const u32x4*)(sl + (i * 5 + j) * 1024), hq[st % G2_NS][i], acc[j]);
  }
  STAMP(3, 2);
  WAIT_LGKM0();
  BAR();
  LAS f32x4* xch = (LAS f32x4*)lds;
#pragma unroll
  for (int j = 0; j < 3; ++j) { if (kh == 0) { if (j < 2) xch[(w * 3 + j) * 64 + lane] = acc[3 + j]; } else xch[(w * 3 + j) * 64 + lane] = acc[j]; }
  WAIT_LGKM0(); BAR();
#pragma unroll
  for (int j = 0; j < 3; ++j) {
    if (kh == 1 && j == 2) break;
    const int df = kh ? 3 + j : j;
    const f32x4 o = xch[((w ^ 4) * 3 + j) * 64 + lane];
    const f32x4 a = kh ? acc[3 + (j < 2 ? j : 0)] : acc[j];
    float r[4];
#pragma unroll
    for (int i = 0; i < 4; ++i) r[i] = bflo((u32)res[j][i]) + rbf(a[i] + o[i]);
    *(u32x2*)(out + (size_t)srow * D + dt * 80 + df * 16 + 4 * g) = (u32x2){pack2(r[0], r[1]), pack2(r[2], r[3])};
  }
  STAMP(3, 3);
}

extern "C" void kernel_launch(void* const* d_in, const int* in_sizes, int n_in, void* d_out, int out_size, void* d_ws, size_t ws_size, hipStream_t stream) {
  const u16* q = (const u16*)d_in[0]; const u16* k = (const u16*)d_in[1]; const u16* v = (const u16*)d_in[2];
  const u16* shk = (const u16*)d_in[3]; const u16* fk = (const u16*)d_in[4]; const u16* sD = (const u16*)d_in[5];
  const u16* wg = (const u16*)d_in[7]; const u16* wu = (const u16*)d_in[8]; const u16* wd = (const u16*)d_in[9];
  char* ws = (char*)d_ws;
  (void)hipFuncSetAttribute((const void*)k1_conv, hipFuncAttributeMaxDynamicSharedMemorySize, K1_LDS);
  (void)hipFuncSetAttribute((const void*)k2_gemm1, hipFuncAttributeMaxDynamicSharedMemorySize, G1_LDS);
  (void)hipFuncSetAttribute((const void*)k3_gemm2, hipFuncAttributeMaxDynamicSharedMemorySize, G2_LDS);
  k0_prep<<<K0_TILES + K0_STATS, 256, 0, stream>>>(q, k, v, shk, ws);
  k1_conv<<<K1_CONV + K1_W1 + K1_W2, 512, K1_LDS, stream>>>(fk, sD, wg, wu, wd, ws);
  k2_gemm1<<<256, 512, G1_LDS, stream>>>(ws);
  k3_gemm2<<<256, 512, G2_LDS, stream>>>(ws, (u16*)d_out);
}
```

```cpp
#include <hip/hip_runtime.h>
#include <hip/hip_bf16.h>

typedef unsigned short u16;
typedef unsigned int u32;
typedef __attribute__((ext_vector_type(2))) unsigned int u32x2;
typedef __attribute__((ext_vector_type(4))) unsigned int u32x4;
typedef __attribute__((ext_vector_type(8))) short bf16x8;
typedef __attribute__((ext_vector_type(2))) __bf16 bf16x2;
typedef __attribute__((ext_vector_type(2))) float f32x2;
typedef __attribute__((ext_vector_type(4))) float f32x4;
typedef __attribute__((ext_vector_type(4))) unsigned short u16x4;
#define LAS __attribute__((address_space(3)))

static constexpr int S = 2048, D = 640, F = 1728;
static constexpr int NKS1 = D / 32;
static constexpr int NKS2 = F / 32;

static constexpr size_t WS_UR1 = 0;
static constexpr size_t WS_HB  = WS_UR1 + (size_t)D * S * 2;
static constexpr size_t WS_ST1 = WS_HB  + (size_t)D * S * 2;
static constexpr size_t WS_ST2 = WS_ST1 + (size_t)S * 8;
static constexpr size_t WS_W1T = WS_ST2 + (size_t)16 * 1024;
static constexpr int NKS2P = 56;
static constexpr size_t WS_W2T = WS_W1T + (size_t)14 * 8 * NKS1 * 2048;
static constexpr size_t WS_HF  = WS_W2T + (size_t)8 * NKS2P * 5 * 1024;
static constexpr size_t WS_END = WS_HF  + (size_t)128 * NKS2 * 1024;

__device__ __forceinline__ float bflo(u32 w) { return __uint_as_float(w << 16); }
__device__ __forceinline__ float bfhi(u32 w) { return __uint_as_float(w & 0xffff0000u); }
__device__ __forceinline__ u32 pack2(float lo, float hi) { f32x2 v = {lo, hi}; bf16x2 b = __builtin_convertvector(v, bf16x2); return __builtin_bit_cast(u32, b); }
__device__ __forceinline__ float rbf(float x) { return bflo(pack2(x, 0.f)); }
__device__ __forceinline__ float dot2(u32 a, u32 b, float c) { return __builtin_amdgcn_fdot2_f32_bf16(__builtin_bit_cast(bf16x2, a), __builtin_bit_cast(bf16x2, b), c, false); }
__device__ __forceinline__ f32x4 mfma16(u32x4 a, u32x4 b, f32x4 c) { return __builtin_amdgcn_mfma_f32_16x16x32_bf16(__builtin_bit_cast(bf16x8, a), __builtin_bit_cast(bf16x8, b), c, 0, 0, 0); }
__device__ __forceinline__ void dma16(const void* g, LAS void* l) { __builtin_amdgcn_global_load_lds((const u32*)g, (LAS u32*)l, 16, 0, 0); }
#define WAIT_VM(n) asm volatile("s_waitcnt vmcnt(%0)" ::"n"(n) : "memory")
#define WAIT_LGKM0() asm volatile("s_waitcnt lgkmcnt(0)" ::: "memory")
#define BAR() do { asm volatile("" ::: "memory"); __builtin_amdgcn_s_barrier(); asm volatile("" ::: "memory"); } while (0)
#define SB() __builtin_amdgcn_sched_barrier(0)
#define MEMFENCE() asm volatile("" ::: "memory")
#define MAKE_RSRC(p) __builtin_amdgcn_make_buffer_rsrc((void*)(p), 0, 0x7fffffff, 0x00020000)
#define ST16(rs, off, v) __builtin_amdgcn_raw_buffer_store_b128((v), (rs), (int)(off), 0, 16)

template <int CTRL, int RM> __device__ __forceinline__ float dpp_add(float v) { return v + __builtin_bit_cast(float, __builtin_amdgcn_update_dpp(0, __builtin_bit_cast(int, v), CTRL, RM, 0xf, true)); }
__device__ __forceinline__ float wave_sum(float v) {
  v = dpp_add<0x111, 0xf>(v); v = dpp_add<0x112, 0xf>(v); v = dpp_add<0x114, 0xf>(v); v = dpp_add<0x118, 0xf>(v);
  v = dpp_add<0x142, 0xa>(v); v = dpp_add<0x143, 0xc>(v);
  return __builtin_bit_cast(float, __builtin_amdgcn_readlane(__builtin_bit_cast(int, v), 63));
}

#define STAMP(kid, i) do {} while (0)
#define STAMP_DECL do {} while (0)
#define STAMP_FLUSH(kid) do {} while (0)

typedef __attribute__((ext_vector_type(4))) short s16x4;
__device__ __forceinline__ u16x4 tr_read(u32 addr) { u16x4 r; asm volatile("ds_read_b64_tr_b16 %0, %1" : "=v"(r) : "v"(addr) : "memory"); return r; }
#define DS_TR(dst, addr, off) asm volatile("ds_read_b64_tr_b16 %0, %1 offset:%2" : "=v"(dst) : "v"(addr), "n"(off))
#define DS_B64(dst, addr, off) asm volatile("ds_read_b64 %0, %1 offset:%2" : "=v"(dst) : "v"(addr), "n"(off))
#define DS_B128(dst, addr, off) asm volatile("ds_read_b128 %0, %1 offset:%2" : "=v"(dst) : "v"(addr), "n"(off))
#define MFMA_IP(acc, a, b) asm volatile("v_mfma_f32_16x16x32_bf16 %0, %1, %2, %0" : "+v"(acc) : "v"(a), "v"(b))
#define MFMA_Z(acc, a, b) asm volatile("v_mfma_f32_16x16x32_bf16 %0, %1, %2, 0" : "=&v"(acc) : "v"(a), "v"(b))
#define GLD16(dst, voff, sbase, off) asm volatile("global_load_dwordx4 %0, %1, %2 offset:%3" : "=v"(dst) : "v"(voff), "s"(sbase), "n"(off) : "memory")
#define WAIT_LGKM(n) asm volatile("s_waitcnt lgkmcnt(%0)" ::"n"(n) : "memory")

static constexpr int K0_TILES = 32 * 10, K0_STATS = 128;
static constexpr int K0_RAW = 17 * 1024, K0_LDS = K0_RAW + 64 * 144;
__global__ __launch_bounds__(256) void k0_prep(const u16* __restrict__ x, const u16* __restrict__ k, const u16* __restrict__ v,
                                               const u16* __restrict__ shk, char* __restrict__ ws) {
  extern __shared__ __attribute__((aligned(16))) char shm[];
  LAS char* lds = (LAS char*)shm;
  const int t = threadIdx.x, lane = t & 63, w = __builtin_amdgcn_readfirstlane(t >> 6), b = blockIdx.x;
  STAMP_DECL; STAMP(0, 0);
  const auto rs = MAKE_RSRC(ws);
  if (b < K0_TILES) {
    const int st = b / 10, ct = b % 10, s0 = st * 64, c0 = ct * 64;
#pragma unroll
    for (int i = 0; i < 5; ++i) {
      const int pc = w + 4 * i;
      if (pc < 17) {
        const u16* src = pc < 9 ? k : v;
        int row = (pc < 9 ? s0 - 8 + 8 * pc : s0 + 8 * (pc - 9)) + (lane >> 3);
        row = row < 0 ? 0 : row;
        dma16(src + (size_t)row * D + c0 + (lane & 7) * 8, lds + pc * 1024);
      }
    }
    const int cp = t & 31, rg = t >> 5, ch = c0 + 2 * cp;
    const u32x4 sh = *(const u32x4*)(shk + ch * 4);
    WAIT_VM(0); __syncthreads();
    STAMP(0, 1);
    u32 kk[11], vv[8];
    LAS const char* rk = lds + (8 * rg + 5) * 128 + cp * 4;
#pragma unroll
    for (int i = 0; i < 11; ++i) kk[i] = *(LAS const u32*)(rk + i * 128);
#pragma unroll
    for (int i = 0; i < 8; ++i) vv[i] = *(LAS const u32*)(lds + 9 * 1024 + (8 * rg + i) * 128 + cp * 4);
    if (st == 0 && rg == 0) { kk[0] = kk[1] = kk[2] = 0u; }
    const float a0 = bflo(sh.x), a1 = bfhi(sh.x), a2 = bflo(sh.y), a3 = bfhi(sh.y);
    const float b0 = bflo(sh.z), b1 = bfhi(sh.z), b2 = bflo(sh.w), b3 = bfhi(sh.w);
    float ka[11], kb[11];
#pragma unroll
    for (int i = 0; i < 11; ++i) { ka[i] = bflo(kk[i]); kb[i] = bfhi(kk[i]); }
    float ua[8], ub[8];
#pragma unroll
    for (int i = 0; i < 8; ++i) {
      ua[i] = (ka[i] * a3 + ka[i + 1] * a2 + ka[i + 2] * a1 + ka[i + 3] * a0) * bflo(vv[i]);
      ub[i] = (kb[i] * b3 + kb[i + 1] * b2 + kb[i + 2] * b1 + kb[i + 3] * b0) * bfhi(vv[i]);
    }
    LAS char* L1 = lds + K0_RAW;
    const int o = (7 - rg) * 16;
    *(LAS u32x4*)(L1 + (2 * cp) * 144 + o) = (u32x4){pack2(ua[7], ua[6]), pack2(ua[5], ua[4]), pack2(ua[3], ua[2]), pack2(ua[1], ua[0])};
    *(LAS u32x4*)(L1 + (2 * cp + 1) * 144 + o) = (u32x4){pack2(ub[7], ub[6]), pack2(ub[5], ub[4]), pack2(ub[3], ub[2]), pack2(ub[1], ub[0])};
    __syncthreads();
    u32x4 o1[2];
#pragma unroll
    for (int i = 0; i < 2; ++i) o1[i] = *(LAS const u32x4*)(L1 + ((t >> 3) + 32 * i) * 144 + (t & 7) * 16);
#pragma unroll
    for (int i = 0; i < 2; ++i) ST16(rs, WS_UR1 + ((size_t)(c0 + (t >> 3) + 32 * i) * S + (1984 - s0)) * 2 + (t & 7) * 16, o1[i]);
    STAMP(0, 2);
  } else {
    const int rbase = (b - K0_TILES) * 16 + w * 4;
    u32x4 xa[4], xb[4];
#pragma unroll
    for (int r = 0; r < 4; ++r) {
      xa[r] = *(const u32x4*)(x + (size_t)(rbase + r) * D + lane * 8);
      xb[r] = *(const u32x4*)(x + (size_t)(rbase + r) * D + 512 + (lane & 15) * 8);
    }
    const float mk = lane < 16 ? 1.f : 0.f;
    float mean[4], q[4];
#pragma unroll
    for (int r = 0; r < 4; ++r) {
      float s = 0.f, s2 = 0.f;
#pragma unroll
      for (int j = 0; j < 4; ++j) { s += bflo(xa[r][j]) + bfhi(xa[r][j]); s2 += bflo(xb[r][j]) + bfhi(xb[r][j]); }
      mean[r] = wave_sum(s + mk * s2) * (1.0f / D);
    }
#pragma unroll
    for (int r = 0; r < 4; ++r) {
      float s = 0.f, s2 = 0.f;
#pragma unroll
      for (int j = 0; j < 4; ++j) {
        float a = bflo(xa[r][j]) - mean[r], c = bfhi(xa[r][j]) - mean[r]; s += a * a + c * c;
        a = bflo(xb[r][j]) - mean[r]; c = bfhi(xb[r][j]) - mean[r]; s2 += a * a + c * c;
      }
      q[r] = wave_sum(s + mk * s2) * (1.0f / D);
    }
    STAMP(0, 1);
    if (lane < 2) {
      const float m0 = lane ? mean[2] : mean[0], m1 = lane ? mean[3] : mean[1], v0 = lane ? q[2] : q[0], v1 = lane ? q[3] : q[1];
      const float sd0 = rbf(sqrtf(rbf(rbf(v0) + 1e-5f))), sd1 = rbf(sqrtf(rbf(rbf(v1) + 1e-5f)));
      const f32x4 o = {rbf(m0), 1.0f / sd0, rbf(m1), 1.0f / sd1};
      ST16(rs, WS_ST1 + (size_t)(rbase + 2 * lane) * 8, __builtin_bit_cast(u32x4, o));
    }
    STAMP(0, 2);
  }
  STAMP_FLUSH(0);
}

static constexpr int CH_BYTES = 13568, CH_F = 0, CH_U1 = 4224, CH_U0 = 4224 + 4608 + 32;
static constexpr int K1_XCH = 4 * CH_BYTES;
static constexpr int K1_YL = K1_XCH + 8 * 4096;
static constexpr int K1_YP = 8192 + 64;
static constexpr int K1_LDS = K1_YL + 4 * K1_YP;
#define K1_DELAY 80
static constexpr int K1_CONV = 160, K1_W1 = 54, K1_W2 = 30;

template <bool W1>
__device__ __forceinline__ void retile_job(LAS char* lds, const u16* __restrict__ src, int pitch, int cg, int mat, int ks0, char* ws, int w, int lane) {
  constexpr int NU = W1 ? 10 : 9;
  const auto rs = MAKE_RSRC(ws);
#pragma unroll
  for (int i = 0; i < NU; ++i) {
    const int pc = w + 8 * i, row = 8 * pc + (lane >> 3);
    dma16(src + (size_t)(ks0 * 32 + row) * pitch + (((lane & 7) ^ (row & 7)) * 8), lds + pc * 1024);
  }
  WAIT_VM(0); BAR();
  const int g = lane >> 4, q = (lane & 15) >> 2, p = lane & 3, fb = w & 3;
  const u32 l0 = (u32)(size_t)lds;
  u16x4 lo[NU], hi[NU];
#pragma unroll
  for (int i = 0; i < NU; ++i) {
    const int ks = (w >> 2) + 2 * i, row0 = 32 * ks + 8 * g + q, row1 = row0 + 4, chk = 2 * fb + (p >> 1);
    lo[i] = tr_read(l0 + row0 * 128 + ((chk ^ (row0 & 7)) * 16) + 8 * (p & 1));
    hi[i] = tr_read(l0 + row1 * 128 + ((chk ^ (row1 & 7)) * 16) + 8 * (p & 1));
  }
#pragma unroll
  for (int i = 0; i < NU; ++i) asm volatile("s_waitcnt lgkmcnt(0)" : "+v"(lo[i]), "+v"(hi[i])::"memory");
  float m = 0.f;
  if (W1) {
    float cs = 0.f;
#pragma unroll
    for (int i = 0; i < NU; ++i) {
      const u32x2 l2 = __builtin_bit_cast(u32x2, lo[i]), h2 = __builtin_bit_cast(u32x2, hi[i]);
      cs = dot2(l2.x, 0x3f803f80u, dot2(l2.y, 0x3f803f80u, dot2(h2.x, 0x3f803f80u, dot2(h2.y, 0x3f803f80u, cs))));
    }
    cs += __shfl_xor(cs, 16); cs += __shfl_xor(cs, 32);
    LAS float* red = (LAS float*)(lds + 80 * 1024);
    if (lane < 16) red[w * 16 + lane] = cs;
    WAIT_LGKM0(); BAR();
    m = (red[fb * 16 + (lane & 15)] + red[(fb + 4) * 16 + (lane & 15)]) * (1.0f / D);
  }
#pragma unroll
  for (int i = 0; i < NU; ++i) {
    const int ks = (w >> 2) + 2 * i;
    const u32x2 l2 = __builtin_bit_cast(u32x2, lo[i]), h2 = __builtin_bit_cast(u32x2, hi[i]);
    if (W1) {
      const u32x4 o = {pack2(bflo(l2.x) - m, bfhi(l2.x) - m), pack2(bflo(l2.y) - m, bfhi(l2.y) - m), pack2(bflo(h2.x) - m, bfhi(h2.x) - m), pack2(bflo(h2.y) - m, bfhi(h2.y) - m)};
      const int blk = cg * 4 + fb;
      ST16(rs, WS_W1T + ((size_t)(blk * NKS1 + ks) * 2 + mat) * 1024 + lane * 16, o);
    } else {
      const int df = cg * 4 + fb, dt = df / 5, j = df % 5;
      ST16(rs, WS_W2T + ((size_t)(dt * NKS2P + ks0 + ks) * 5 + j) * 1024 + lane * 16, ((u32x4){l2.x, l2.y, h2.x, h2.y}));
    }
  }
  if (!W1 && ks0 == 36 && cg < 8) {
#pragma unroll
    for (int i = 0; i < 2; ++i) { const int pc = w + 8 * i; if (pc < 10) ST16(rs, WS_W2T + ((size_t)(cg * NKS2P + NKS2) * 5 + pc) * 1024 + lane * 16, ((u32x4){0u, 0u, 0u, 0u})); }
  }
  if (W1 && cg == 26 && mat == 0) {
#pragma unroll
    for (int i = 0; i < 3; ++i) { const int ks = w + 8 * i; if (ks < NKS1) ST16(rs, WS_W1T + ((size_t)(108 * NKS1 + ks) * 2) * 1024 + lane * 16, ((u32x4){0x3f803f80u, 0x3f803f80u, 0x3f803f80u, 0x3f803f80u})); }
  }
}

__global__ __launch_bounds__(512, 2) void k1_conv(const u16* __restrict__ x, const u16* __restrict__ fk, const u16* __restrict__ sD, const u16* __restrict__ wg,
                                                  const u16* __restrict__ wu, const u16* __restrict__ wd, char* __restrict__ ws) {
  extern __shared__ __attribute__((aligned(16))) char shm[];
  LAS char* lds = (LAS char*)shm;
  const int t = threadIdx.x, lane = t & 63, w = __builtin_amdgcn_readfirstlane(t >> 6), b = blockIdx.x;
  STAMP_DECL; STAMP(1, 0);
  if (b < K1_CONV) {
    const auto rs = MAKE_RSRC(ws);
    const int cgp = (b & 7) * 20 + (b >> 3);
    const int c = w & 3, hf = w >> 2, d = cgp * 4 + c;
    LAS char* cb = lds + c * CH_BYTES;
    {
      const char* gF = (const char*)fk + (size_t)d * S * 2 + lane * 16;
      const char* g1 = ws + WS_UR1 + (size_t)d * S * 2 + lane * 16;
#pragma unroll
      for (int i = 0; i < 2; ++i) {
        const int pc = hf * 2 + i;
        dma16(g1 + pc * 1024, cb + CH_U1 + pc * 1024);
        dma16(gF + pc * 1024, cb + CH_F + 64 + pc * 1024);
      }
      if (hf == 0) { if (lane < 32) *(LAS u32x4*)(cb + CH_U1 + 4096 + lane * 16) = (u32x4){0, 0, 0, 0}; if (lane < 4) *(LAS u32x4*)(cb + CH_F + lane * 16) = (u32x4){0, 0, 0, 0}; }
      else { if (lane < 32) *(LAS u32x4*)(cb + CH_U0 + 4096 + lane * 16) = (u32x4){0, 0, 0, 0}; }
    }
    const u32 dw = sD[d];
    WAIT_VM(0); WAIT_LGKM0(); BAR();
#pragma unroll
    for (int i = 0; i < 2; ++i) {
      const int p0 = 1024 * hf + 512 * i + 8 * lane;
      const u32x4 in = *(LAS const u32x4*)(cb + CH_U1 + p0 * 2); const u32 nx = *(LAS const u32*)(cb + CH_U1 + p0 * 2 + 16);
      *(LAS u32x4*)(cb + CH_U0 + p0 * 2) = (u32x4){__builtin_amdgcn_alignbit(in.y, in.x, 16), __builtin_amdgcn_alignbit(in.z, in.y, 16), __builtin_amdgcn_alignbit(in.w, in.z, 16), __builtin_amdgcn_alignbit(nx, in.w, 16)};
    }
    WAIT_LGKM0(); BAR();
    STAMP(1, 1);
    const int r = lane & 15, g = lane >> 4;
    f32x4 acc[8];
    const int a = r >> 1, e = r & 1;
    const u32 ub = (u32)(size_t)(cb + (e ? CH_U1 : CH_U0) + 448 - 64 * a + 16 * g + 256 * hf);
    u32 fbp[4];
#pragma unroll
    for (int i = 0; i < 4; ++i) fbp[i] = (u32)(size_t)(cb + CH_F + 16 * g + 4 * r + 4 + 256 * hf) + 1024 * i;
    u32x2 xr[4]; u32x4 stv[2];
    const char* gx = (const char*)x + (size_t)(2 * t) * D * 2 + cgp * 8;
#define C_LDX(i) do { \
      if ((i) == 0) asm volatile("global_load_dwordx2 %0, %1, off" : "=v"(xr[0]) : "v"(gx) : "memory"); \
      if ((i) == 1) asm volatile("global_load_dwordx2 %0, %1, off offset:1280" : "=v"(xr[1]) : "v"(gx) : "memory"); \
      if ((i) == 2) asm volatile("global_load_dwordx2 %0, %1, off" : "=v"(xr[2]) : "v"(gx + (size_t)1024 * D * 2) : "memory"); \
      if ((i) == 3) asm volatile("global_load_dwordx2 %0, %1, off offset:1280" : "=v"(xr[3]) : "v"(gx + (size_t)1024 * D * 2) : "memory"); \
      if ((i) == 4) GLD16(stv[0], t * 16, ws + WS_ST1, 0); \
      if ((i) == 5) GLD16(stv[1], t * 16, ws + WS_ST1 + 8192, 0); } while (0)
    u32x4 U[2][8]; u32x2 Fr[2][8][2];
#define C_RDU(buf, i, cc) DS_B128(U[buf][i], ub, 512 * (7 - (i)) + 64 * (cc))
#define C_RDF(buf, i, cc, hh) asm volatile("ds_read2_b32 %0, %1 offset0:%2 offset1:%3" : "=v"(Fr[buf][i][hh]) : "v"(fbp[(i) >> 1]), "n"(128 * ((i) & 1) + 16 * (cc) + 2 * (hh)), "n"(128 * ((i) & 1) + 16 * (cc) + 2 * (hh) + 1))
#define C_F(buf, i) ((u32x4){Fr[buf][i][0].x, Fr[buf][i][0].y, Fr[buf][i][1].x, Fr[buf][i][1].y})
#define C_WAIT(buf) do { \
      asm volatile("s_waitcnt lgkmcnt(0)" : "+v"(U[buf][0]), "+v"(U[buf][1]), "+v"(U[buf][2]), "+v"(U[buf][3]), "+v"(U[buf][4]), "+v"(U[buf][5]), "+v"(U[buf][6]), "+v"(U[buf][7])::"memory"); \
      asm volatile("" : "+v"(Fr[buf][0][0]), "+v"(Fr[buf][0][1]), "+v"(Fr[buf][1][0]), "+v"(Fr[buf][1][1]), "+v"(Fr[buf][2][0]), "+v"(Fr[buf][2][1]), "+v"(Fr[buf][3][0]), "+v"(Fr[buf][3][1]), \
                        "+v"(Fr[buf][4][0]), "+v"(Fr[buf][4][1]), "+v"(Fr[buf][5][0]), "+v"(Fr[buf][5][1]), "+v"(Fr[buf][6][0]), "+v"(Fr[buf][6][1]), "+v"(Fr[buf][7][0]), "+v"(Fr[buf][7][1])); } while (0)
#pragma unroll
    for (int i = 0; i < 8; ++i) { C_RDF(0, i, 0, 0); C_RDF(0, i, 0, 1); C_RDU(0, i, 0); }
    C_WAIT(0);
#pragma unroll
    for (int cc = 0; cc < 4; ++cc) {
      const int cur = cc & 1, nxt = cur ^ 1;
      int n = 0;
#pragma unroll
      for (int c1 = 0; c1 < 8; ++c1)
#pragma unroll
        for (int i = c1; i < 8; ++i) {
          if (cc == 0 && c1 == 0) MFMA_Z(acc[i], U[cur][i - c1], C_F(cur, c1)); else MFMA_IP(acc[i], U[cur][i - c1], C_F(cur, c1));
          if (cc < 3 && n < 24) { const int j = n / 3, kk = n % 3; if (kk == 0) C_RDU(nxt, j, cc + 1); else C_RDF(nxt, j, cc + 1, kk - 1); }
          if (cc < 3 && n == 26) C_LDX(2 * cc);
          if (cc < 3 && n == 32) C_LDX(2 * cc + 1);
          ++n;
        }
      if (cc < 3) C_WAIT(nxt);
    }
    asm volatile("s_nop 15\n\ts_nop 15" ::: "memory");
    STAMP(1, 2);
    LAS char* xo = lds + K1_XCH + w * 4096 + lane * 16;
    LAS char* xi = lds + K1_XCH + (w ^ 4) * 4096 + lane * 16;
#pragma unroll
    for (int j = 0; j < 4; ++j) *(LAS f32x4*)(xo + j * 1024) = hf ? acc[j] : acc[4 + j];
    WAIT_LGKM0(); BAR();
    STAMP(1, 3);
    LAS char* yl = lds + K1_YL + c * K1_YP;
    const float Dd = bflo(dw);
#pragma unroll
    for (int j = 0; j < 4; ++j) {
      f32x4 y = *(LAS const f32x4*)(xi + j * 1024);
      y += hf ? acc[4 + j] : acc[j];
#pragma unroll
      for (int hh = 0; hh < 2; ++hh) {
        const int s = 256 * (4 * hf + j) + 32 * (2 * g + hh) + 2 * r;
        const u32 uw = *(LAS const u32*)(cb + CH_U1 + (2046 - s) * 2);
        *(LAS f32x2*)(yl + s * 4) = (f32x2){y[2 * hh] + bfhi(uw) * Dd, y[2 * hh + 1] + bflo(uw) * Dd};
      }
    }
    asm volatile("s_waitcnt vmcnt(0) lgkmcnt(0)" : "+v"(xr[0]), "+v"(xr[1]), "+v"(xr[2]), "+v"(xr[3]), "+v"(stv[0]), "+v"(stv[1])::"memory");
    BAR();
    STAMP(1, 4);
#pragma unroll
    for (int i = 0; i < 2; ++i) {
      const int s = 2 * t + 1024 * i;
      f32x2 y2[4];
#pragma unroll
      for (int cc = 0; cc < 4; ++cc) y2[cc] = *(LAS const f32x2*)(lds + K1_YL + cc * K1_YP + s * 4);
      const f32x4 st = __builtin_bit_cast(f32x4, stv[i]);
      const u32x2 x0 = xr[2 * i], x1 = xr[2 * i + 1];
      const float xa[4] = {bflo(x0.x), bfhi(x0.x), bflo(x0.y), bfhi(x0.y)}, xb[4] = {bflo(x1.x), bfhi(x1.x), bflo(x1.y), bfhi(x1.y)};
      float ha[4], hb[4];
#pragma unroll
      for (int cc = 0; cc < 4; ++cc) { ha[cc] = xa[cc] + (xa[cc] - st[0]) * st[1] * y2[cc][0]; hb[cc] = xb[cc] + (xb[cc] - st[2]) * st[3] * y2[cc][1]; }
      ST16(rs, WS_HB + ((size_t)cgp * S + s) * 8, ((u32x4){pack2(ha[0], ha[1]), pack2(ha[2], ha[3]), pack2(hb[0], hb[1]), pack2(hb[2], hb[3])}));
    }
    STAMP(1, 5);
  } else {
    { const unsigned long long t0 = __builtin_amdgcn_s_memrealtime(); while (__builtin_amdgcn_s_memrealtime() - t0 < K1_DELAY) __builtin_amdgcn_s_sleep(4); }
    const int jb = b - K1_CONV;
    if (jb < K1_W1) retile_job<true>(lds, ((jb & 1) ? wu : wg) + (jb >> 1) * 64, F, jb >> 1, jb & 1, 0, ws, w, lane);
    else if (jb < K1_W1 + K1_W2) { const int j2 = jb - K1_W1; retile_job<false>(lds, wd + (j2 / 3) * 64, D, j2 / 3, 0, (j2 % 3) * 18, ws, w, lane); }
    STAMP(1, 5);
  }
  STAMP_FLUSH(1);
}

#define G1BAR() BAR()
static constexpr int G1_NS = 16, G1_SLOT = 8192, G1_LDS = G1_NS * G1_SLOT, G1_WR = 4;
__global__ __launch_bounds__(512, 2) void k2_gemm1(char* __restrict__ ws) {
  extern __shared__ __attribute__((aligned(16))) char shm[];
  LAS char* lds = (LAS char*)shm;
  const u32 lds0 = (u32)(size_t)lds;
  const int t = threadIdx.x, lane = t & 63, w = __builtin_amdgcn_readfirstlane(t >> 6);
  STAMP_DECL; STAMP(2, 0);
  const int xg = blockIdx.x & 7, ix = blockIdx.x >> 3;
  const int rt = (xg & 3) * 4 + (ix & 3), ct = (xg >> 2) * 7 + (ix >> 2);
  const int s0 = rt * 128;
  const u32 voff = lane * 16;
  const auto rs = MAKE_RSRC(ws);
  if (w >= 4) {
    const int j = w - 4;
    const u32 hv = voff ^ ((j & 1) * 128);
    const char* hsb = ws + WS_HB + ((size_t)(2 * j) * S + s0) * 8;
    const u32 ldsw = lds0 + 2 * j * 1024;
#define G1_SLAB(ks) do { \
      asm volatile("s_mov_b32 m0, %0\n\ts_nop 0\n\tglobal_load_lds_dwordx4 %1, %2" :: "s"(ldsw + ((ks) % G1_NS) * G1_SLOT), "v"(hv), "s"(hsb + (size_t)(ks) * 8 * S * 8) : "memory"); \
      asm volatile("s_mov_b32 m0, %0\n\ts_nop 0\n\tglobal_load_lds_dwordx4 %1, %2" :: "s"(ldsw + ((ks) % G1_NS) * G1_SLOT + 1024), "v"(hv), "s"(hsb + (size_t)(ks) * 8 * S * 8 + S * 8) : "memory"); } while (0)
    G1_SLAB(0); G1_SLAB(1); G1_SLAB(2); G1_SLAB(3); G1_SLAB(4);
    WAIT_VM(8); BAR();
    G1_SLAB(5); G1_SLAB(6); G1_SLAB(7); G1_SLAB(8);
    WAIT_VM(8); BAR();
    G1_SLAB(9); G1_SLAB(10); G1_SLAB(11); G1_SLAB(12);
    WAIT_VM(8); BAR();
    G1_SLAB(13); G1_SLAB(14); G1_SLAB(15);
    WAIT_VM(6); BAR();
    G1_SLAB(16); G1_SLAB(17); G1_SLAB(18); G1_SLAB(19);
    WAIT_VM(6); BAR();
    WAIT_VM(0); BAR();
    if (ct == 13) BAR();
  } else {
    const int blk0 = ct * 8 + 2 * w;
    const char* wsA = ws + WS_W1T + (size_t)(blk0 > 108 ? 108 : blk0) * NKS1 * 2048;
    const char* wsB = ws + WS_W1T + (size_t)(blk0 + 1 > 108 ? 108 : blk0 + 1) * NKS1 * 2048;
    const int g = lane >> 4;
    const u32 hbase = lds0 + g * 2048 + (lane & 15) * 8;
    const u32 hoffE = hbase + (g & 1) * 128, hoffO = hbase + (1 - (g & 1)) * 128 - 128;
    const u32 hoffE2 = hoffE + 65536, hoffO2 = hoffO + 65536;
    f32x4 acc[8][4];
    const int stat = __builtin_amdgcn_readfirstlane(blk0 == 108 ? 1 : 0);
#define G1_LDW(ks, i) do { if ((i) == 0) GLD16(wr[(ks) % G1_WR][0], voff, wsA + (ks) * 2048, 0); if ((i) == 1) GLD16(wr[(ks) % G1_WR][1], voff, wsA + (ks) * 2048, 1024); \
      if ((i) == 2) GLD16(wr[(ks) % G1_WR][2], voff, wsB + (ks) * 2048, 0); if ((i) == 3) GLD16(wr[(ks) % G1_WR][3], voff, wsB + (ks) * 2048, 1024); } while (0)
#define G1_RD(sf, hh, ks) DS_B64(hf[sf][hh], ((ks) % G1_NS) >= 8 ? (((sf) & 1) ? hoffO2 : hoffE2) : (((sf) & 1) ? hoffO : hoffE), (((ks) % G1_NS) & 7) * G1_SLOT + (hh) * 1024 + (sf) * 128)
#define G1_H(sf) ((u32x4){hf[sf][0].x, hf[sf][0].y, hf[sf][1].x, hf[sf][1].y})
#define G1_MM(ks, sf, i, STAT) do { \
      if (STAT) { if ((i) == 0) { if ((ks) == 0) MFMA_Z(acc[sf][0], wr[0][0], G1_H(sf)); else MFMA_IP(acc[sf][0], wr[(ks) % G1_WR][0], G1_H(sf)); } \
                  if ((i) == 2) { if ((ks) == 0) MFMA_Z(acc[sf][2], G1_H(sf), G1_H(sf)); else MFMA_IP(acc[sf][2], G1_H(sf), G1_H(sf)); } } \
      else { if ((ks) == 0) MFMA_Z(acc[sf][i], wr[0][i], G1_H(sf)); else MFMA_IP(acc[sf][i], wr[(ks) % G1_WR][i], G1_H(sf)); } } while (0)
#define G1_BODY(STAT) { \
      u32x4 wr[G1_WR][4]; u32x2 hf[8][2]; \
      _Pragma("unroll") for (int ks = 0; ks < 3; ++ks) { G1_LDW(ks, 0); G1_LDW(ks, 1); G1_LDW(ks, 2); G1_LDW(ks, 3); } \
      BAR();                                                   \
      STAMP(2, 1); \
      _Pragma("unroll") for (int sf = 0; sf < 7; ++sf) { G1_RD(sf, 0, 0); G1_RD(sf, 1, 0); } \
      _Pragma("unroll") for (int ks = 0; ks < NKS1; ++ks) { \
        if ((ks & 3) == 0) G1BAR(); \
          \
        if (ks + 2 < NKS1) WAIT_VM(8); else if (ks + 1 < NKS1) WAIT_VM(4); else WAIT_VM(0); \
        asm volatile("" : "+v"(wr[ks % G1_WR][0]), "+v"(wr[ks % G1_WR][1]), "+v"(wr[ks % G1_WR][2]), "+v"(wr[ks % G1_WR][3])); \
        _Pragma("unroll") for (int sf = 0; sf < 8; ++sf) { \
            \
          if (ks + 1 < NKS1 || sf == 0) asm volatile("s_waitcnt lgkmcnt(12)" : "+v"(hf[sf][0]), "+v"(hf[sf][1])::"memory"); \
          else if (sf < 7) asm volatile("s_waitcnt lgkmcnt(%2)" : "+v"(hf[sf][0]), "+v"(hf[sf][1]) : "n"(14 - 2 * sf) : "memory"); \
          else asm volatile("s_waitcnt lgkmcnt(0)" : "+v"(hf[sf][0]), "+v"(hf[sf][1])::"memory"); \
          G1_MM(ks, sf, 0, STAT); \
          if (sf == 0) G1_RD(7, 0, ks); else if (ks + 1 < NKS1) G1_RD(sf - 1, 0, ks + 1); \
          G1_MM(ks, sf, 1, STAT); \
          if (sf == 0) G1_RD(7, 1, ks); else if (ks + 1 < NKS1) G1_RD(sf - 1, 1, ks + 1); \
          G1_MM(ks, sf, 2, STAT); \
          if ((sf & 1) && ks + 3 < NKS1) G1_LDW(ks + 3, sf >> 1); \
          G1_MM(ks, sf, 3, STAT); \
        } } }
    if (stat) G1_BODY(true) else G1_BODY(false)
    asm volatile("s_nop 15\n\ts_nop 15" ::: "memory");
    STAMP(2, 2);
    if (stat) {
      const int sx = lane & 15;
#pragma unroll
      for (int sp = 0; sp < 2; ++sp) {
        float r2[4];
#pragma unroll
        for (int q = 0; q < 4; ++q) {
          const int sf = 4 * sp + q;
          const float dg = (sx & 2) ? ((sx & 1) ? acc[sf][2][3] : acc[sf][2][2]) : ((sx & 1) ? acc[sf][2][1] : acc[sf][2][0]);
          const float ssq = __shfl(dg, (sx >> 2) * 16 + sx);
          const float mean = acc[sf][0][0] * (1.0f / D);
          const float var = fmaxf(ssq * (1.0f / D) - mean * mean, 0.f);
          const float r = 1.0f / rbf(sqrtf(rbf(rbf(var) + 1e-5f)));
          r2[q] = r * r;
        }
        const float mine = g == 0 ? r2[0] : (g == 1 ? r2[1] : (g == 2 ? r2[2] : r2[3]));
        const int b4 = lane & ~3;
        const f32x4 o = {__shfl(mine, b4), __shfl(mine, b4 + 1), __shfl(mine, b4 + 2), __shfl(mine, b4 + 3)};
        if ((lane & 3) == 0) ST16(rs, WS_ST2 + (size_t)(s0 + 64 * sp + lane) * 4, __builtin_bit_cast(u32x4, o));
      }
    } else {
#pragma unroll
      for (int bi = 0; bi < 2; ++bi) {
        const int blk = blk0 + bi;
        if (blk < 108) {
          const u32 obase = (u32)(WS_HF + ((size_t)(rt * 8 + (lane >> 5)) * NKS2 + (blk >> 1)) * 1024) + (blk & 1) * 512 + (lane & 31) * 16;
#pragma unroll
          for (int pr = 0; pr < 4; ++pr) {
            const f32x4 z4 = {0.f, 0.f, 0.f, 0.f};
            const f32x4 hE = __builtin_elementwise_max(acc[2 * pr][2 * bi], z4) * acc[2 * pr][2 * bi + 1], hO = __builtin_elementwise_max(acc[2 * pr + 1][2 * bi], z4) * acc[2 * pr + 1][2 * bi + 1];
            u32 e0 = pack2(hE[0], hE[1]), e1 = pack2(hE[2], hE[3]), o0 = pack2(hO[0], hO[1]), o1 = pack2(hO[2], hO[3]);
            auto r0 = __builtin_amdgcn_permlane32_swap(e0, o0, false, false); auto r0b = __builtin_amdgcn_permlane16_swap(r0[0], r0[1], false, false);
            auto r1 = __builtin_amdgcn_permlane32_swap(e1, o1, false, false); auto r1b = __builtin_amdgcn_permlane16_swap(r1[0], r1[1], false, false);
            ST16(rs, obase + pr * 2 * NKS2 * 1024, ((u32x4){r0b[0], r1b[0], r0b[1], r1b[1]}));
          }
        }
      }
    }
    if (ct == 13) BAR();
  }
  STAMP(2, 3);
  STAMP_FLUSH(2);
}

static constexpr int G2_LDS = 160 * 1024;
__global__ __launch_bounds__(512, 2) void k3_gemm2(char* __restrict__ ws, u16* __restrict__ out) {
  extern __shared__ __attribute__((aligned(16))) char shm[];
  LAS char* lds = (LAS char*)shm;
  const int t = threadIdx.x, lane = t & 63, w = __builtin_amdgcn_readfirstlane(t >> 6);
  STAMP_DECL; STAMP(3, 0);
  const int xg = blockIdx.x & 7, ix = blockIdx.x >> 3;
  const int rt = (xg & 3) * 8 + (ix & 7), dt = (xg >> 2) * 4 + (ix >> 3);
  const int g = lane >> 4;
  const int k0 = w < 6 ? 7 * w : 42 + 6 * (w - 6);
  const u32 voff = lane * 16;
  u32x2 res[3]; float r2[3];
#pragma unroll
  for (int j = 0; j < 3; ++j) {
    const int f = w + 8 * j < 20 ? w + 8 * j : w, sf = f / 5, df = f % 5, srow = rt * 64 + sf * 16 + (lane & 15);
    res[j] = *(const u32x2*)(ws + WS_HB + ((size_t)(dt * 20 + df * 4 + g) * S + srow) * 8);
    r2[j] = *(const float*)(ws + WS_ST2 + (size_t)srow * 4);
  }
  MEMFENCE();
  const char* wsb = ws + WS_W2T + ((size_t)dt * NKS2P + k0) * 5 * 1024;
  const char* hsb = ws + WS_HF + ((size_t)(rt * 4) * NKS2 + k0) * 1024;
  const char* wsz = ws + WS_W2T + ((size_t)dt * NKS2P + NKS2) * 5 * 1024;
  const char* wlast = w < 6 ? wsb + 6 * 5120 : wsz;
  const char* hlast = w < 6 ? hsb + 6 * 1024 : hsb + 5 * 1024;
  u32x4 fw[4][5], fh[4][4];
#define G2_LD(i, x) do { \
    if ((x) < 5) GLD16(fw[(i) & 3][(x) < 5 ? (x) : 0], voff, ((i) < 6 ? wsb + (i) * 5120 : wlast) + ((x) >= 4 ? 4096 : 0), ((x) >= 4 ? 0 : (x) * 1024)); \
    else GLD16(fh[(i) & 3][(x) >= 5 ? (x) - 5 : 0], voff, ((i) < 6 ? hsb + (i) * 1024 : hlast) + (size_t)((x) - 5) * NKS2 * 1024, 0); } while (0)
#define G2_LDALL(i) do { G2_LD(i, 5); G2_LD(i, 0); G2_LD(i, 1); G2_LD(i, 6); G2_LD(i, 2); G2_LD(i, 3); G2_LD(i, 7); G2_LD(i, 4); G2_LD(i, 8); } while (0)
  G2_LDALL(0); G2_LDALL(1);
  f32x4 acc[20];
#define G2_PIN(i) asm volatile("" : "+v"(fw[(i) & 3][0]), "+v"(fw[(i) & 3][1]), "+v"(fw[(i) & 3][2]), "+v"(fw[(i) & 3][3]), "+v"(fw[(i) & 3][4]), "+v"(fh[(i) & 3][0]), "+v"(fh[(i) & 3][1]), "+v"(fh[(i) & 3][2]), "+v"(fh[(i) & 3][3]))
#define G2_MM(i, m) do { if ((i) == 0) MFMA_Z(acc[m], fw[0][(m) % 5], fh[0][(m) / 5]); else MFMA_IP(acc[m], fw[(i) & 3][(m) % 5], fh[(i) & 3][(m) / 5]); } while (0)
#define G2_STEP(i, PEND) do { WAIT_VM(PEND); G2_PIN(i); \
    _Pragma("unroll") for (int m = 0; m < 20; ++m) { G2_MM(i, m); \
      if ((i) == 0) { if (m < 9) G2_LD(2, m < 9 ? m : 0); else if (m < 18) G2_LD(3, m >= 9 && m < 18 ? m - 9 : 0); } \
      else if ((i) + 3 < 7 && (m & 1) == 0 && m < 18) G2_LD((i) + 3 < 7 ? (i) + 3 : 0, m / 2); } } while (0)
  G2_STEP(0, 9);
  STAMP(3, 1);
  G2_STEP(1, 18); G2_STEP(2, 18); G2_STEP(3, 18); G2_STEP(4, 18); G2_STEP(5, 9); G2_STEP(6, 0);
  asm volatile("s_nop 15\n\ts_nop 15" ::: "memory");
  STAMP(3, 2);
#pragma unroll
  for (int f = 0; f < 20; ++f) *(LAS f32x4*)(lds + (8 * f + w) * 1024 + lane * 16) = acc[f];
  STAMP(3, 3);
  WAIT_LGKM0(); BAR();
  STAMP(3, 4);
  u32 pk[3][2];
#pragma unroll
  for (int j = 0; j < 3; ++j) {
    const int f = w + 8 * j < 20 ? w + 8 * j : w;
    f32x4 a = *(LAS const f32x4*)(lds + (8 * f) * 1024 + lane * 16);
#pragma unroll
    for (int q = 1; q < 8; ++q) a += *(LAS const f32x4*)(lds + (8 * f + q) * 1024 + lane * 16);
    pk[j][0] = pack2(bflo(res[j].x) + a[0] * r2[j], bfhi(res[j].x) + a[1] * r2[j]);
    pk[j][1] = pack2(bflo(res[j].y) + a[2] * r2[j], bfhi(res[j].y) + a[3] * r2[j]);
  }
  const auto ro = MAKE_RSRC(out);
  {
    auto r0 = __builtin_amdgcn_permlane32_swap(pk[0][0], pk[1][0], false, false); auto r0b = __builtin_amdgcn_permlane16_swap(r0[0], r0[1], false, false);
    auto r1 = __builtin_amdgcn_permlane32_swap(pk[0][1], pk[1][1], false, false); auto r1b = __builtin_amdgcn_permlane16_swap(r1[0], r1[1], false, false);
    const int f = w + 8 * (lane >> 5), sf = f / 5, df = f % 5;
    ST16(ro, ((size_t)(rt * 64 + sf * 16 + (lane & 15)) * D + dt * 80 + df * 16 + ((lane >> 4) & 1) * 8) * 2, ((u32x4){r0b[0], r1b[0], r0b[1], r1b[1]}));
  }
  if (w < 4) {
    auto r0 = __builtin_amdgcn_permlane32_swap(pk[2][0], pk[2][0], false, false); auto r0b = __builtin_amdgcn_permlane16_swap(r0[0], r0[1], false, false);
    auto r1 = __builtin_amdgcn_permlane32_swap(pk[2][1], pk[2][1], false, false); auto r1b = __builtin_amdgcn_permlane16_swap(r1[0], r1[1], false, false);
    const int f = w + 16, sf = f / 5, df = f % 5;
    if (lane < 32) ST16(ro, ((size_t)(rt * 64 + sf * 16 + (lane & 15)) * D + dt * 80 + df * 16 + ((lane >> 4) & 1) * 8) * 2, ((u32x4){r0b[0], r1b[0], r0b[1], r1b[1]}));
  }
  STAMP(3, 5);
  STAMP_FLUSH(3);
}

extern "C" void kernel_launch(void* const* d_in, const int* in_sizes, int n_in, void* d_out, int out_size, void* d_ws, size_t ws_size, hipStream_t stream) {
  const u16* q = (const u16*)d_in[0]; const u16* k = (const u16*)d_in[1]; const u16* v = (const u16*)d_in[2];
  const u16* shk = (const u16*)d_in[3]; const u16* fk = (const u16*)d_in[4]; const u16* sD = (const u16*)d_in[5];
  const u16* wg = (const u16*)d_in[7]; const u16* wu = (const u16*)d_in[8]; const u16* wd = (const u16*)d_in[9];
  char* ws = (char*)d_ws;
  (void)hipFuncSetAttribute((const void*)k0_prep, hipFuncAttributeMaxDynamicSharedMemorySize, K0_LDS);
  (void)hipFuncSetAttribute((const void*)k1_conv, hipFuncAttributeMaxDynamicSharedMemorySize, K1_LDS);
  (void)hipFuncSetAttribute((const void*)k2_gemm1, hipFuncAttributeMaxDynamicSharedMemorySize, G1_LDS);
  (void)hipFuncSetAttribute((const void*)k3_gemm2, hipFuncAttributeMaxDynamicSharedMemorySize, G2_LDS);
  k0_prep<<<K0_TILES + K0_STATS, 256, K0_LDS, stream>>>(q, k, v, shk, ws);
  k1_conv<<<K1_CONV + K1_W1 + K1_W2, 512, K1_LDS, stream>>>(q, fk, sD, wg, wu, wd, ws);
  k2_gemm1<<<224, 512, G1_LDS, stream>>>(ws);
  k3_gemm2<<<256, 512, G2_LDS, stream>>>(ws, (u16*)d_out);
}
```

```cpp
#include <hip/hip_runtime.h>
#include <hip/hip_bf16.h>

typedef unsigned short u16;
typedef unsigned int u32;
typedef __attribute__((ext_vector_type(2))) unsigned int u32x2;
typedef __attribute__((ext_vector_type(4))) unsigned int u32x4;
typedef __attribute__((ext_vector_type(8))) short bf16x8;
typedef __attribute__((ext_vector_type(2))) __bf16 bf16x2;
typedef __attribute__((ext_vector_type(2))) float f32x2;
typedef __attribute__((ext_vector_type(4))) float f32x4;
typedef __attribute__((ext_vector_type(4))) unsigned short u16x4;
#define LAS __attribute__((address_space(3)))

static constexpr int S = 2048, D = 640, F = 1728;
static constexpr int NKS1 = D / 32;
static constexpr int NKS2 = F / 32;

static constexpr size_t WS_UR1 = 0;
static constexpr size_t WS_HB  = WS_UR1 + (size_t)D * S * 2;
static constexpr size_t WS_ST1 = WS_HB  + (size_t)D * S * 2;
static constexpr size_t WS_ST2 = WS_ST1 + (size_t)S * 8;
static constexpr size_t WS_W1T = WS_ST2 + (size_t)16 * 1024;
static constexpr int NKS2P = 56;
static constexpr size_t WS_W2T = WS_W1T + (size_t)14 * 8 * NKS1 * 2048;
static constexpr size_t WS_HF  = WS_W2T + (size_t)8 * NKS2P * 5 * 1024;
static constexpr size_t WS_FLG = WS_HF  + (size_t)128 * NKS2 * 1024;
static constexpr size_t WS_FLP = WS_FLG + 4096;
static constexpr size_t WS_END = WS_FLP + 4096;

__device__ __forceinline__ float bflo(u32 w) { return __uint_as_float(w << 16); }
__device__ __forceinline__ float bfhi(u32 w) { return __uint_as_float(w & 0xffff0000u); }
__device__ __forceinline__ u32 pack2(float lo, float hi) { f32x2 v = {lo, hi}; bf16x2 b = __builtin_convertvector(v, bf16x2); return __builtin_bit_cast(u32, b); }
__device__ __forceinline__ float rbf(float x) { return bflo(pack2(x, 0.f)); }
__device__ __forceinline__ float dot2(u32 a, u32 b, float c) { return __builtin_amdgcn_fdot2_f32_bf16(__builtin_bit_cast(bf16x2, a), __builtin_bit_cast(bf16x2, b), c, false); }
__device__ __forceinline__ f32x4 mfma16(u32x4 a, u32x4 b, f32x4 c) { return __builtin_amdgcn_mfma_f32_16x16x32_bf16(__builtin_bit_cast(bf16x8, a), __builtin_bit_cast(bf16x8, b), c, 0, 0, 0); }
__device__ __forceinline__ void dma16(const void* g, LAS void* l) { __builtin_amdgcn_global_load_lds((const u32*)g, (LAS u32*)l, 16, 0, 0); }
#define WAIT_VM(n) asm volatile("s_waitcnt vmcnt(%0)" ::"n"(n) : "memory")
#define WAIT_LGKM0() asm volatile("s_waitcnt lgkmcnt(0)" ::: "memory")
#define BAR() do { asm volatile("" ::: "memory"); __builtin_amdgcn_s_barrier(); asm volatile("" ::: "memory"); } while (0)
#define SB() __builtin_amdgcn_sched_barrier(0)
#define MEMFENCE() asm volatile("" ::: "memory")
#define MAKE_RSRC(p) __builtin_amdgcn_make_buffer_rsrc((void*)(p), 0, 0x7fffffff, 0x00020000)
#define ST16(rs, off, v) __builtin_amdgcn_raw_buffer_store_b128((v), (rs), (int)(off), 0, 16)

template <int CTRL, int RM> __device__ __forceinline__ float dpp_add(float v) { return v + __builtin_bit_cast(float, __builtin_amdgcn_update_dpp(0, __builtin_bit_cast(int, v), CTRL, RM, 0xf, true)); }
__device__ __forceinline__ float wave_sum(float v) {
  v = dpp_add<0x111, 0xf>(v); v = dpp_add<0x112, 0xf>(v); v = dpp_add<0x114, 0xf>(v); v = dpp_add<0x118, 0xf>(v);
  v = dpp_add<0x142, 0xa>(v); v = dpp_add<0x143, 0xc>(v);
  return __builtin_bit_cast(float, __builtin_amdgcn_readlane(__builtin_bit_cast(int, v), 63));
}

#define STAMP(kid, i) do {} while (0)
#define STAMP_DECL do {} while (0)
#define STAMP_FLUSH(kid) do {} while (0)

typedef __attribute__((ext_vector_type(4))) short s16x4;
__device__ __forceinline__ u16x4 tr_read(u32 addr) { u16x4 r; asm volatile("ds_read_b64_tr_b16 %0, %1" : "=v"(r) : "v"(addr) : "memory"); return r; }
#define DS_TR(dst, addr, off) asm volatile("ds_read_b64_tr_b16 %0, %1 offset:%2" : "=v"(dst) : "v"(addr), "n"(off))
#define DS_B64(dst, addr, off) asm volatile("ds_read_b64 %0, %1 offset:%2" : "=v"(dst) : "v"(addr), "n"(off))
#define DS_B128(dst, addr, off) asm volatile("ds_read_b128 %0, %1 offset:%2" : "=v"(dst) : "v"(addr), "n"(off))
#define MFMA_IP(acc, a, b) asm volatile("v_mfma_f32_16x16x32_bf16 %0, %1, %2, %0" : "+v"(acc) : "v"(a), "v"(b))
#define MFMA_Z(acc, a, b) asm volatile("v_mfma_f32_16x16x32_bf16 %0, %1, %2, 0" : "=&v"(acc) : "v"(a), "v"(b))
#define GLD16(dst, voff, sbase, off) asm volatile("global_load_dwordx4 %0, %1, %2 offset:%3" : "=v"(dst) : "v"(voff), "s"(sbase), "n"(off) : "memory")
#define WAIT_LGKM(n) asm volatile("s_waitcnt lgkmcnt(%0)" ::"n"(n) : "memory")

static constexpr int K0_TILES = 32 * 10, K0_STATS = 128;
static constexpr int K0_RAW = 17 * 1024, K0_LDS = K0_RAW + 64 * 144;
__global__ __launch_bounds__(256) void k0_prep(const u16* __restrict__ x, const u16* __restrict__ k, const u16* __restrict__ v,
                                               const u16* __restrict__ shk, char* __restrict__ ws) {
  extern __shared__ __attribute__((aligned(16))) char shm[];
  LAS char* lds = (LAS char*)shm;
  const int t = threadIdx.x, lane = t & 63, w = __builtin_amdgcn_readfirstlane(t >> 6), b = blockIdx.x;
  STAMP_DECL; STAMP(0, 0);
  const auto rs = MAKE_RSRC(ws);
  if (b < K0_TILES) {
    const int st = b / 10, ct = b % 10, s0 = st * 64, c0 = ct * 64;
#pragma unroll
    for (int i = 0; i < 5; ++i) {
      const int pc = w + 4 * i;
      if (pc < 17) {
        const u16* src = pc < 9 ? k : v;
        int row = (pc < 9 ? s0 - 8 + 8 * pc : s0 + 8 * (pc - 9)) + (lane >> 3);
        row = row < 0 ? 0 : row;
        dma16(src + (size_t)row * D + c0 + (lane & 7) * 8, lds + pc * 1024);
      }
    }
    const int cp = t & 31, rg = t >> 5, ch = c0 + 2 * cp;
    const u32x4 sh = *(const u32x4*)(shk + ch * 4);
    WAIT_VM(0); __syncthreads();
    STAMP(0, 1);
    u32 kk[11], vv[8];
    LAS const char* rk = lds + (8 * rg + 5) * 128 + cp * 4;
#pragma unroll
    for (int i = 0; i < 11; ++i) kk[i] = *(LAS const u32*)(rk + i * 128);
#pragma unroll
    for (int i = 0; i < 8; ++i) vv[i] = *(LAS const u32*)(lds + 9 * 1024 + (8 * rg + i) * 128 + cp * 4);
    if (st == 0 && rg == 0) { kk[0] = kk[1] = kk[2] = 0u; }
    const float a0 = bflo(sh.x), a1 = bfhi(sh.x), a2 = bflo(sh.y), a3 = bfhi(sh.y);
    const float b0 = bflo(sh.z), b1 = bfhi(sh.z), b2 = bflo(sh.w), b3 = bfhi(sh.w);
    float ka[11], kb[11];
#pragma unroll
    for (int i = 0; i < 11; ++i) { ka[i] = bflo(kk[i]); kb[i] = bfhi(kk[i]); }
    float ua[8], ub[8];
#pragma unroll
    for (int i = 0; i < 8; ++i) {
      ua[i] = (ka[i] * a3 + ka[i + 1] * a2 + ka[i + 2] * a1 + ka[i + 3] * a0) * bflo(vv[i]);
      ub[i] = (kb[i] * b3 + kb[i + 1] * b2 + kb[i + 2] * b1 + kb[i + 3] * b0) * bfhi(vv[i]);
    }
    LAS char* L1 = lds + K0_RAW;
    const int o = (7 - rg) * 16;
    *(LAS u32x4*)(L1 + (2 * cp) * 144 + o) = (u32x4){pack2(ua[7], ua[6]), pack2(ua[5], ua[4]), pack2(ua[3], ua[2]), pack2(ua[1], ua[0])};
    *(LAS u32x4*)(L1 + (2 * cp + 1) * 144 + o) = (u32x4){pack2(ub[7], ub[6]), pack2(ub[5], ub[4]), pack2(ub[3], ub[2]), pack2(ub[1], ub[0])};
    __syncthreads();
    u32x4 o1[2];
#pragma unroll
    for (int i = 0; i < 2; ++i) o1[i] = *(LAS const u32x4*)(L1 + ((t >> 3) + 32 * i) * 144 + (t & 7) * 16);
#pragma unroll
    for (int i = 0; i < 2; ++i) ST16(rs, WS_UR1 + ((size_t)(c0 + (t >> 3) + 32 * i) * S + (1984 - s0)) * 2 + (t & 7) * 16, o1[i]);
    STAMP(0, 2);
  } else {
    const int rbase = (b - K0_TILES) * 16 + w * 4;
    u32x4 xa[4], xb[4];
#pragma unroll
    for (int r = 0; r < 4; ++r) {
      xa[r] = *(const u32x4*)(x + (size_t)(rbase + r) * D + lane * 8);
      xb[r] = *(const u32x4*)(x + (size_t)(rbase + r) * D + 512 + (lane & 15) * 8);
    }
    const float mk = lane < 16 ? 1.f : 0.f;
    float mean[4], q[4];
#pragma unroll
    for (int r = 0; r < 4; ++r) {
      float s = 0.f, s2 = 0.f;
#pragma unroll
      for (int j = 0; j < 4; ++j) { s += bflo(xa[r][j]) + bfhi(xa[r][j]); s2 += bflo(xb[r][j]) + bfhi(xb[r][j]); }
      mean[r] = wave_sum(s + mk * s2) * (1.0f / D);
    }
#pragma unroll
    for (int r = 0; r < 4; ++r) {
      float s = 0.f, s2 = 0.f;
#pragma unroll
      for (int j = 0; j < 4; ++j) {
        float a = bflo(xa[r][j]) - mean[r], c = bfhi(xa[r][j]) - mean[r]; s += a * a + c * c;
        a = bflo(xb[r][j]) - mean[r]; c = bfhi(xb[r][j]) - mean[r]; s2 += a * a + c * c;
      }
      q[r] = wave_sum(s + mk * s2) * (1.0f / D);
    }
    STAMP(0, 1);
    if (lane < 2) {
      const float m0 = lane ? mean[2] : mean[0], m1 = lane ? mean[3] : mean[1], v0 = lane ? q[2] : q[0], v1 = lane ? q[3] : q[1];
      const float sd0 = rbf(sqrtf(rbf(rbf(v0) + 1e-5f))), sd1 = rbf(sqrtf(rbf(rbf(v1) + 1e-5f)));
      const f32x4 o = {rbf(m0), 1.0f / sd0, rbf(m1), 1.0f / sd1};
      ST16(rs, WS_ST1 + (size_t)(rbase + 2 * lane) * 8, __builtin_bit_cast(u32x4, o));
    }
    STAMP(0, 2);
  }
  if (b == K0_TILES + K0_STATS - 1) { ST16(rs, WS_FLG + t * 16, ((u32x4){0u, 0u, 0u, 0u})); ST16(rs, WS_FLP + t * 16, ((u32x4){0u, 0u, 0u, 0u})); }
  STAMP_FLUSH(0);
}

typedef __attribute__((address_space(1))) unsigned gu32;
__device__ __forceinline__ void publish(char* ws, size_t off) {
  WAIT_VM(0); BAR();
  if (threadIdx.x == 0) __hip_atomic_store((gu32*)(ws + off), 1u, __ATOMIC_RELAXED, __HIP_MEMORY_SCOPE_AGENT);
}
__device__ __forceinline__ void await_flags(char* ws, size_t off, int n, int lane) {
  const unsigned long long t0 = __builtin_amdgcn_s_memrealtime();
  for (;;) {
    bool ok = true;
#pragma unroll
    for (int i = 0; i < 4; ++i) {
      const int idx = lane + 64 * i;
      if (64 * i < n) { const unsigned v = __hip_atomic_load((gu32*)(ws + off + (size_t)(idx < n ? idx : 0) * 16), __ATOMIC_RELAXED, __HIP_MEMORY_SCOPE_AGENT); ok &= v == 1u; }
    }
    if (__all(ok)) break;
    if (__builtin_amdgcn_s_memrealtime() - t0 > 400000ull) break;
    __builtin_amdgcn_s_sleep(2);
  }
  asm volatile("" ::: "memory");
}

static constexpr int CH_BYTES = 13568, CH_F = 0, CH_U1 = 4224, CH_U0 = 4224 + 4608 + 32;
static constexpr int K1_YL = 4 * CH_BYTES;
static constexpr int K1_YP = 8192 + 64;
#define K1_DELAY 80
static constexpr int K1_CONV = 160, K1_W1 = 54, K1_W2 = 30;

template <bool W1>
__device__ __forceinline__ void retile_job(LAS char* lds, const u16* __restrict__ src, int pitch, int cg, int mat, int ks0, char* ws, int w, int lane) {
  constexpr int NU = W1 ? 10 : 9;
  const auto rs = MAKE_RSRC(ws);
#pragma unroll
  for (int i = 0; i < NU; ++i) {
    const int pc = w + 8 * i, row = 8 * pc + (lane >> 3);
    dma16(src + (size_t)(ks0 * 32 + row) * pitch + (((lane & 7) ^ (row & 7)) * 8), lds + pc * 1024);
  }
  WAIT_VM(0); BAR();
  const int g = lane >> 4, q = (lane & 15) >> 2, p = lane & 3, fb = w & 3;
  const u32 l0 = (u32)(size_t)lds;
  u16x4 lo[NU], hi[NU];
#pragma unroll
  for (int i = 0; i < NU; ++i) {
    const int ks = (w >> 2) + 2 * i, row0 = 32 * ks + 8 * g + q, row1 = row0 + 4, chk = 2 * fb + (p >> 1);
    lo[i] = tr_read(l0 + row0 * 128 + ((chk ^ (row0 & 7)) * 16) + 8 * (p & 1));
    hi[i] = tr_read(l0 + row1 * 128 + ((chk ^ (row1 & 7)) * 16) + 8 * (p & 1));
  }
#pragma unroll
  for (int i = 0; i < NU; ++i) asm volatile("s_waitcnt lgkmcnt(0)" : "+v"(lo[i]), "+v"(hi[i])::"memory");
  float m = 0.f;
  if (W1) {
    float cs = 0.f;
#pragma unroll
    for (int i = 0; i < NU; ++i) {
      const u32x2 l2 = __builtin_bit_cast(u32x2, lo[i]), h2 = __builtin_bit_cast(u32x2, hi[i]);
      cs = dot2(l2.x, 0x3f803f80u, dot2(l2.y, 0x3f803f80u, dot2(h2.x, 0x3f803f80u, dot2(h2.y, 0x3f803f80u, cs))));
    }
    cs += __shfl_xor(cs, 16); cs += __shfl_xor(cs, 32);
    LAS float* red = (LAS float*)(lds + 80 * 1024);
    if (lane < 16) red[w * 16 + lane] = cs;
    WAIT_LGKM0(); BAR();
    m = (red[fb * 16 + (lane & 15)] + red[(fb + 4) * 16 + (lane & 15)]) * (1.0f / D);
  }
#pragma unroll
  for (int i = 0; i < NU; ++i) {
    const int ks = (w >> 2) + 2 * i;
    const u32x2 l2 = __builtin_bit_cast(u32x2, lo[i]), h2 = __builtin_bit_cast(u32x2, hi[i]);
    if (W1) {
      const u32x4 o = {pack2(bflo(l2.x) - m, bfhi(l2.x) - m), pack2(bflo(l2.y) - m, bfhi(l2.y) - m), pack2(bflo(h2.x) - m, bfhi(h2.x) - m), pack2(bflo(h2.y) - m, bfhi(h2.y) - m)};
      const int blk = cg * 4 + fb;
      ST16(rs, WS_W1T + ((size_t)(blk * NKS1 + ks) * 2 + mat) * 1024 + lane * 16, o);
    } else {
      const int df = cg * 4 + fb, dt = df / 5, j = df % 5;
      ST16(rs, WS_W2T + ((size_t)(dt * NKS2P + ks0 + ks) * 5 + j) * 1024 + lane * 16, ((u32x4){l2.x, l2.y, h2.x, h2.y}));
    }
  }
  if (!W1 && ks0 == 36 && cg < 8) {
#pragma unroll
    for (int i = 0; i < 2; ++i) { const int pc = w + 8 * i; if (pc < 10) ST16(rs, WS_W2T + ((size_t)(cg * NKS2P + NKS2) * 5 + pc) * 1024 + lane * 16, ((u32x4){0u, 0u, 0u, 0u})); }
  }
  if (W1 && cg == 26 && mat == 0) {
#pragma unroll
    for (int i = 0; i < 3; ++i) { const int ks = w + 8 * i; if (ks < NKS1) ST16(rs, WS_W1T + ((size_t)(108 * NKS1 + ks) * 2) * 1024 + lane * 16, ((u32x4){0x3f803f80u, 0x3f803f80u, 0x3f803f80u, 0x3f803f80u})); }
  }
}

#define SARG
#define SPASS

__device__ __forceinline__ void conv_phase(LAS char* lds, const u16* __restrict__ x, const u16* __restrict__ fk, const u16* __restrict__ sD, char* __restrict__ ws, int b, int t, int lane, int w SARG) {
  const auto rs = MAKE_RSRC(ws);
  const int cgp = (b & 7) * 20 + (b >> 3);
  const int c = w & 3, hf = w >> 2, d = cgp * 4 + c;
  LAS char* cb = lds + c * CH_BYTES;
  {
    const char* gF = (const char*)fk + (size_t)d * S * 2 + lane * 16;
    const char* g1 = ws + WS_UR1 + (size_t)d * S * 2 + lane * 16;
#pragma unroll
    for (int i = 0; i < 2; ++i) {
      const int pc = hf * 2 + i;
      dma16(g1 + pc * 1024, cb + CH_U1 + pc * 1024);
      dma16(gF + pc * 1024, cb + CH_F + 64 + pc * 1024);
    }
    if (hf == 0) { if (lane < 32) *(LAS u32x4*)(cb + CH_U1 + 4096 + lane * 16) = (u32x4){0, 0, 0, 0}; if (lane < 4) *(LAS u32x4*)(cb + CH_F + lane * 16) = (u32x4){0, 0, 0, 0}; }
    else { if (lane < 32) *(LAS u32x4*)(cb + CH_U0 + 4096 + lane * 16) = (u32x4){0, 0, 0, 0}; }
  }
  const u32 dw = sD[d];
  WAIT_VM(0); WAIT_LGKM0(); BAR();
#pragma unroll
  for (int i = 0; i < 2; ++i) {
    const int p0 = 1024 * hf + 512 * i + 8 * lane;
    const u32x4 in = *(LAS const u32x4*)(cb + CH_U1 + p0 * 2); const u32 nx = *(LAS const u32*)(cb + CH_U1 + p0 * 2 + 16);
    *(LAS u32x4*)(cb + CH_U0 + p0 * 2) = (u32x4){__builtin_amdgcn_alignbit(in.y, in.x, 16), __builtin_amdgcn_alignbit(in.z, in.y, 16), __builtin_amdgcn_alignbit(in.w, in.z, 16), __builtin_amdgcn_alignbit(nx, in.w, 16)};
  }
  WAIT_LGKM0(); BAR();
  STAMP(1, 1);
  u32x2 xr[4]; u32x4 stv[2];
  const char* gx = (const char*)x + (size_t)(2 * t) * D * 2 + cgp * 8;
#define C_LDX(i) do { \
      if ((i) == 0) asm volatile("global_load_dwordx2 %0, %1, off" : "=v"(xr[0]) : "v"(gx) : "memory"); \
      if ((i) == 1) asm volatile("global_load_dwordx2 %0, %1, off offset:1280" : "=v"(xr[1]) : "v"(gx) : "memory"); \
      if ((i) == 2) asm volatile("global_load_dwordx2 %0, %1, off" : "=v"(xr[2]) : "v"(gx + (size_t)1024 * D * 2) : "memory"); \
      if ((i) == 3) asm volatile("global_load_dwordx2 %0, %1, off offset:1280" : "=v"(xr[3]) : "v"(gx + (size_t)1024 * D * 2) : "memory"); \
      if ((i) == 4) GLD16(stv[0], t * 16, ws + WS_ST1, 0); \
      if ((i) == 5) GLD16(stv[1], t * 16, ws + WS_ST1 + 8192, 0); } while (0)
#define C_XWAIT() asm volatile("s_waitcnt vmcnt(0) lgkmcnt(0)" : "+v"(xr[0]), "+v"(xr[1]), "+v"(xr[2]), "+v"(xr[3]), "+v"(stv[0]), "+v"(stv[1])::"memory")
  if (hf == 0) {
    const int r = lane & 15, g = lane >> 4;
    f32x4 acc[8];
    const int a = r >> 1, e = r & 1;
    const u32 ub = (u32)(size_t)(cb + (e ? CH_U1 : CH_U0) + 448 - 64 * a + 16 * g);
    u32 fbp[4];
#pragma unroll
    for (int i = 0; i < 4; ++i) fbp[i] = (u32)(size_t)(cb + CH_F + 16 * g + 4 * r + 4) + 1024 * i;
    u32x4 U[2][8]; u32x2 Fr[2][8][2];
#define C_RDU(buf, i, cc) DS_B128(U[buf][i], ub, 512 * (7 - (i)) + 64 * (cc))
#define C_RDF(buf, i, cc, hh) asm volatile("ds_read2_b32 %0, %1 offset0:%2 offset1:%3" : "=v"(Fr[buf][i][hh]) : "v"(fbp[(i) >> 1]), "n"(128 * ((i) & 1) + 16 * (cc) + 2 * (hh)), "n"(128 * ((i) & 1) + 16 * (cc) + 2 * (hh) + 1))
#define C_F(buf, i) ((u32x4){Fr[buf][i][0].x, Fr[buf][i][0].y, Fr[buf][i][1].x, Fr[buf][i][1].y})
#define C_WAIT(buf) do { \
      asm volatile("s_waitcnt lgkmcnt(0)" : "+v"(U[buf][0]), "+v"(U[buf][1]), "+v"(U[buf][2]), "+v"(U[buf][3]), "+v"(U[buf][4]), "+v"(U[buf][5]), "+v"(U[buf][6]), "+v"(U[buf][7])::"memory"); \
      asm volatile("" : "+v"(Fr[buf][0][0]), "+v"(Fr[buf][0][1]), "+v"(Fr[buf][1][0]), "+v"(Fr[buf][1][1]), "+v"(Fr[buf][2][0]), "+v"(Fr[buf][2][1]), "+v"(Fr[buf][3][0]), "+v"(Fr[buf][3][1]), \
                        "+v"(Fr[buf][4][0]), "+v"(Fr[buf][4][1]), "+v"(Fr[buf][5][0]), "+v"(Fr[buf][5][1]), "+v"(Fr[buf][6][0]), "+v"(Fr[buf][6][1]), "+v"(Fr[buf][7][0]), "+v"(Fr[buf][7][1])); } while (0)
#pragma unroll
    for (int i = 0; i < 8; ++i) { C_RDF(0, i, 0, 0); C_RDF(0, i, 0, 1); C_RDU(0, i, 0); }
    C_WAIT(0);
#pragma unroll
    for (int cc = 0; cc < 8; ++cc) {
      const int cur = cc & 1, nxt = cur ^ 1;
      int n = 0;
#pragma unroll
      for (int c1 = 0; c1 < 8; ++c1)
#pragma unroll
        for (int i = c1; i < 8; ++i) {
          if (cc == 0 && c1 == 0) MFMA_Z(acc[i], U[cur][i - c1], C_F(cur, c1)); else MFMA_IP(acc[i], U[cur][i - c1], C_F(cur, c1));
          if (cc < 7 && n < 24) { const int j = n / 3, kk = n % 3; if (kk == 0) C_RDU(nxt, j, cc + 1); else C_RDF(nxt, j, cc + 1, kk - 1); }
          if (cc < 6 && n == 28) C_LDX(cc);
          ++n;
        }
      if (cc < 7) C_WAIT(nxt);
    }
    asm volatile("s_nop 15\n\ts_nop 15" ::: "memory");
    STAMP(1, 2);
    LAS char* yl = lds + K1_YL + c * K1_YP;
    const float Dd = bflo(dw);
#pragma unroll
    for (int j = 0; j < 8; ++j)
#pragma unroll
      for (int hh = 0; hh < 2; ++hh) {
        const int s = 256 * j + 32 * (2 * g + hh) + 2 * r;
        const u32 uw = *(LAS const u32*)(cb + CH_U1 + (2046 - s) * 2);
        *(LAS f32x2*)(yl + s * 4) = (f32x2){acc[j][2 * hh] + bfhi(uw) * Dd, acc[j][2 * hh + 1] + bflo(uw) * Dd};
      }
    C_XWAIT();
  } else {
    C_LDX(0); C_LDX(1); C_LDX(2); C_LDX(3); C_LDX(4); C_LDX(5);
    C_XWAIT();
  }
  BAR();
  STAMP(1, 3);
#pragma unroll
  for (int i = 0; i < 2; ++i) {
    const int s = 2 * t + 1024 * i;
    f32x2 y2[4];
#pragma unroll
    for (int cc = 0; cc < 4; ++cc) y2[cc] = *(LAS const f32x2*)(lds + K1_YL + cc * K1_YP + s * 4);
    const f32x4 st = __builtin_bit_cast(f32x4, stv[i]);
    const u32x2 x0 = xr[2 * i], x1 = xr[2 * i + 1];
    const float xa[4] = {bflo(x0.x), bfhi(x0.x), bflo(x0.y), bfhi(x0.y)}, xb[4] = {bflo(x1.x), bfhi(x1.x), bflo(x1.y), bfhi(x1.y)};
    float ha[4], hb[4];
#pragma unroll
    for (int cc = 0; cc < 4; ++cc) { ha[cc] = xa[cc] + (xa[cc] - st[0]) * st[1] * y2[cc][0]; hb[cc] = xb[cc] + (xb[cc] - st[2]) * st[3] * y2[cc][1]; }
    ST16(rs, WS_HB + ((size_t)cgp * S + s) * 8, ((u32x4){pack2(ha[0], ha[1]), pack2(ha[2], ha[3]), pack2(hb[0], hb[1]), pack2(hb[2], hb[3])}));
  }
  STAMP(1, 4);
}

static constexpr int G1_NS = 16, G1_SLOT = 8192, G1_WR = 4;
#define DMA_SC1(m0v, voffv, sbase, off) asm volatile("s_mov_b32 m0, %0\n\ts_nop 0\n\tglobal_load_lds_dwordx4 %1, %2 offset:%3 sc1" :: "s"(m0v), "v"(voffv), "s"(sbase), "n"(off) : "memory")
#define DMA_PL(m0v, voffv, sbase, off) asm volatile("s_mov_b32 m0, %0\n\ts_nop 0\n\tglobal_load_lds_dwordx4 %1, %2 offset:%3" :: "s"(m0v), "v"(voffv), "s"(sbase), "n"(off) : "memory")
#define GLD16C(dst, voff, sbase, off) asm volatile("global_load_dwordx4 %0, %1, %2 offset:%3 sc1" : "=v"(dst) : "v"(voff), "s"(sbase), "n"(off) : "memory")

__device__ __forceinline__ void gemm1_phase(LAS char* lds, char* __restrict__ ws, int rt, int ct, int lane, int w SARG) {
  const u32 lds0 = (u32)(size_t)lds;
  const int s0 = rt * 128;
  const u32 voff = lane * 16;
  const auto rs = MAKE_RSRC(ws);
  if (w >= 4) {
    const int j = w - 4;
    const u32 hv = voff ^ ((j & 1) * 128);
    const char* hsb = ws + WS_HB + ((size_t)(2 * j) * S + s0) * 8;
    const u32 ldsw = lds0 + 2 * j * 1024;
#define G1_SLAB(ks) do { DMA_SC1(ldsw + ((ks) % G1_NS) * G1_SLOT, hv, hsb + (size_t)(ks) * 8 * S * 8, 0); DMA_SC1(ldsw + ((ks) % G1_NS) * G1_SLOT + 1024, hv, hsb + (size_t)(ks) * 8 * S * 8 + S * 8, 0); } while (0)
    G1_SLAB(0); G1_SLAB(1); G1_SLAB(2); G1_SLAB(3); G1_SLAB(4);
    WAIT_VM(8); BAR();
    G1_SLAB(5); G1_SLAB(6); G1_SLAB(7); G1_SLAB(8);
    WAIT_VM(8); BAR();
    G1_SLAB(9); G1_SLAB(10); G1_SLAB(11); G1_SLAB(12);
    WAIT_VM(8); BAR();
    G1_SLAB(13); G1_SLAB(14); G1_SLAB(15);
    WAIT_VM(6); BAR();
    G1_SLAB(16); G1_SLAB(17); G1_SLAB(18); G1_SLAB(19);
    WAIT_VM(6); BAR();
    WAIT_VM(0); BAR();
    BAR();
  } else {
    const int blk0 = ct * 8 + 2 * w;
    const char* wsA = ws + WS_W1T + (size_t)(blk0 > 108 ? 108 : blk0) * NKS1 * 2048;
    const char* wsB = ws + WS_W1T + (size_t)(blk0 + 1 > 108 ? 108 : blk0 + 1) * NKS1 * 2048;
    const int g = lane >> 4;
    const u32 hbase = lds0 + g * 2048 + (lane & 15) * 8;
    const u32 hoffE = hbase + (g & 1) * 128, hoffO = hbase + (1 - (g & 1)) * 128 - 128;
    const u32 hoffE2 = hoffE + 65536, hoffO2 = hoffO + 65536;
    f32x4 acc[8][4];
    const int stat = __builtin_amdgcn_readfirstlane(blk0 == 108 ? 1 : 0);
#define G1_LDW(ks, i) do { if ((i) == 0) GLD16C(wr[(ks) % G1_WR][0], voff, wsA + (ks) * 2048, 0); if ((i) == 1) GLD16C(wr[(ks) % G1_WR][1], voff, wsA + (ks) * 2048, 1024); \
      if ((i) == 2) GLD16C(wr[(ks) % G1_WR][2], voff, wsB + (ks) * 2048, 0); if ((i) == 3) GLD16C(wr[(ks) % G1_WR][3], voff, wsB + (ks) * 2048, 1024); } while (0)
#define G1_RD(sf, hh, ks) DS_B64(hf[sf][hh], ((ks) % G1_NS) >= 8 ? (((sf) & 1) ? hoffO2 : hoffE2) : (((sf) & 1) ? hoffO : hoffE), (((ks) % G1_NS) & 7) * G1_SLOT + (hh) * 1024 + (sf) * 128)
#define G1_H(sf) ((u32x4){hf[sf][0].x, hf[sf][0].y, hf[sf][1].x, hf[sf][1].y})
#define G1_MM(ks, sf, i, STAT) do { \
      if (STAT) { if ((i) == 0) { if ((ks) == 0) MFMA_Z(acc[sf][0], wr[0][0], G1_H(sf)); else MFMA_IP(acc[sf][0], wr[(ks) % G1_WR][0], G1_H(sf)); } \
                  if ((i) == 2) { if ((ks) == 0) MFMA_Z(acc[sf][2], G1_H(sf), G1_H(sf)); else MFMA_IP(acc[sf][2], G1_H(sf), G1_H(sf)); } } \
      else { if ((ks) == 0) MFMA_Z(acc[sf][i], wr[0][i], G1_H(sf)); else MFMA_IP(acc[sf][i], wr[(ks) % G1_WR][i], G1_H(sf)); } } while (0)
#define G1_BODY(STAT) { \
      u32x4 wr[G1_WR][4]; u32x2 hf[8][2]; \
      _Pragma("unroll") for (int ks = 0; ks < 3; ++ks) { G1_LDW(ks, 0); G1_LDW(ks, 1); G1_LDW(ks, 2); G1_LDW(ks, 3); } \
      BAR();                                                   \
      STAMP(1, 7); \
      _Pragma("unroll") for (int sf = 0; sf < 7; ++sf) { G1_RD(sf, 0, 0); G1_RD(sf, 1, 0); } \
      _Pragma("unroll") for (int ks = 0; ks < NKS1; ++ks) { \
        if ((ks & 3) == 0) BAR(); \
          \
        if (ks + 2 < NKS1) WAIT_VM(8); else if (ks + 1 < NKS1) WAIT_VM(4); else WAIT_VM(0); \
        asm volatile("" : "+v"(wr[ks % G1_WR][0]), "+v"(wr[ks % G1_WR][1]), "+v"(wr[ks % G1_WR][2]), "+v"(wr[ks % G1_WR][3])); \
        _Pragma("unroll") for (int sf = 0; sf < 8; ++sf) { \
            \
          if (ks + 1 < NKS1 || sf == 0) asm volatile("s_waitcnt lgkmcnt(12)" : "+v"(hf[sf][0]), "+v"(hf[sf][1])::"memory"); \
          else if (sf < 7) asm volatile("s_waitcnt lgkmcnt(%2)" : "+v"(hf[sf][0]), "+v"(hf[sf][1]) : "n"(14 - 2 * sf) : "memory"); \
          else asm volatile("s_waitcnt lgkmcnt(0)" : "+v"(hf[sf][0]), "+v"(hf[sf][1])::"memory"); \
          G1_MM(ks, sf, 0, STAT); \
          if (sf == 0) G1_RD(7, 0, ks); else if (ks + 1 < NKS1) G1_RD(sf - 1, 0, ks + 1); \
          G1_MM(ks, sf, 1, STAT); \
          if (sf == 0) G1_RD(7, 1, ks); else if (ks + 1 < NKS1) G1_RD(sf - 1, 1, ks + 1); \
          G1_MM(ks, sf, 2, STAT); \
          if ((sf & 1) && ks + 3 < NKS1) G1_LDW(ks + 3, sf >> 1); \
          G1_MM(ks, sf, 3, STAT); \
        } } }
    if (stat) G1_BODY(true) else G1_BODY(false)
    BAR();
    asm volatile("s_nop 15\n\ts_nop 15" ::: "memory");
    STAMP(1, 8);
    if (stat) {
      const int sx = lane & 15;
#pragma unroll
      for (int sp = 0; sp < 2; ++sp) {
        float r2[4];
#pragma unroll
        for (int q = 0; q < 4; ++q) {
          const int sf = 4 * sp + q;
          const float dg = (sx & 2) ? ((sx & 1) ? acc[sf][2][3] : acc[sf][2][2]) : ((sx & 1) ? acc[sf][2][1] : acc[sf][2][0]);
          const float ssq = __shfl(dg, (sx >> 2) * 16 + sx);
          const float mean = acc[sf][0][0] * (1.0f / D);
          const float var = fmaxf(ssq * (1.0f / D) - mean * mean, 0.f);
          const float r = 1.0f / rbf(sqrtf(rbf(rbf(var) + 1e-5f)));
          r2[q] = r * r;
        }
        const float mine = g == 0 ? r2[0] : (g == 1 ? r2[1] : (g == 2 ? r2[2] : r2[3]));
        const int b4 = lane & ~3;
        const f32x4 o = {__shfl(mine, b4), __shfl(mine, b4 + 1), __shfl(mine, b4 + 2), __shfl(mine, b4 + 3)};
        if ((lane & 3) == 0) ST16(rs, WS_ST2 + (size_t)(s0 + 64 * sp + lane) * 4, __builtin_bit_cast(u32x4, o));
      }
    } else {
#pragma unroll
      for (int bi = 0; bi < 2; ++bi) {
        const int blk = blk0 + bi;
        if (blk < 108) {
          const u32 obase = (u32)(WS_HF + ((size_t)(rt * 8 + (lane >> 5)) * NKS2 + (blk >> 1)) * 1024) + (blk & 1) * 512 + (lane & 31) * 16;
#pragma unroll
          for (int pr = 0; pr < 4; ++pr) {
            const f32x4 z4 = {0.f, 0.f, 0.f, 0.f};
            const f32x4 hE = __builtin_elementwise_max(acc[2 * pr][2 * bi], z4) * acc[2 * pr][2 * bi + 1], hO = __builtin_elementwise_max(acc[2 * pr + 1][2 * bi], z4) * acc[2 * pr + 1][2 * bi + 1];
            u32 e0 = pack2(hE[0], hE[1]), e1 = pack2(hE[2], hE[3]), o0 = pack2(hO[0], hO[1]), o1 = pack2(hO[2], hO[3]);
            auto r0 = __builtin_amdgcn_permlane32_swap(e0, o0, false, false); auto r0b = __builtin_amdgcn_permlane16_swap(r0[0], r0[1], false, false);
            auto r1 = __builtin_amdgcn_permlane32_swap(e1, o1, false, false); auto r1b = __builtin_amdgcn_permlane16_swap(r1[0], r1[1], false, false);
            ST16(rs, obase + pr * 2 * NKS2 * 1024, ((u32x4){r0b[0], r1b[0], r0b[1], r1b[1]}));
          }
        }
      }
    }
  }
}

static constexpr int G2_STAGE = 15 * 1024, G2_NS = 5;
__device__ __forceinline__ void gemm2_prefetch(LAS char* lds, char* __restrict__ ws, int dt, int lane, int iw, int nw) {
  const u32 lds0 = (u32)(size_t)lds, voff = lane * 16;
  const char* wb = ws + WS_W2T + (size_t)dt * NKS2P * 5 * 1024;
  for (int pc = iw; pc < 150; pc += nw) {
    const int kh = pc >= 75, o = (pc - 75 * kh) * 1024;
    const u32 m0v = __builtin_amdgcn_readfirstlane(lds0 + kh * G2_NS * G2_STAGE + o);
    const char* sb = wb + kh * 27 * 5120 + o;
    DMA_SC1(m0v, voff, sb, 0);
  }
}
__device__ __forceinline__ void gemm2_phase(LAS char* lds, char* __restrict__ ws, u16* __restrict__ out, int rt, int dt, int lane, int w SARG) {
  const int sf = w & 3, kh = w >> 2, g = lane >> 4;
  const u32 voff = lane * 16;
  const char* hsrc = ws + WS_HF + ((size_t)(rt * 4 + sf) * NKS2 + kh * 27) * 1024;
  LAS char* ring = lds + kh * G2_NS * G2_STAGE;
  u32x4 hq[G2_NS][3];
  const u32 ldsw = (u32)(size_t)ring + sf * 3840;
  const char* wsb = ws + WS_W2T + ((size_t)dt * NKS2P + kh * 27) * 5 * 1024 + sf * 3840;
  const unsigned long long m48 = 0x0000ffffffffffffull;
  const int srow = rt * 64 + sf * 16 + (lane & 15);
  float r2; u32x2 res[3];
  asm volatile("global_load_dword %0, %1, off sc1" : "=v"(r2) : "v"(ws + WS_ST2 + (size_t)srow * 4) : "memory");
#pragma unroll
  for (int j = 0; j < 3; ++j) { const int df = kh ? (j < 2 ? 3 + j : 3) : j; asm volatile("global_load_dwordx2 %0, %1, off sc1" : "=v"(res[j]) : "v"(ws + WS_HB + ((size_t)(dt * 20 + df * 4 + g) * S + srow) * 8) : "memory"); }
#define G2_LH(st, i) GLD16C(hq[(st) % G2_NS][i], voff, hsrc + (st) * 3072, (i) * 1024)
#define G2_LW(st, pc) do { \
    if ((pc) < 3) DMA_SC1(ldsw + ((st) % G2_NS) * G2_STAGE, voff, wsb + (st) * G2_STAGE, (pc) * 1024);     \
    else asm volatile("s_mov_b32 m0, %0\n\ts_mov_b64 exec, %4\n\tglobal_load_lds_dwordx4 %1, %2 offset:%3 sc1\n\ts_mov_b64 exec, -1" :: "s"(ldsw + ((st) % G2_NS) * G2_STAGE), "v"(voff), "s"(wsb + (st) * G2_STAGE), "n"(3072), "s"(m48) : "memory"); } while (0)
#define G2_X(x) do { constexpr int x_ = (x); \
    if (x_ < 6) G2_LH(3 + x_ / 3, x_ % 3); \
    else if (x_ < 34) { constexpr int y_ = x_ >= 6 ? x_ - 6 : 0, st_ = 5 + y_ / 7, pc_ = y_ % 7; if (pc_ < 4) G2_LW(st_, pc_); else G2_LH(st_, pc_ >= 4 ? pc_ - 4 : 0); } } while (0)
  G2_LH(0, 0); G2_LH(0, 1); G2_LH(0, 2); G2_LH(1, 0); G2_LH(1, 1); G2_LH(1, 2); G2_LH(2, 0); G2_LH(2, 1); G2_LH(2, 2);
  f32x4 acc[5];
  const u32 rbase = (u32)(size_t)ring + lane * 16;
  u32x4 wf[2][5];
#define G2_OFF(n) ((((n) / 3) % G2_NS) * G2_STAGE + ((n) % 3) * 5120)
#define G2_READ(n) do { const u32 a_ = rbase + (G2_OFF(n) & ~0x7fff); \
    DS_B128(wf[(n) & 1][0], a_, G2_OFF(n) & 0x7fff); DS_B128(wf[(n) & 1][1], a_, (G2_OFF(n) & 0x7fff) + 1024); DS_B128(wf[(n) & 1][2], a_, (G2_OFF(n) & 0x7fff) + 2048); \
    DS_B128(wf[(n) & 1][3], a_, (G2_OFF(n) & 0x7fff) + 3072); DS_B128(wf[(n) & 1][4], a_, (G2_OFF(n) & 0x7fff) + 4096); } while (0)
  G2_READ(0);
  WAIT_VM(6);
  asm volatile("" : "+v"(r2), "+v"(res[0]), "+v"(res[1]), "+v"(res[2]));
  STAMP(1, 11);
#define G2_NEED(s) ((s) <= 2 ? 3 * ((s) + 1) : ((s) <= 4 ? 9 + 3 * ((s) - 2) : 15 + 7 * ((s) - 4)))
#define G2_MM(n, j) do { if ((n) == 0) MFMA_Z(acc[j], wf[0][j], hq[0][0]); else MFMA_IP(acc[j], wf[(n) & 1][j], hq[((n) / 3) % G2_NS][(n) % 3]); } while (0)
#define G2_STEP(n) do { constexpr int st = (n) / 3, i = (n) % 3; \
    if (i == 2 && st + 1 < 9) { \
        \
      constexpr int issued = 9 + (2 * (n) < 34 ? 2 * (n) : 34); \
      WAIT_VM(issued - G2_NEED(st + 1)); \
      WAIT_LGKM(0); \
      BAR(); \
    } \
    if (i == 0) asm volatile("" : "+v"(hq[st % G2_NS][0]), "+v"(hq[st % G2_NS][1]), "+v"(hq[st % G2_NS][2])); \
    asm volatile("s_waitcnt lgkmcnt(0)" : "+v"(wf[(n) & 1][0]), "+v"(wf[(n) & 1][1]), "+v"(wf[(n) & 1][2]), "+v"(wf[(n) & 1][3]), "+v"(wf[(n) & 1][4])::"memory"); \
    if ((n) + 1 < 27) G2_READ(((n) + 1 < 27 ? (n) + 1 : 0)); \
    G2_MM(n, 0); G2_X(2 * (n)); \
    G2_MM(n, 1); G2_MM(n, 2); G2_X(2 * (n) + 1); \
    G2_MM(n, 3); G2_MM(n, 4); } while (0)
  G2_STEP(0); G2_STEP(1); G2_STEP(2); G2_STEP(3); G2_STEP(4); G2_STEP(5); G2_STEP(6); G2_STEP(7); G2_STEP(8);
  G2_STEP(9); G2_STEP(10); G2_STEP(11); G2_STEP(12); G2_STEP(13); G2_STEP(14); G2_STEP(15); G2_STEP(16); G2_STEP(17);
  G2_STEP(18); G2_STEP(19); G2_STEP(20); G2_STEP(21); G2_STEP(22); G2_STEP(23); G2_STEP(24); G2_STEP(25); G2_STEP(26);
  asm volatile("s_nop 15\n\ts_nop 15" ::: "memory");
  STAMP(1, 12);
  BAR();
  LAS f32x4* xch = (LAS f32x4*)lds;
#pragma unroll
  for (int j = 0; j < 3; ++j) { if (kh == 0) { if (j < 2) xch[(w * 3 + j) * 64 + lane] = acc[3 + j]; } else xch[(w * 3 + j) * 64 + lane] = acc[j]; }
  WAIT_LGKM0(); BAR();
  u32 pk[3][2];
#pragma unroll
  for (int j = 0; j < 3; ++j) {
    const f32x4 o = xch[((w ^ 4) * 3 + (kh && j == 2 ? 0 : j)) * 64 + lane];
    const f32x4 a = kh ? acc[3 + (j < 2 ? j : 0)] : acc[j];
    const u32x2 rr = res[j];
    pk[j][0] = pack2(bflo(rr.x) + (a[0] + o[0]) * r2, bfhi(rr.x) + (a[1] + o[1]) * r2);
    pk[j][1] = pack2(bflo(rr.y) + (a[2] + o[2]) * r2, bfhi(rr.y) + (a[3] + o[3]) * r2);
  }
  const auto ro = MAKE_RSRC(out);
  {
    auto r0 = __builtin_amdgcn_permlane32_swap(pk[0][0], pk[1][0], false, false); auto r0b = __builtin_amdgcn_permlane16_swap(r0[0], r0[1], false, false);
    auto r1 = __builtin_amdgcn_permlane32_swap(pk[0][1], pk[1][1], false, false); auto r1b = __builtin_amdgcn_permlane16_swap(r1[0], r1[1], false, false);
    const int col = dt * 80 + (kh ? 48 : 0) + (lane >> 5) * 16 + ((lane >> 4) & 1) * 8;
    ST16(ro, ((size_t)srow * D + col) * 2, ((u32x4){r0b[0], r1b[0], r0b[1], r1b[1]}));
  }
  if (kh == 0) {
    auto r0 = __builtin_amdgcn_permlane32_swap(pk[2][0], pk[2][0], false, false); auto r0b = __builtin_amdgcn_permlane16_swap(r0[0], r0[1], false, false);
    auto r1 = __builtin_amdgcn_permlane32_swap(pk[2][1], pk[2][1], false, false); auto r1b = __builtin_amdgcn_permlane16_swap(r1[0], r1[1], false, false);
    const int col = dt * 80 + 32 + ((lane >> 4) & 1) * 8;
    if (lane < 32) ST16(ro, ((size_t)srow * D + col) * 2, ((u32x4){r0b[0], r1b[0], r0b[1], r1b[1]}));
  }
}

static constexpr int KF_LDS = 160 * 1024;
__global__ __launch_bounds__(512, 2) void kf_fused(const u16* __restrict__ x, const u16* __restrict__ fk, const u16* __restrict__ sD, const u16* __restrict__ wg,
                                                   const u16* __restrict__ wu, const u16* __restrict__ wd, char* __restrict__ ws, u16* __restrict__ out) {
  extern __shared__ __attribute__((aligned(16))) char shm[];
  LAS char* lds = (LAS char*)shm;
  const int t = threadIdx.x, lane = t & 63, w = __builtin_amdgcn_readfirstlane(t >> 6), b = blockIdx.x;
  STAMP_DECL; STAMP(1, 0);
  if (b < K1_CONV) conv_phase(lds, x, fk, sD, ws, b, t, lane, w SPASS);
  else {
    { const unsigned long long t0 = __builtin_amdgcn_s_memrealtime(); while (__builtin_amdgcn_s_memrealtime() - t0 < K1_DELAY) __builtin_amdgcn_s_sleep(4); }
    const int jb = b - K1_CONV;
    if (jb < K1_W1) retile_job<true>(lds, ((jb & 1) ? wu : wg) + (jb >> 1) * 64, F, jb >> 1, jb & 1, 0, ws, w, lane);
    else if (jb < K1_W1 + K1_W2) { const int j2 = jb - K1_W1; retile_job<false>(lds, wd + (j2 / 3) * 64, D, j2 / 3, 0, (j2 % 3) * 18, ws, w, lane); }
  }
  publish(ws, WS_FLG + (size_t)b * 16);
  STAMP(1, 5);
  const int xg = b & 7, ix = b >> 3;
  const int p = (xg & 3) * 4 + (ix & 3), j = (xg >> 2) * 8 + (ix >> 2);
  if (w == 7) await_flags(ws, WS_FLG, K1_CONV + K1_W1 + K1_W2, lane);
  BAR();
  STAMP(1, 6);
  if (j < 14) {
    gemm1_phase(lds, ws, p, j, lane, w SPASS);
    if (w >= 4) gemm2_prefetch(lds, ws, j >> 1, lane, w - 4, 4);
    publish(ws, WS_FLP + (size_t)(p * 16 + j) * 16);
  } else {
    gemm2_prefetch(lds, ws, j >> 1, lane, w, 8);
    WAIT_VM(0);
  }
  STAMP(1, 9);
  if (w == 7) await_flags(ws, WS_FLP + (size_t)p * 256, 14, lane);
  BAR();
  STAMP(1, 10);
  gemm2_phase(lds, ws, out, p * 2 + (j & 1), j >> 1, lane, w SPASS);
  STAMP(1, 13);
  STAMP_FLUSH(1);
}

extern "C" void kernel_launch(void* const* d_in, const int* in_sizes, int n_in, void* d_out, int out_size, void* d_ws, size_t ws_size, hipStream_t stream) {
  const u16* q = (const u16*)d_in[0]; const u16* k = (const u16*)d_in[1]; const u16* v = (const u16*)d_in[2];
  const u16* shk = (const u16*)d_in[3]; const u16* fk = (const u16*)d_in[4]; const u16* sD = (const u16*)d_in[5];
  const u16* wg = (const u16*)d_in[7]; const u16* wu = (const u16*)d_in[8]; const u16* wd = (const u16*)d_in[9];
  char* ws = (char*)d_ws;
  (void)hipFuncSetAttribute((const void*)k0_prep, hipFuncAttributeMaxDynamicSharedMemorySize, K0_LDS);
  (void)hipFuncSetAttribute((const void*)kf_fused, hipFuncAttributeMaxDynamicSharedMemorySize, KF_LDS);
  k0_prep<<<K0_TILES + K0_STATS, 256, K0_LDS, stream>>>(q, k, v, shk, ws);
  kf_fused<<<256, 512, KF_LDS, stream>>>(q, fk, sD, wg, wu, wd, ws, (u16*)d_out);
}
```

```cpp
#define K0_NPF 0
#define K1_DELAY 80
#include <hip/hip_runtime.h>
#include <hip/hip_bf16.h>

typedef unsigned short u16;
typedef unsigned int u32;
typedef __attribute__((ext_vector_type(2))) unsigned int u32x2;
typedef __attribute__((ext_vector_type(4))) unsigned int u32x4;
typedef __attribute__((ext_vector_type(8))) short bf16x8;
typedef __attribute__((ext_vector_type(2))) __bf16 bf16x2;
typedef __attribute__((ext_vector_type(2))) float f32x2;
typedef __attribute__((ext_vector_type(4))) float f32x4;
typedef __attribute__((ext_vector_type(4))) unsigned short u16x4;
#define LAS __attribute__((address_space(3)))

static constexpr int S = 2048, D = 640, F = 1728;
static constexpr int NKS1 = D / 32;
static constexpr int NKS2 = F / 32;

static constexpr size_t WS_UR1 = 0;
static constexpr size_t WS_HB  = WS_UR1 + (size_t)D * S * 2;
static constexpr size_t WS_ST1 = WS_HB  + (size_t)D * S * 2;
static constexpr size_t WS_ST2 = WS_ST1 + (size_t)S * 8;
static constexpr size_t WS_W1T = WS_ST2 + (size_t)16 * 1024;
static constexpr int NKS2P = 56;
static constexpr size_t WS_W2T = WS_W1T + (size_t)14 * 8 * NKS1 * 2048;
static constexpr size_t WS_HF  = WS_W2T + (size_t)8 * NKS2P * 5 * 1024;
static constexpr size_t WS_FLG = WS_HF  + (size_t)128 * NKS2 * 1024;
static constexpr size_t WS_FLP = WS_FLG + 4096;
static constexpr size_t WS_END = WS_FLP + 4096;

__device__ __forceinline__ float bflo(u32 w) { return __uint_as_float(w << 16); }
__device__ __forceinline__ float bfhi(u32 w) { return __uint_as_float(w & 0xffff0000u); }
__device__ __forceinline__ u32 pack2(float lo, float hi) { f32x2 v = {lo, hi}; bf16x2 b = __builtin_convertvector(v, bf16x2); return __builtin_bit_cast(u32, b); }
__device__ __forceinline__ float rbf(float x) { return bflo(pack2(x, 0.f)); }
__device__ __forceinline__ float dot2(u32 a, u32 b, float c) { return __builtin_amdgcn_fdot2_f32_bf16(__builtin_bit_cast(bf16x2, a), __builtin_bit_cast(bf16x2, b), c, false); }
__device__ __forceinline__ f32x4 mfma16(u32x4 a, u32x4 b, f32x4 c) { return __builtin_amdgcn_mfma_f32_16x16x32_bf16(__builtin_bit_cast(bf16x8, a), __builtin_bit_cast(bf16x8, b), c, 0, 0, 0); }
__device__ __forceinline__ void dma16(const void* g, LAS void* l) { __builtin_amdgcn_global_load_lds((const u32*)g, (LAS u32*)l, 16, 0, 0); }
#define WAIT_VM(n) asm volatile("s_waitcnt vmcnt(%0)" ::"n"(n) : "memory")
#define WAIT_LGKM0() asm volatile("s_waitcnt lgkmcnt(0)" ::: "memory")
#define BAR() do { asm volatile("" ::: "memory"); __builtin_amdgcn_s_barrier(); asm volatile("" ::: "memory"); } while (0)
#define SB() __builtin_amdgcn_sched_barrier(0)
#define MEMFENCE() asm volatile("" ::: "memory")
#define MAKE_RSRC(p) __builtin_amdgcn_make_buffer_rsrc((void*)(p), 0, 0x7fffffff, 0x00020000)
#define ST16(rs, off, v) __builtin_amdgcn_raw_buffer_store_b128((v), (rs), (int)(off), 0, 16)

template <int CTRL, int RM> __device__ __forceinline__ float dpp_add(float v) { return v + __builtin_bit_cast(float, __builtin_amdgcn_update_dpp(0, __builtin_bit_cast(int, v), CTRL, RM, 0xf, true)); }
__device__ __forceinline__ float wave_sum(float v) {
  v = dpp_add<0x111, 0xf>(v); v = dpp_add<0x112, 0xf>(v); v = dpp_add<0x114, 0xf>(v); v = dpp_add<0x118, 0xf>(v);
  v = dpp_add<0x142, 0xa>(v); v = dpp_add<0x143, 0xc>(v);
  return __builtin_bit_cast(float, __builtin_amdgcn_readlane(__builtin_bit_cast(int, v), 63));
}

#define STAMP(kid, i) do {} while (0)
#define STAMP_DECL do {} while (0)
#define STAMP_FLUSH(kid) do {} while (0)

typedef __attribute__((ext_vector_type(4))) short s16x4;
__device__ __forceinline__ u16x4 tr_read(u32 addr) { u16x4 r; asm volatile("ds_read_b64_tr_b16 %0, %1" : "=v"(r) : "v"(addr) : "memory"); return r; }
#define DS_TR(dst, addr, off) asm volatile("ds_read_b64_tr_b16 %0, %1 offset:%2" : "=v"(dst) : "v"(addr), "n"(off))
#define DS_B64(dst, addr, off) asm volatile("ds_read_b64 %0, %1 offset:%2" : "=v"(dst) : "v"(addr), "n"(off))
#define DS_B128(dst, addr, off) asm volatile("ds_read_b128 %0, %1 offset:%2" : "=v"(dst) : "v"(addr), "n"(off))
#define MFMA_IP(acc, a, b) asm volatile("v_mfma_f32_16x16x32_bf16 %0, %1, %2, %0" : "+v"(acc) : "v"(a), "v"(b))
#define MFMA_Z(acc, a, b) asm volatile("v_mfma_f32_16x16x32_bf16 %0, %1, %2, 0" : "=&v"(acc) : "v"(a), "v"(b))
#define GLD16(dst, voff, sbase, off) asm volatile("global_load_dwordx4 %0, %1, %2 offset:%3" : "=v"(dst) : "v"(voff), "s"(sbase), "n"(off) : "memory")
#define WAIT_LGKM(n) asm volatile("s_waitcnt lgkmcnt(%0)" ::"n"(n) : "memory")

static constexpr int K0_TILES = 32 * 10, K0_STATS = 128, K0_PF = 148;
#define K0_TOUCH 1
static constexpr int K0_RAW = 17 * 1024, K0_LDS = K0_RAW + 64 * 144;
__global__ __launch_bounds__(256) void k0_prep(const u16* __restrict__ x, const u16* __restrict__ k, const u16* __restrict__ v,
                                               const u16* __restrict__ shk, const u16* __restrict__ fk, const u16* __restrict__ wg, const u16* __restrict__ wu,
                                               const u16* __restrict__ wd, const u16* __restrict__ outp, char* __restrict__ ws) {
  extern __shared__ __attribute__((aligned(16))) char shm[];
  LAS char* lds = (LAS char*)shm;
  const int t = threadIdx.x, lane = t & 63, w = __builtin_amdgcn_readfirstlane(t >> 6), b = blockIdx.x;
  STAMP_DECL; STAMP(0, 0);
  const auto rs = MAKE_RSRC(ws);
  if (b < K0_TILES) {
    const int st = b / 10, ct = b % 10, s0 = st * 64, c0 = ct * 64;
#pragma unroll
    for (int i = 0; i < 5; ++i) {
      const int pc = w + 4 * i;
      if (pc < 17) {
        const u16* src = pc < 9 ? k : v;
        int row = (pc < 9 ? s0 - 8 + 8 * pc : s0 + 8 * (pc - 9)) + (lane >> 3);
        row = row < 0 ? 0 : row;
        dma16(src + (size_t)row * D + c0 + (lane & 7) * 8, lds + pc * 1024);
      }
    }
    const int cp = t & 31, rg = t >> 5, ch = c0 + 2 * cp;
    const u32x4 sh = *(const u32x4*)(shk + ch * 4);
    WAIT_VM(0); __syncthreads();
    STAMP(0, 1);
    u32 kk[11], vv[8];
    LAS const char* rk = lds + (8 * rg + 5) * 128 + cp * 4;
#pragma unroll
    for (int i = 0; i < 11; ++i) kk[i] = *(LAS const u32*)(rk + i * 128);
#pragma unroll
    for (int i = 0; i < 8; ++i) vv[i] = *(LAS const u32*)(lds + 9 * 1024 + (8 * rg + i) * 128 + cp * 4);
    if (st == 0 && rg == 0) { kk[0] = kk[1] = kk[2] = 0u; }
    const float a0 = bflo(sh.x), a1 = bfhi(sh.x), a2 = bflo(sh.y), a3 = bfhi(sh.y);
    const float b0 = bflo(sh.z), b1 = bfhi(sh.z), b2 = bflo(sh.w), b3 = bfhi(sh.w);
    float ka[11], kb[11];
#pragma unroll
    for (int i = 0; i < 11; ++i) { ka[i] = bflo(kk[i]); kb[i] = bfhi(kk[i]); }
    float ua[8], ub[8];
#pragma unroll
    for (int i = 0; i < 8; ++i) {
      ua[i] = (ka[i] * a3 + ka[i + 1] * a2 + ka[i + 2] * a1 + ka[i + 3] * a0) * bflo(vv[i]);
      ub[i] = (kb[i] * b3 + kb[i + 1] * b2 + kb[i + 2] * b1 + kb[i + 3] * b0) * bfhi(vv[i]);
    }
    LAS char* L1 = lds + K0_RAW;
    const int o = (7 - rg) * 16;
    *(LAS u32x4*)(L1 + (2 * cp) * 144 + o) = (u32x4){pack2(ua[7], ua[6]), pack2(ua[5], ua[4]), pack2(ua[3], ua[2]), pack2(ua[1], ua[0])};
    *(LAS u32x4*)(L1 + (2 * cp + 1) * 144 + o) = (u32x4){pack2(ub[7], ub[6]), pack2(ub[5], ub[4]), pack2(ub[3], ub[2]), pack2(ub[1], ub[0])};
    __syncthreads();
    u32x4 o1[2];
#pragma unroll
    for (int i = 0; i < 2; ++i) o1[i] = *(LAS const u32x4*)(L1 + ((t >> 3) + 32 * i) * 144 + (t & 7) * 16);
#pragma unroll
    for (int i = 0; i < 2; ++i) ST16(rs, WS_UR1 + ((size_t)(c0 + (t >> 3) + 32 * i) * S + (1984 - s0)) * 2 + (t & 7) * 16, o1[i]);
    STAMP(0, 2);
  } else if (b >= K0_TILES + K0_STATS) {
    const int pf = b - K0_TILES - K0_STATS;
    u32x4 sink[16];
    if (pf < 142) {
      const char* base; size_t bytes; int idx;
      if (pf < 40) { base = (const char*)fk; bytes = (size_t)D * S * 2; idx = pf; }
      else { const int m = (pf - 40) / 34; base = (const char*)(m == 0 ? wg : (m == 1 ? wu : wd)); bytes = (size_t)D * F * 2; idx = (pf - 40) % 34; }
#pragma unroll
      for (int i = 0; i < 16; ++i) { size_t o = (size_t)idx * 65536 + i * 4096 + t * 16; o = o < bytes ? o : bytes - 16; asm volatile("global_load_dwordx4 %0, %1, off" : "=v"(sink[i]) : "v"(base + o) : "memory"); }
      asm volatile("s_waitcnt vmcnt(0)" : "+v"(sink[0]), "+v"(sink[1]), "+v"(sink[2]), "+v"(sink[3]), "+v"(sink[4]), "+v"(sink[5]), "+v"(sink[6]), "+v"(sink[7]),
                                          "+v"(sink[8]), "+v"(sink[9]), "+v"(sink[10]), "+v"(sink[11]), "+v"(sink[12]), "+v"(sink[13]), "+v"(sink[14]), "+v"(sink[15])::"memory");
    } else if (K0_TOUCH) {
      const int i0 = (pf - 142) * 256 + t;
      size_t o = WS_HB + (size_t)i0 * 65536; o = o < WS_END - 16 ? o : WS_HB;
      const int io = i0 < 40 ? i0 : 0;
      asm volatile("global_load_dwordx4 %0, %1, off" : "=v"(sink[0]) : "v"(ws + o) : "memory");
      asm volatile("global_load_dwordx4 %0, %1, off" : "=v"(sink[1]) : "v"((const char*)outp + (size_t)io * 65536) : "memory");
      asm volatile("s_waitcnt vmcnt(0)" : "+v"(sink[0]), "+v"(sink[1])::"memory");
    }
  } else {
    const int rbase = (b - K0_TILES) * 16 + w * 4;
    u32x4 xa[4], xb[4];
#pragma unroll
    for (int r = 0; r < 4; ++r) {
      xa[r] = *(const u32x4*)(x + (size_t)(rbase + r) * D + lane * 8);
      xb[r] = *(const u32x4*)(x + (size_t)(rbase + r) * D + 512 + (lane & 15) * 8);
    }
    const float mk = lane < 16 ? 1.f : 0.f;
    float mean[4], q[4];
#pragma unroll
    for (int r = 0; r < 4; ++r) {
      float s = 0.f, s2 = 0.f;
#pragma unroll
      for (int j = 0; j < 4; ++j) { s += bflo(xa[r][j]) + bfhi(xa[r][j]); s2 += bflo(xb[r][j]) + bfhi(xb[r][j]); }
      mean[r] = wave_sum(s + mk * s2) * (1.0f / D);
    }
#pragma unroll
    for (int r = 0; r < 4; ++r) {
      float s = 0.f, s2 = 0.f;
#pragma unroll
      for (int j = 0; j < 4; ++j) {
        float a = bflo(xa[r][j]) - mean[r], c = bfhi(xa[r][j]) - mean[r]; s += a * a + c * c;
        a = bflo(xb[r][j]) - mean[r]; c = bfhi(xb[r][j]) - mean[r]; s2 += a * a + c * c;
      }
      q[r] = wave_sum(s + mk * s2) * (1.0f / D);
    }
    STAMP(0, 1);
    if (lane < 2) {
      const float m0 = lane ? mean[2] : mean[0], m1 = lane ? mean[3] : mean[1], v0 = lane ? q[2] : q[0], v1 = lane ? q[3] : q[1];
      const float sd0 = rbf(sqrtf(rbf(rbf(v0) + 1e-5f))), sd1 = rbf(sqrtf(rbf(rbf(v1) + 1e-5f)));
      const f32x4 o = {rbf(m0), 1.0f / sd0, rbf(m1), 1.0f / sd1};
      ST16(rs, WS_ST1 + (size_t)(rbase + 2 * lane) * 8, __builtin_bit_cast(u32x4, o));
    }
    STAMP(0, 2);
  }
  if (b == K0_TILES + K0_STATS - 1) { ST16(rs, WS_FLG + t * 16, ((u32x4){0u, 0u, 0u, 0u})); ST16(rs, WS_FLP + t * 16, ((u32x4){0u, 0u, 0u, 0u})); }
  STAMP_FLUSH(0);
}

#define POLL_SLEEP 1
typedef __attribute__((address_space(1))) unsigned gu32;
__device__ __forceinline__ void publish(char* ws, size_t off) {
  WAIT_VM(0); BAR();
  if (threadIdx.x == 0) __hip_atomic_store((gu32*)(ws + off), 1u, __ATOMIC_RELAXED, __HIP_MEMORY_SCOPE_AGENT);
}
__device__ __forceinline__ void await_flags(char* ws, size_t off, int n, int lane) {
  const unsigned long long t0 = __builtin_amdgcn_s_memrealtime();
  for (;;) {
    bool ok = true;
#pragma unroll
    for (int i = 0; i < 4; ++i) {
      const int idx = lane + 64 * i;
      if (64 * i < n) { const unsigned v = __hip_atomic_load((gu32*)(ws + off + (size_t)(idx < n ? idx : 0) * 16), __ATOMIC_RELAXED, __HIP_MEMORY_SCOPE_AGENT); ok &= v == 1u; }
    }
    if (__all(ok)) break;
    if (__builtin_amdgcn_s_memrealtime() - t0 > 400000ull) break;
    __builtin_amdgcn_s_sleep(POLL_SLEEP);
  }
  asm volatile("" ::: "memory");
}

static constexpr int CH_BYTES = 13568, CH_F = 0, CH_U1 = 4224, CH_U0 = 4224 + 4608 + 32;
static constexpr int K1_YL = 4 * CH_BYTES;
static constexpr int K1_YP = 8192 + 64;
static constexpr int K1_CONV = 160, K1_W1 = 54, K1_W2 = 30;

template <bool W1>
__device__ __forceinline__ void retile_job(LAS char* lds, const u16* __restrict__ src, int pitch, int cg, int mat, int ks0, char* ws, int w, int lane) {
  constexpr int NU = W1 ? 10 : 9;
  const auto rs = MAKE_RSRC(ws);
#pragma unroll
  for (int i = 0; i < NU; ++i) {
    const int pc = w + 8 * i, row = 8 * pc + (lane >> 3);
    dma16(src + (size_t)(ks0 * 32 + row) * pitch + (((lane & 7) ^ (row & 7)) * 8), lds + pc * 1024);
  }
  WAIT_VM(0); BAR();
  const int g = lane >> 4, q = (lane & 15) >> 2, p = lane & 3, fb = w & 3;
  const u32 l0 = (u32)(size_t)lds;
  u16x4 lo[NU], hi[NU];
#pragma unroll
  for (int i = 0; i < NU; ++i) {
    const int ks = (w >> 2) + 2 * i, row0 = 32 * ks + 8 * g + q, row1 = row0 + 4, chk = 2 * fb + (p >> 1);
    lo[i] = tr_read(l0 + row0 * 128 + ((chk ^ (row0 & 7)) * 16) + 8 * (p & 1));
    hi[i] = tr_read(l0 + row1 * 128 + ((chk ^ (row1 & 7)) * 16) + 8 * (p & 1));
  }
#pragma unroll
  for (int i = 0; i < NU; ++i) asm volatile("s_waitcnt lgkmcnt(0)" : "+v"(lo[i]), "+v"(hi[i])::"memory");
  float m = 0.f;
  if (W1) {
    float cs = 0.f;
#pragma unroll
    for (int i = 0; i < NU; ++i) {
      const u32x2 l2 = __builtin_bit_cast(u32x2, lo[i]), h2 = __builtin_bit_cast(u32x2, hi[i]);
      cs = dot2(l2.x, 0x3f803f80u, dot2(l2.y, 0x3f803f80u, dot2(h2.x, 0x3f803f80u, dot2(h2.y, 0x3f803f80u, cs))));
    }
    cs += __shfl_xor(cs, 16); cs += __shfl_xor(cs, 32);
    LAS float* red = (LAS float*)(lds + 80 * 1024);
    if (lane < 16) red[w * 16 + lane] = cs;
    WAIT_LGKM0(); BAR();
    m = (red[fb * 16 + (lane & 15)] + red[(fb + 4) * 16 + (lane & 15)]) * (1.0f / D);
  }
#pragma unroll
  for (int i = 0; i < NU; ++i) {
    const int ks = (w >> 2) + 2 * i;
    const u32x2 l2 = __builtin_bit_cast(u32x2, lo[i]), h2 = __builtin_bit_cast(u32x2, hi[i]);
    if (W1) {
      const u32x4 o = {pack2(bflo(l2.x) - m, bfhi(l2.x) - m), pack2(bflo(l2.y) - m, bfhi(l2.y) - m), pack2(bflo(h2.x) - m, bfhi(h2.x) - m), pack2(bflo(h2.y) - m, bfhi(h2.y) - m)};
      const int blk = cg * 4 + fb;
      ST16(rs, WS_W1T + ((size_t)(blk * NKS1 + ks) * 2 + mat) * 1024 + lane * 16, o);
    } else {
      const int df = cg * 4 + fb, dt = df / 5, j = df % 5;
      ST16(rs, WS_W2T + ((size_t)(dt * NKS2P + ks0 + ks) * 5 + j) * 1024 + lane * 16, ((u32x4){l2.x, l2.y, h2.x, h2.y}));
    }
  }
  if (!W1 && ks0 == 36 && cg < 8) {
#pragma unroll
    for (int i = 0; i < 2; ++i) { const int pc = w + 8 * i; if (pc < 10) ST16(rs, WS_W2T + ((size_t)(cg * NKS2P + NKS2) * 5 + pc) * 1024 + lane * 16, ((u32x4){0u, 0u, 0u, 0u})); }
  }
  if (W1 && cg == 26 && mat == 0) {
#pragma unroll
    for (int i = 0; i < 3; ++i) { const int ks = w + 8 * i; if (ks < NKS1) ST16(rs, WS_W1T + ((size_t)(108 * NKS1 + ks) * 2) * 1024 + lane * 16, ((u32x4){0x3f803f80u, 0x3f803f80u, 0x3f803f80u, 0x3f803f80u})); }
  }
}

#define SARG
#define SPASS

__device__ __forceinline__ void conv_phase(LAS char* lds, const u16* __restrict__ x, const u16* __restrict__ fk, const u16* __restrict__ sD, char* __restrict__ ws, int b, int t, int lane, int w SARG) {
  const auto rs = MAKE_RSRC(ws);
  const int cgp = (b & 7) * 20 + (b >> 3);
  const int c = w & 3, hf = w >> 2, d = cgp * 4 + c;
  LAS char* cb = lds + c * CH_BYTES;
  {
    const char* gF = (const char*)fk + (size_t)d * S * 2 + lane * 16;
    const char* g1 = ws + WS_UR1 + (size_t)d * S * 2 + lane * 16;
#pragma unroll
    for (int i = 0; i < 2; ++i) {
      const int pc = hf * 2 + i;
      dma16(g1 + pc * 1024, cb + CH_U1 + pc * 1024);
      dma16(gF + pc * 1024, cb + CH_F + 64 + pc * 1024);
    }
    if (hf == 0) { if (lane < 32) *(LAS u32x4*)(cb + CH_U1 + 4096 + lane * 16) = (u32x4){0, 0, 0, 0}; if (lane < 4) *(LAS u32x4*)(cb + CH_F + lane * 16) = (u32x4){0, 0, 0, 0}; }
    else { if (lane < 32) *(LAS u32x4*)(cb + CH_U0 + 4096 + lane * 16) = (u32x4){0, 0, 0, 0}; }
  }
  const u32 dw = sD[d];
  WAIT_VM(0); WAIT_LGKM0(); BAR();
#pragma unroll
  for (int i = 0; i < 2; ++i) {
    const int p0 = 1024 * hf + 512 * i + 8 * lane;
    const u32x4 in = *(LAS const u32x4*)(cb + CH_U1 + p0 * 2); const u32 nx = *(LAS const u32*)(cb + CH_U1 + p0 * 2 + 16);
    *(LAS u32x4*)(cb + CH_U0 + p0 * 2) = (u32x4){__builtin_amdgcn_alignbit(in.y, in.x, 16), __builtin_amdgcn_alignbit(in.z, in.y, 16), __builtin_amdgcn_alignbit(in.w, in.z, 16), __builtin_amdgcn_alignbit(nx, in.w, 16)};
  }
  WAIT_LGKM0(); BAR();
  STAMP(1, 1);
  u32x2 xr[4]; u32x4 stv[2];
  const char* gx = (const char*)x + (size_t)(2 * t) * D * 2 + cgp * 8;
#define C_LDX(i) do { \
      if ((i) == 0) asm volatile("global_load_dwordx2 %0, %1, off" : "=v"(xr[0]) : "v"(gx) : "memory"); \
      if ((i) == 1) asm volatile("global_load_dwordx2 %0, %1, off offset:1280" : "=v"(xr[1]) : "v"(gx) : "memory"); \
      if ((i) == 2) asm volatile("global_load_dwordx2 %0, %1, off" : "=v"(xr[2]) : "v"(gx + (size_t)1024 * D * 2) : "memory"); \
      if ((i) == 3) asm volatile("global_load_dwordx2 %0, %1, off offset:1280" : "=v"(xr[3]) : "v"(gx + (size_t)1024 * D * 2) : "memory"); \
      if ((i) == 4) GLD16(stv[0], t * 16, ws + WS_ST1, 0); \
      if ((i) == 5) GLD16(stv[1], t * 16, ws + WS_ST1 + 8192, 0); } while (0)
#define C_XWAIT() asm volatile("s_waitcnt vmcnt(0) lgkmcnt(0)" : "+v"(xr[0]), "+v"(xr[1]), "+v"(xr[2]), "+v"(xr[3]), "+v"(stv[0]), "+v"(stv[1])::"memory")
  if (hf == 0) {
    const int r = lane & 15, g = lane >> 4;
    f32x4 acc[8];
    const int a = r >> 1, e = r & 1;
    const u32 ub = (u32)(size_t)(cb + (e ? CH_U1 : CH_U0) + 448 - 64 * a + 16 * g);
    u32 fbp[4];
#pragma unroll
    for (int i = 0; i < 4; ++i) fbp[i] = (u32)(size_t)(cb + CH_F + 16 * g + 4 * r + 4) + 1024 * i;
    u32x4 U[2][8]; u32x2 Fr[2][8][2];
#define C_RDU(buf, i, cc) DS_B128(U[buf][i], ub, 512 * (7 - (i)) + 64 * (cc))
#define C_RDF(buf, i, cc, hh) asm volatile("ds_read2_b32 %0, %1 offset0:%2 offset1:%3" : "=v"(Fr[buf][i][hh]) : "v"(fbp[(i) >> 1]), "n"(128 * ((i) & 1) + 16 * (cc) + 2 * (hh)), "n"(128 * ((i) & 1) + 16 * (cc) + 2 * (hh) + 1))
#define C_F(buf, i) ((u32x4){Fr[buf][i][0].x, Fr[buf][i][0].y, Fr[buf][i][1].x, Fr[buf][i][1].y})
#define C_WAIT(buf) do { \
      asm volatile("s_waitcnt lgkmcnt(0)" : "+v"(U[buf][0]), "+v"(U[buf][1]), "+v"(U[buf][2]), "+v"(U[buf][3]), "+v"(U[buf][4]), "+v"(U[buf][5]), "+v"(U[buf][6]), "+v"(U[buf][7])::"memory"); \
      asm volatile("" : "+v"(Fr[buf][0][0]), "+v"(Fr[buf][0][1]), "+v"(Fr[buf][1][0]), "+v"(Fr[buf][1][1]), "+v"(Fr[buf][2][0]), "+v"(Fr[buf][2][1]), "+v"(Fr[buf][3][0]), "+v"(Fr[buf][3][1]), \
                        "+v"(Fr[buf][4][0]), "+v"(Fr[buf][4][1]), "+v"(Fr[buf][5][0]), "+v"(Fr[buf][5][1]), "+v"(Fr[buf][6][0]), "+v"(Fr[buf][6][1]), "+v"(Fr[buf][7][0]), "+v"(Fr[buf][7][1])); } while (0)
#pragma unroll
    for (int i = 0; i < 8; ++i) { C_RDF(0, i, 0, 0); C_RDF(0, i, 0, 1); C_RDU(0, i, 0); }
    C_WAIT(0);
#pragma unroll
    for (int cc = 0; cc < 8; ++cc) {
      const int cur = cc & 1, nxt = cur ^ 1;
      int n = 0;
#pragma unroll
      for (int c1 = 0; c1 < 8; ++c1)
#pragma unroll
        for (int i = c1; i < 8; ++i) {
          if (cc == 0 && c1 == 0) MFMA_Z(acc[i], U[cur][i - c1], C_F(cur, c1)); else MFMA_IP(acc[i], U[cur][i - c1], C_F(cur, c1));
          if (cc < 7 && n < 24) { const int j = n / 3, kk = n % 3; if (kk == 0) C_RDU(nxt, j, cc + 1); else C_RDF(nxt, j, cc + 1, kk - 1); }
          if (cc < 6 && n == 28) C_LDX(cc);
          ++n;
        }
      if (cc < 7) C_WAIT(nxt);
    }
    asm volatile("s_nop 15\n\ts_nop 15" ::: "memory");
    STAMP(1, 2);
    LAS char* yl = lds + K1_YL + c * K1_YP;
    const float Dd = bflo(dw);
#pragma unroll
    for (int j = 0; j < 8; ++j)
#pragma unroll
      for (int hh = 0; hh < 2; ++hh) {
        const int s = 256 * j + 32 * (2 * g + hh) + 2 * r;
        const u32 uw = *(LAS const u32*)(cb + CH_U1 + (2046 - s) * 2);
        *(LAS f32x2*)(yl + s * 4) = (f32x2){acc[j][2 * hh] + bfhi(uw) * Dd, acc[j][2 * hh + 1] + bflo(uw) * Dd};
      }
    C_XWAIT();
  } else {
    C_LDX(0); C_LDX(1); C_LDX(2); C_LDX(3); C_LDX(4); C_LDX(5);
    C_XWAIT();
  }
  BAR();
  STAMP(1, 3);
#pragma unroll
  for (int i = 0; i < 2; ++i) {
    const int s = 2 * t + 1024 * i;
    f32x2 y2[4];
#pragma unroll
    for (int cc = 0; cc < 4; ++cc) y2[cc] = *(LAS const f32x2*)(lds + K1_YL + cc * K1_YP + s * 4);
    const f32x4 st = __builtin_bit_cast(f32x4, stv[i]);
    const u32x2 x0 = xr[2 * i], x1 = xr[2 * i + 1];
    const float xa[4] = {bflo(x0.x), bfhi(x0.x), bflo(x0.y), bfhi(x0.y)}, xb[4] = {bflo(x1.x), bfhi(x1.x), bflo(x1.y), bfhi(x1.y)};
    float ha[4], hb[4];
#pragma unroll
    for (int cc = 0; cc < 4; ++cc) { ha[cc] = xa[cc] + (xa[cc] - st[0]) * st[1] * y2[cc][0]; hb[cc] = xb[cc] + (xb[cc] - st[2]) * st[3] * y2[cc][1]; }
    ST16(rs, WS_HB + ((size_t)cgp * S + s) * 8, ((u32x4){pack2(ha[0], ha[1]), pack2(ha[2], ha[3]), pack2(hb[0], hb[1]), pack2(hb[2], hb[3])}));
  }
  STAMP(1, 4);
}

static constexpr int G1_NS = 8, G1_SLOT = 8192, G1_WR = 4, G1_RING = 96 * 1024;
static constexpr int G2_STAGE = 15 * 1024, G2_NS = 5;
static constexpr int KF_CNT = 92 * 1024;
__host__ __device__ constexpr int g2_slot(int kh, int s) { return s < 3 ? (kh * 3 + s) * G2_STAGE : 96 * 1024 + (kh * 2 + s - 3) * G2_STAGE; }
#define DMA_SC1(m0v, voffv, sbase, off) asm volatile("s_mov_b32 m0, %0\n\ts_nop 0\n\tglobal_load_lds_dwordx4 %1, %2 offset:%3 sc1" :: "s"(m0v), "v"(voffv), "s"(sbase), "n"(off) : "memory")
#define DMA_PL(m0v, voffv, sbase, off) asm volatile("s_mov_b32 m0, %0\n\ts_nop 0\n\tglobal_load_lds_dwordx4 %1, %2 offset:%3" :: "s"(m0v), "v"(voffv), "s"(sbase), "n"(off) : "memory")
#define GLD16C(dst, voff, sbase, off) asm volatile("global_load_dwordx4 %0, %1, %2 offset:%3 sc1" : "=v"(dst) : "v"(voff), "s"(sbase), "n"(off) : "memory")

__device__ __forceinline__ void gemm2_prefetch(LAS char* lds, char* __restrict__ ws, int dt, int lane, int first, int last, int step) {
  const u32 lds0 = (u32)(size_t)lds, voff = lane * 16;
  const char* wb = ws + WS_W2T + (size_t)dt * NKS2P * 5 * 1024;
  for (int pc = first; pc < last; pc += step) {
    int kh, o, dst;
    if (pc < 90) { kh = pc >= 45; o = (pc - 45 * kh) * 1024; dst = kh * 3 * G2_STAGE + o; }
    else { const int q = pc - 90; kh = q >= 30; o = 3 * G2_STAGE + (q - 30 * kh) * 1024; dst = 96 * 1024 + kh * 2 * G2_STAGE + (q - 30 * kh) * 1024; }
    const u32 m0v = __builtin_amdgcn_readfirstlane(lds0 + dst);
    const char* sb = wb + kh * 27 * 5120 + o;
    DMA_SC1(m0v, voff, sb, 0);
  }
}

__device__ __forceinline__ void gemm1_phase(LAS char* lds, char* __restrict__ ws, int rt, int ct, int dt2, int lane, int w SARG) {
  const u32 lds0 = (u32)(size_t)lds + G1_RING;
  const int s0 = rt * 128;
  const u32 voff = lane * 16;
  const auto rs = MAKE_RSRC(ws);
  if (w >= 4) {
    const int j = w - 4;
    const u32 hv = voff ^ ((j & 1) * 128);
    const char* hsb = ws + WS_HB + ((size_t)(2 * j) * S + s0) * 8;
    const u32 ldsw = lds0 + 2 * j * 1024;
#define G1_SLAB(ks) do { DMA_SC1(ldsw + ((ks) % G1_NS) * G1_SLOT, hv, hsb + (size_t)(ks) * 8 * S * 8, 0); DMA_SC1(ldsw + ((ks) % G1_NS) * G1_SLOT + 1024, hv, hsb + (size_t)(ks) * 8 * S * 8 + S * 8, 0); } while (0)
    G1_SLAB(0); G1_SLAB(1); G1_SLAB(2); G1_SLAB(3); G1_SLAB(4);
    WAIT_VM(8); BAR();
    G1_SLAB(5);
    WAIT_VM(6); BAR();
    G1_SLAB(6); G1_SLAB(7);
    WAIT_VM(6); BAR();
    G1_SLAB(8); G1_SLAB(9);
    WAIT_VM(6); BAR();
#define G1_HSTEP(m) do { gemm2_prefetch(lds, ws, dt2, lane, j + 16 * ((m) - 2), 16 * ((m) - 2) + 16 < 90 ? 16 * ((m) - 2) + 16 : 90, 4); \
      if (2 * (m) + 6 < NKS1) { G1_SLAB(2 * (m) + 6); G1_SLAB(2 * (m) + 7); WAIT_VM(6); } else if (2 * (m) + 4 < NKS1) WAIT_VM(2 * (NKS1 - 1 - (2 * (m) + 4))); else WAIT_VM(0); BAR(); } while (0)
    G1_HSTEP(2); G1_HSTEP(3); G1_HSTEP(4); G1_HSTEP(5); G1_HSTEP(6); G1_HSTEP(7); G1_HSTEP(8);
    BAR();
  } else {
    const int blk0 = ct * 8 + 2 * w;
    const char* wsA = ws + WS_W1T + (size_t)(blk0 > 108 ? 108 : blk0) * NKS1 * 2048;
    const char* wsB = ws + WS_W1T + (size_t)(blk0 + 1 > 108 ? 108 : blk0 + 1) * NKS1 * 2048;
    const int g = lane >> 4;
    const u32 hbase = lds0 + g * 2048 + (lane & 15) * 8;
    const u32 hoffE = hbase + (g & 1) * 128, hoffO = hbase + (1 - (g & 1)) * 128 - 128;
    f32x4 acc[8][4];
    const int stat = __builtin_amdgcn_readfirstlane(blk0 == 108 ? 1 : 0);
#define G1_LDW(ks, i) do { if ((i) == 0) GLD16C(wr[(ks) % G1_WR][0], voff, wsA + (ks) * 2048, 0); if ((i) == 1) GLD16C(wr[(ks) % G1_WR][1], voff, wsA + (ks) * 2048, 1024); \
      if ((i) == 2) GLD16C(wr[(ks) % G1_WR][2], voff, wsB + (ks) * 2048, 0); if ((i) == 3) GLD16C(wr[(ks) % G1_WR][3], voff, wsB + (ks) * 2048, 1024); } while (0)
#define G1_RD(sf, hh, ks) DS_B64(hf[sf][hh], ((sf) & 1) ? hoffO : hoffE, ((ks) % G1_NS) * G1_SLOT + (hh) * 1024 + (sf) * 128)
#define G1_H(sf) ((u32x4){hf[sf][0].x, hf[sf][0].y, hf[sf][1].x, hf[sf][1].y})
#define G1_MM(ks, sf, i, STAT) do { \
      if (STAT) { if ((i) == 0) { if ((ks) == 0) MFMA_Z(acc[sf][0], wr[0][0], G1_H(sf)); else MFMA_IP(acc[sf][0], wr[(ks) % G1_WR][0], G1_H(sf)); } \
                  if ((i) == 2) { if ((ks) == 0) MFMA_Z(acc[sf][2], G1_H(sf), G1_H(sf)); else MFMA_IP(acc[sf][2], G1_H(sf), G1_H(sf)); } } \
      else { if ((ks) == 0) MFMA_Z(acc[sf][i], wr[0][i], G1_H(sf)); else MFMA_IP(acc[sf][i], wr[(ks) % G1_WR][i], G1_H(sf)); } } while (0)
#define G1_BODY(STAT) { \
      u32x4 wr[G1_WR][4]; u32x2 hf[8][2]; \
      _Pragma("unroll") for (int ks = 0; ks < 3; ++ks) { G1_LDW(ks, 0); G1_LDW(ks, 1); G1_LDW(ks, 2); G1_LDW(ks, 3); } \
      BAR();                                                   \
      STAMP(1, 7); \
      _Pragma("unroll") for (int sf = 0; sf < 7; ++sf) { G1_RD(sf, 0, 0); G1_RD(sf, 1, 0); } \
      _Pragma("unroll") for (int ks = 0; ks < NKS1; ++ks) { \
        if ((ks & 1) == 0) BAR(); \
          \
        if (ks + 2 < NKS1) WAIT_VM(8); else if (ks + 1 < NKS1) WAIT_VM(4); else WAIT_VM(0); \
        asm volatile("" : "+v"(wr[ks % G1_WR][0]), "+v"(wr[ks % G1_WR][1]), "+v"(wr[ks % G1_WR][2]), "+v"(wr[ks % G1_WR][3])); \
        _Pragma("unroll") for (int sf = 0; sf < 8; ++sf) { \
            \
          if (ks + 1 < NKS1 || sf == 0) asm volatile("s_waitcnt lgkmcnt(12)" : "+v"(hf[sf][0]), "+v"(hf[sf][1])::"memory"); \
          else if (sf < 7) asm volatile("s_waitcnt lgkmcnt(%2)" : "+v"(hf[sf][0]), "+v"(hf[sf][1]) : "n"(14 - 2 * sf) : "memory"); \
          else asm volatile("s_waitcnt lgkmcnt(0)" : "+v"(hf[sf][0]), "+v"(hf[sf][1])::"memory"); \
          G1_MM(ks, sf, 0, STAT); \
          if (sf == 0) G1_RD(7, 0, ks); else if (ks + 1 < NKS1) G1_RD(sf - 1, 0, ks + 1); \
          G1_MM(ks, sf, 1, STAT); \
          if (sf == 0) G1_RD(7, 1, ks); else if (ks + 1 < NKS1) G1_RD(sf - 1, 1, ks + 1); \
          G1_MM(ks, sf, 2, STAT); \
          if ((sf & 1) && ks + 3 < NKS1) G1_LDW(ks + 3, sf >> 1); \
          G1_MM(ks, sf, 3, STAT); \
        } } }
    if (stat) G1_BODY(true) else G1_BODY(false)
    BAR();
    asm volatile("s_nop 15\n\ts_nop 15" ::: "memory");
    STAMP(1, 8);
    if (stat) {
      const int sx = lane & 15;
#pragma unroll
      for (int sp = 0; sp < 2; ++sp) {
        float r2[4];
#pragma unroll
        for (int q = 0; q < 4; ++q) {
          const int sf = 4 * sp + q;
          const float dg = (sx & 2) ? ((sx & 1) ? acc[sf][2][3] : acc[sf][2][2]) : ((sx & 1) ? acc[sf][2][1] : acc[sf][2][0]);
          const float ssq = __shfl(dg, (sx >> 2) * 16 + sx);
          const float mean = acc[sf][0][0] * (1.0f / D);
          const float var = fmaxf(ssq * (1.0f / D) - mean * mean, 0.f);
          const float r = 1.0f / rbf(sqrtf(rbf(rbf(var) + 1e-5f)));
          r2[q] = r * r;
        }
        const float mine = g == 0 ? r2[0] : (g == 1 ? r2[1] : (g == 2 ? r2[2] : r2[3]));
        const int b4 = lane & ~3;
        const f32x4 o = {__shfl(mine, b4), __shfl(mine, b4 + 1), __shfl(mine, b4 + 2), __shfl(mine, b4 + 3)};
        if ((lane & 3) == 0) ST16(rs, WS_ST2 + (size_t)(s0 + 64 * sp + lane) * 4, __builtin_bit_cast(u32x4, o));
      }
    } else {
#pragma unroll
      for (int bi = 0; bi < 2; ++bi) {
        const int blk = blk0 + bi;
        if (blk < 108) {
          const u32 obase = (u32)(WS_HF + ((size_t)(rt * 8 + (lane >> 5)) * NKS2 + (blk >> 1)) * 1024) + (blk & 1) * 512 + (lane & 31) * 16;
#pragma unroll
          for (int pr = 0; pr < 4; ++pr) {
            const f32x4 z4 = {0.f, 0.f, 0.f, 0.f};
            const f32x4 hE = __builtin_elementwise_max(acc[2 * pr][2 * bi], z4) * acc[2 * pr][2 * bi + 1], hO = __builtin_elementwise_max(acc[2 * pr + 1][2 * bi], z4) * acc[2 * pr + 1][2 * bi + 1];
            u32 e0 = pack2(hE[0], hE[1]), e1 = pack2(hE[2], hE[3]), o0 = pack2(hO[0], hO[1]), o1 = pack2(hO[2], hO[3]);
            auto r0 = __builtin_amdgcn_permlane32_swap(e0, o0, false, false); auto r0b = __builtin_amdgcn_permlane16_swap(r0[0], r0[1], false, false);
            auto r1 = __builtin_amdgcn_permlane32_swap(e1, o1, false, false); auto r1b = __builtin_amdgcn_permlane16_swap(r1[0], r1[1], false, false);
            ST16(rs, obase + pr * 2 * NKS2 * 1024, ((u32x4){r0b[0], r1b[0], r0b[1], r1b[1]}));
          }
        }
      }
    }
  }
}

__device__ __forceinline__ void gemm2_phase(LAS char* lds, char* __restrict__ ws, u16* __restrict__ out, int rt, int dt, int lane, int w SARG) {
  const int sf = w & 3, kh = w >> 2, g = lane >> 4;
  const u32 voff = lane * 16;
  const char* hsrc = ws + WS_HF + ((size_t)(rt * 4 + sf) * NKS2 + kh * 27) * 1024;
  u32x4 hq[G2_NS][3];
  const u32 rlo = (u32)(size_t)lds + kh * 3 * G2_STAGE, rhi = (u32)(size_t)lds + 96 * 1024 + kh * 2 * G2_STAGE - 3 * G2_STAGE;
#define G2_RB(s) (((s) % G2_NS) < 3 ? rlo : rhi)
  const char* wsb = ws + WS_W2T + ((size_t)dt * NKS2P + kh * 27) * 5 * 1024 + sf * 3840;
  const unsigned long long m48 = 0x0000ffffffffffffull;
  const int srow = rt * 64 + sf * 16 + (lane & 15);
  float r2; u32x2 res[3];
  asm volatile("global_load_dword %0, %1, off sc1" : "=v"(r2) : "v"(ws + WS_ST2 + (size_t)srow * 4) : "memory");
#pragma unroll
  for (int j = 0; j < 3; ++j) { const int df = kh ? (j < 2 ? 3 + j : 3) : j; asm volatile("global_load_dwordx2 %0, %1, off sc1" : "=v"(res[j]) : "v"(ws + WS_HB + ((size_t)(dt * 20 + df * 4 + g) * S + srow) * 8) : "memory"); }
#define G2_LH(st, i) GLD16C(hq[(st) % G2_NS][i], voff, hsrc + (st) * 3072, (i) * 1024)
#define G2_LW(st, pc) do { \
    if ((pc) < 3) DMA_SC1(G2_RB(st) + sf * 3840 + ((st) % G2_NS) * G2_STAGE, voff, wsb + (st) * G2_STAGE, (pc) * 1024);     \
    else asm volatile("s_mov_b32 m0, %0\n\ts_mov_b64 exec, %4\n\tglobal_load_lds_dwordx4 %1, %2 offset:%3 sc1\n\ts_mov_b64 exec, -1" :: "s"(G2_RB(st) + sf * 3840 + ((st) % G2_NS) * G2_STAGE), "v"(voff), "s"(wsb + (st) * G2_STAGE), "n"(3072), "s"(m48) : "memory"); } while (0)
#define G2_X(x) do { constexpr int x_ = (x); \
    if (x_ < 6) G2_LH(3 + x_ / 3, x_ % 3); \
    else if (x_ < 34) { constexpr int y_ = x_ >= 6 ? x_ - 6 : 0, st_ = 5 + y_ / 7, pc_ = y_ % 7; if (pc_ < 4) G2_LW(st_, pc_); else G2_LH(st_, pc_ >= 4 ? pc_ - 4 : 0); } } while (0)
  G2_LH(0, 0); G2_LH(0, 1); G2_LH(0, 2); G2_LH(1, 0); G2_LH(1, 1); G2_LH(1, 2); G2_LH(2, 0); G2_LH(2, 1); G2_LH(2, 2);
  f32x4 acc[5];
  const u32 rblo = rlo + lane * 16, rbhi = rhi + lane * 16;
  u32x4 wf[2][5];
#define G2_OFF(n) ((((n) / 3) % G2_NS) * G2_STAGE + ((n) % 3) * 5120)
#define G2_READ(n) do { const u32 a_ = ((((n) / 3) % G2_NS) < 3 ? rblo : rbhi) + (G2_OFF(n) & ~0x7fff); \
    DS_B128(wf[(n) & 1][0], a_, G2_OFF(n) & 0x7fff); DS_B128(wf[(n) & 1][1], a_, (G2_OFF(n) & 0x7fff) + 1024); DS_B128(wf[(n) & 1][2], a_, (G2_OFF(n) & 0x7fff) + 2048); \
    DS_B128(wf[(n) & 1][3], a_, (G2_OFF(n) & 0x7fff) + 3072); DS_B128(wf[(n) & 1][4], a_, (G2_OFF(n) & 0x7fff) + 4096); } while (0)
  G2_READ(0);
  WAIT_VM(6);
  asm volatile("" : "+v"(r2), "+v"(res[0]), "+v"(res[1]), "+v"(res[2]));
  STAMP(1, 11);
#define G2_NEED(s) ((s) <= 2 ? 3 * ((s) + 1) : ((s) <= 4 ? 9 + 3 * ((s) - 2) : 15 + 7 * ((s) - 4)))
#define G2_MM(n, j) do { if ((n) == 0) MFMA_Z(acc[j], wf[0][j], hq[0][0]); else MFMA_IP(acc[j], wf[(n) & 1][j], hq[((n) / 3) % G2_NS][(n) % 3]); } while (0)
#define G2_STEP(n) do { constexpr int st = (n) / 3, i = (n) % 3; \
    if (i == 2 && st + 1 < 9) { \
        \
      constexpr int issued = 9 + (2 * (n) < 34 ? 2 * (n) : 34); \
      WAIT_VM(issued - G2_NEED(st + 1)); \
      WAIT_LGKM(0); \
      BAR(); \
    } \
    if (i == 0) asm volatile("" : "+v"(hq[st % G2_NS][0]), "+v"(hq[st % G2_NS][1]), "+v"(hq[st % G2_NS][2])); \
    asm volatile("s_waitcnt lgkmcnt(0)" : "+v"(wf[(n) & 1][0]), "+v"(wf[(n) & 1][1]), "+v"(wf[(n) & 1][2]), "+v"(wf[(n) & 1][3]), "+v"(wf[(n) & 1][4])::"memory"); \
    if ((n) + 1 < 27) G2_READ(((n) + 1 < 27 ? (n) + 1 : 0)); \
    G2_MM(n, 0); G2_X(2 * (n)); \
    G2_MM(n, 1); G2_MM(n, 2); G2_X(2 * (n) + 1); \
    G2_MM(n, 3); G2_MM(n, 4); } while (0)
  G2_STEP(0); G2_STEP(1); G2_STEP(2); G2_STEP(3); G2_STEP(4); G2_STEP(5); G2_STEP(6); G2_STEP(7); G2_STEP(8);
  G2_STEP(9); G2_STEP(10); G2_STEP(11); G2_STEP(12); G2_STEP(13); G2_STEP(14); G2_STEP(15); G2_STEP(16); G2_STEP(17);
  G2_STEP(18); G2_STEP(19); G2_STEP(20); G2_STEP(21); G2_STEP(22); G2_STEP(23); G2_STEP(24); G2_STEP(25); G2_STEP(26);
  asm volatile("s_nop 15\n\ts_nop 15" ::: "memory");
  STAMP(1, 12);
  BAR();
  LAS f32x4* xch = (LAS f32x4*)lds;
#pragma unroll
  for (int j = 0; j < 3; ++j) { if (kh == 0) { if (j < 2) xch[(w * 3 + j) * 64 + lane] = acc[3 + j]; } else xch[(w * 3 + j) * 64 + lane] = acc[j]; }
  WAIT_LGKM0(); BAR();
  u32 pk[3][2];
#pragma unroll
  for (int j = 0; j < 3; ++j) {
    const f32x4 o = xch[((w ^ 4) * 3 + (kh && j == 2 ? 0 : j)) * 64 + lane];
    const f32x4 a = kh ? acc[3 + (j < 2 ? j : 0)] : acc[j];
    const u32x2 rr = res[j];
    pk[j][0] = pack2(bflo(rr.x) + (a[0] + o[0]) * r2, bfhi(rr.x) + (a[1] + o[1]) * r2);
    pk[j][1] = pack2(bflo(rr.y) + (a[2] + o[2]) * r2, bfhi(rr.y) + (a[3] + o[3]) * r2);
  }
  const auto ro = MAKE_RSRC(out);
  {
    auto r0 = __builtin_amdgcn_permlane32_swap(pk[0][0], pk[1][0], false, false); auto r0b = __builtin_amdgcn_permlane16_swap(r0[0], r0[1], false, false);
    auto r1 = __builtin_amdgcn_permlane32_swap(pk[0][1], pk[1][1], false, false); auto r1b = __builtin_amdgcn_permlane16_swap(r1[0], r1[1], false, false);
    const int col = dt * 80 + (kh ? 48 : 0) + (lane >> 5) * 16 + ((lane >> 4) & 1) * 8;
    ST16(ro, ((size_t)srow * D + col) * 2, ((u32x4){r0b[0], r1b[0], r0b[1], r1b[1]}));
  }
  if (kh == 0) {
    auto r0 = __builtin_amdgcn_permlane32_swap(pk[2][0], pk[2][0], false, false); auto r0b = __builtin_amdgcn_permlane16_swap(r0[0], r0[1], false, false);
    auto r1 = __builtin_amdgcn_permlane32_swap(pk[2][1], pk[2][1], false, false); auto r1b = __builtin_amdgcn_permlane16_swap(r1[0], r1[1], false, false);
    const int col = dt * 80 + 32 + ((lane >> 4) & 1) * 8;
    if (lane < 32) ST16(ro, ((size_t)srow * D + col) * 2, ((u32x4){r0b[0], r1b[0], r0b[1], r1b[1]}));
  }
}

static constexpr int KF_LDS = 160 * 1024;
__global__ __launch_bounds__(512, 2) void kf_fused(const u16* __restrict__ x, const u16* __restrict__ fk, const u16* __restrict__ sD, const u16* __restrict__ wg,
                                                   const u16* __restrict__ wu, const u16* __restrict__ wd, char* __restrict__ ws, u16* __restrict__ out) {
  extern __shared__ __attribute__((aligned(16))) char shm[];
  LAS char* lds = (LAS char*)shm;
  const int t = threadIdx.x, lane = t & 63, w = __builtin_amdgcn_readfirstlane(t >> 6), b = blockIdx.x;
  STAMP_DECL; STAMP(1, 0);
  if (b < K1_CONV) conv_phase(lds, x, fk, sD, ws, b, t, lane, w SPASS);
  else {
    { const unsigned long long t0 = __builtin_amdgcn_s_memrealtime(); while (__builtin_amdgcn_s_memrealtime() - t0 < K1_DELAY) __builtin_amdgcn_s_sleep(4); }
    const int jb = b - K1_CONV;
    if (jb < K1_W1) retile_job<true>(lds, ((jb & 1) ? wu : wg) + (jb >> 1) * 64, F, jb >> 1, jb & 1, 0, ws, w, lane);
    else if (jb < K1_W1 + K1_W2) { const int j2 = jb - K1_W1; retile_job<false>(lds, wd + (j2 / 3) * 64, D, j2 / 3, 0, (j2 % 3) * 18, ws, w, lane); }
  }
  publish(ws, WS_FLG + (size_t)b * 16);
  STAMP(1, 5);
  const int xg = b & 7, ix = b >> 3;
  const int p = (xg & 3) * 4 + (ix & 3), j = (xg >> 2) * 8 + (ix >> 2);
  if (t == 0) *(LAS unsigned*)(lds + KF_CNT) = 0u;
  if (w == 7) await_flags(ws, WS_FLG, K1_CONV + K1_W1 + K1_W2, lane);
  WAIT_LGKM0(); BAR();
  STAMP(1, 6);
  if (j < 14) {
    gemm1_phase(lds, ws, p, j, j >> 1, lane, w SPASS);
    if (w >= 4) { gemm2_prefetch(lds, ws, j >> 1, lane, 90 + w - 4, 150, 4); WAIT_VM(0); }
    else {
      WAIT_VM(0);
      unsigned old = 0;
      if (lane == 0) old = __hip_atomic_fetch_add((LAS unsigned*)(lds + KF_CNT), 1u, __ATOMIC_RELAXED, __HIP_MEMORY_SCOPE_WORKGROUP);
      if (lane == 0 && old == 3u) __hip_atomic_store((gu32*)(ws + WS_FLP + (size_t)(p * 16 + j) * 16), 1u, __ATOMIC_RELAXED, __HIP_MEMORY_SCOPE_AGENT);
    }
  } else {
    gemm2_prefetch(lds, ws, j >> 1, lane, w, 150, 8);
    WAIT_VM(0);
  }
  STAMP(1, 9);
  if (w == 0) await_flags(ws, WS_FLP + (size_t)p * 256, 14, lane);
  BAR();
  STAMP(1, 10);
  gemm2_phase(lds, ws, out, p * 2 + (j & 1), j >> 1, lane, w SPASS);
  STAMP(1, 13);
  STAMP_FLUSH(1);
}

extern "C" void kernel_launch(void* const* d_in, const int* in_sizes, int n_in, void* d_out, int out_size, void* d_ws, size_t ws_size, hipStream_t stream) {
  const u16* q = (const u16*)d_in[0]; const u16* k = (const u16*)d_in[1]; const u16* v = (const u16*)d_in[2];
  const u16* shk = (const u16*)d_in[3]; const u16* fk = (const u16*)d_in[4]; const u16* sD = (const u16*)d_in[5];
  const u16* wg = (const u16*)d_in[7]; const u16* wu = (const u16*)d_in[8]; const u16* wd = (const u16*)d_in[9];
  char* ws = (char*)d_ws;
  (void)hipFuncSetAttribute((const void*)k0_prep, hipFuncAttributeMaxDynamicSharedMemorySize, K0_LDS);
  (void)hipFuncSetAttribute((const void*)kf_fused, hipFuncAttributeMaxDynamicSharedMemorySize, KF_LDS);
  k0_prep<<<K0_TILES + K0_STATS + K0_NPF, 256, K0_LDS, stream>>>(q, k, v, shk, fk, wg, wu, wd, (const u16*)d_out, ws);
  kf_fused<<<256, 512, KF_LDS, stream>>>(q, fk, sD, wg, wu, wd, ws, (u16*)d_out);
}
```

```cpp
#include <hip/hip_runtime.h>
#include <hip/hip_bf16.h>

typedef unsigned short u16;
typedef unsigned int u32;
typedef __attribute__((ext_vector_type(2))) unsigned int u32x2;
typedef __attribute__((ext_vector_type(4))) unsigned int u32x4;
typedef __attribute__((ext_vector_type(8))) short bf16x8;
typedef __attribute__((ext_vector_type(2))) __bf16 bf16x2;
typedef __attribute__((ext_vector_type(2))) float f32x2;
typedef __attribute__((ext_vector_type(4))) float f32x4;
typedef __attribute__((ext_vector_type(4))) unsigned short u16x4;
#define LAS __attribute__((address_space(3)))

static constexpr int S = 2048, D = 640, F = 1728;
static constexpr int NKS1 = D / 32;
static constexpr int NKS2 = F / 32;

static constexpr size_t WS_UR1 = 0;
static constexpr size_t WS_HB  = WS_UR1 + (size_t)D * S * 2;
static constexpr size_t WS_ST1 = WS_HB  + (size_t)D * S * 2;
static constexpr size_t WS_ST2 = WS_ST1 + (size_t)S * 8;
static constexpr size_t WS_W1T = WS_ST2 + (size_t)16 * 1024;
static constexpr int NKS2P = 56;
static constexpr size_t WS_W2T = WS_W1T + (size_t)14 * 8 * NKS1 * 2048;
static constexpr size_t WS_HF  = WS_W2T + (size_t)8 * NKS2P * 5 * 1024;
static constexpr size_t WS_FLG = WS_HF  + (size_t)128 * NKS2 * 1024;
static constexpr size_t WS_FLP = WS_FLG + 4096;
static constexpr size_t WS_END = WS_FLP + 4096;

__device__ __forceinline__ float bflo(u32 w) { return __uint_as_float(w << 16); }
__device__ __forceinline__ float bfhi(u32 w) { return __uint_as_float(w & 0xffff0000u); }
__device__ __forceinline__ u32 pack2(float lo, float hi) { f32x2 v = {lo, hi}; bf16x2 b = __builtin_convertvector(v, bf16x2); return __builtin_bit_cast(u32, b); }
__device__ __forceinline__ float rbf(float x) { return bflo(pack2(x, 0.f)); }
__device__ __forceinline__ float dot2(u32 a, u32 b, float c) { return __builtin_amdgcn_fdot2_f32_bf16(__builtin_bit_cast(bf16x2, a), __builtin_bit_cast(bf16x2, b), c, false); }
__device__ __forceinline__ f32x4 mfma16(u32x4 a, u32x4 b, f32x4 c) { return __builtin_amdgcn_mfma_f32_16x16x32_bf16(__builtin_bit_cast(bf16x8, a), __builtin_bit_cast(bf16x8, b), c, 0, 0, 0); }
__device__ __forceinline__ void dma16(const void* g, LAS void* l) { __builtin_amdgcn_global_load_lds((const u32*)g, (LAS u32*)l, 16, 0, 0); }
#define WAIT_VM(n) asm volatile("s_waitcnt vmcnt(%0)" ::"n"(n) : "memory")
#define WAIT_LGKM0() asm volatile("s_waitcnt lgkmcnt(0)" ::: "memory")
#define BAR() do { asm volatile("" ::: "memory"); __builtin_amdgcn_s_barrier(); asm volatile("" ::: "memory"); } while (0)
#define SB() __builtin_amdgcn_sched_barrier(0)
#define MEMFENCE() asm volatile("" ::: "memory")
#define MAKE_RSRC(p) __builtin_amdgcn_make_buffer_rsrc((void*)(p), 0, 0x7fffffff, 0x00020000)
#define ST16(rs, off, v) __builtin_amdgcn_raw_buffer_store_b128((v), (rs), (int)(off), 0, 16)

template <int CTRL, int RM> __device__ __forceinline__ float dpp_add(float v) { return v + __builtin_bit_cast(float, __builtin_amdgcn_update_dpp(0, __builtin_bit_cast(int, v), CTRL, RM, 0xf, true)); }
__device__ __forceinline__ float wave_sum(float v) {
  v = dpp_add<0x111, 0xf>(v); v = dpp_add<0x112, 0xf>(v); v = dpp_add<0x114, 0xf>(v); v = dpp_add<0x118, 0xf>(v);
  v = dpp_add<0x142, 0xa>(v); v = dpp_add<0x143, 0xc>(v);
  return __builtin_bit_cast(float, __builtin_amdgcn_readlane(__builtin_bit_cast(int, v), 63));
}

#define STAMP(kid, i) do {} while (0)
#define STAMP_DECL do {} while (0)
#define STAMP_FLUSH(kid) do {} while (0)

typedef __attribute__((ext_vector_type(4))) short s16x4;
__device__ __forceinline__ u16x4 tr_read(u32 addr) { u16x4 r; asm volatile("ds_read_b64_tr_b16 %0, %1" : "=v"(r) : "v"(addr) : "memory"); return r; }
#define DS_TR(dst, addr, off) asm volatile("ds_read_b64_tr_b16 %0, %1 offset:%2" : "=v"(dst) : "v"(addr), "n"(off))
#define DS_B64(dst, addr, off) asm volatile("ds_read_b64 %0, %1 offset:%2" : "=v"(dst) : "v"(addr), "n"(off))
#define DS_B128(dst, addr, off) asm volatile("ds_read_b128 %0, %1 offset:%2" : "=v"(dst) : "v"(addr), "n"(off))
#define MFMA_IP(acc, a, b) asm volatile("v_mfma_f32_16x16x32_bf16 %0, %1, %2, %0" : "+v"(acc) : "v"(a), "v"(b))
#define MFMA_Z(acc, a, b) asm volatile("v_mfma_f32_16x16x32_bf16 %0, %1, %2, 0" : "=&v"(acc) : "v"(a), "v"(b))
#define GLD16(dst, voff, sbase, off) asm volatile("global_load_dwordx4 %0, %1, %2 offset:%3" : "=v"(dst) : "v"(voff), "s"(sbase), "n"(off) : "memory")
#define WAIT_LGKM(n) asm volatile("s_waitcnt lgkmcnt(%0)" ::"n"(n) : "memory")

static constexpr int K0_TILES = 32 * 10, K0_STATS = 128, K0_PF = 148;
#define K0_TOUCH 1
#define K0_NPF 0
static constexpr int K0_RAW = 17 * 1024, K0_LDS = K0_RAW + 64 * 144;
#define CODE_PF 41000
__global__ __launch_bounds__(256) void k0_prep(const u16* __restrict__ x, const u16* __restrict__ k, const u16* __restrict__ v,
                                               const u16* __restrict__ shk, const u16* __restrict__ fk, const u16* __restrict__ wg, const u16* __restrict__ wu,
                                               const u16* __restrict__ wd, const u16* __restrict__ outp, char* __restrict__ ws) {
  extern __shared__ __attribute__((aligned(16))) char shm[];
  LAS char* lds = (LAS char*)shm;
  const int t = threadIdx.x, lane = t & 63, w = __builtin_amdgcn_readfirstlane(t >> 6), b = blockIdx.x;
  STAMP_DECL; STAMP(0, 0);
  const auto rs = MAKE_RSRC(ws);
  if (CODE_PF > 0 && w == 3 && (b >> 3) * 8192 < CODE_PF) {
    unsigned long long pc;
    asm volatile("s_getpc_b64 s[40:41]\n\ts_add_u32 s40, s40, _Z8kf_fusedPKtS0_S0_S0_S0_S0_PcPt@rel32@lo+4\n\ts_addc_u32 s41, s41, _Z8kf_fusedPKtS0_S0_S0_S0_S0_PcPt@rel32@hi+12\n\ts_mov_b64 %0, s[40:41]" : "=s"(pc) :: "scc", "s40", "s41");
    int o = (b >> 3) * 8192 + lane * 128; o = o < CODE_PF - 4 ? o : CODE_PF - 4;
    u32 sink;
    asm volatile("global_load_dword %0, %1, off\n\ts_waitcnt vmcnt(0)" : "=&v"(sink) : "v"((const char*)pc + (o & ~3)) : "memory");
  }
  if (b < K0_TILES) {
    const int st = b / 10, ct = b % 10, s0 = st * 64, c0 = ct * 64;
#pragma unroll
    for (int i = 0; i < 5; ++i) {
      const int pc = w + 4 * i;
      if (pc < 17) {
        const u16* src = pc < 9 ? k : v;
        int row = (pc < 9 ? s0 - 8 + 8 * pc : s0 + 8 * (pc - 9)) + (lane >> 3);
        row = row < 0 ? 0 : row;
        dma16(src + (size_t)row * D + c0 + (lane & 7) * 8, lds + pc * 1024);
      }
    }
    const int cp = t & 31, rg = t >> 5, ch = c0 + 2 * cp;
    const u32x4 sh = *(const u32x4*)(shk + ch * 4);
    WAIT_VM(0); __syncthreads();
    STAMP(0, 1);
    u32 kk[11], vv[8];
    LAS const char* rk = lds + (8 * rg + 5) * 128 + cp * 4;
#pragma unroll
    for (int i = 0; i < 11; ++i) kk[i] = *(LAS const u32*)(rk + i * 128);
#pragma unroll
    for (int i = 0; i < 8; ++i) vv[i] = *(LAS const u32*)(lds + 9 * 1024 + (8 * rg + i) * 128 + cp * 4);
    if (st == 0 && rg == 0) { kk[0] = kk[1] = kk[2] = 0u; }
    const float a0 = bflo(sh.x), a1 = bfhi(sh.x), a2 = bflo(sh.y), a3 = bfhi(sh.y);
    const float b0 = bflo(sh.z), b1 = bfhi(sh.z), b2 = bflo(sh.w), b3 = bfhi(sh.w);
    float ka[11], kb[11];
#pragma unroll
    for (int i = 0; i < 11; ++i) { ka[i] = bflo(kk[i]); kb[i] = bfhi(kk[i]); }
    float ua[8], ub[8];
#pragma unroll
    for (int i = 0; i < 8; ++i) {
      ua[i] = (ka[i] * a3 + ka[i + 1] * a2 + ka[i + 2] * a1 + ka[i + 3] * a0) * bflo(vv[i]);
      ub[i] = (kb[i] * b3 + kb[i + 1] * b2 + kb[i + 2] * b1 + kb[i + 3] * b0) * bfhi(vv[i]);
    }
    LAS char* L1 = lds + K0_RAW;
    const int o = (7 - rg) * 16;
    *(LAS u32x4*)(L1 + (2 * cp) * 144 + o) = (u32x4){pack2(ua[7], ua[6]), pack2(ua[5], ua[4]), pack2(ua[3], ua[2]), pack2(ua[1], ua[0])};
    *(LAS u32x4*)(L1 + (2 * cp + 1) * 144 + o) = (u32x4){pack2(ub[7], ub[6]), pack2(ub[5], ub[4]), pack2(ub[3], ub[2]), pack2(ub[1], ub[0])};
    __syncthreads();
    u32x4 o1[2];
#pragma unroll
    for (int i = 0; i < 2; ++i) o1[i] = *(LAS const u32x4*)(L1 + ((t >> 3) + 32 * i) * 144 + (t & 7) * 16);
#pragma unroll
    for (int i = 0; i < 2; ++i) ST16(rs, WS_UR1 + ((size_t)(c0 + (t >> 3) + 32 * i) * S + (1984 - s0)) * 2 + (t & 7) * 16, o1[i]);
    STAMP(0, 2);
  } else if (b >= K0_TILES + K0_STATS) {
    const int pf = b - K0_TILES - K0_STATS;
    u32x4 sink[16];
    if (pf < 142) {
      const char* base; size_t bytes; int idx;
      if (pf < 40) { base = (const char*)fk; bytes = (size_t)D * S * 2; idx = pf; }
      else { const int m = (pf - 40) / 34; base = (const char*)(m == 0 ? wg : (m == 1 ? wu : wd)); bytes = (size_t)D * F * 2; idx = (pf - 40) % 34; }
#pragma unroll
      for (int i = 0; i < 16; ++i) { size_t o = (size_t)idx * 65536 + i * 4096 + t * 16; o = o < bytes ? o : bytes - 16; asm volatile("global_load_dwordx4 %0, %1, off" : "=v"(sink[i]) : "v"(base + o) : "memory"); }
      asm volatile("s_waitcnt vmcnt(0)" : "+v"(sink[0]), "+v"(sink[1]), "+v"(sink[2]), "+v"(sink[3]), "+v"(sink[4]), "+v"(sink[5]), "+v"(sink[6]), "+v"(sink[7]),
                                          "+v"(sink[8]), "+v"(sink[9]), "+v"(sink[10]), "+v"(sink[11]), "+v"(sink[12]), "+v"(sink[13]), "+v"(sink[14]), "+v"(sink[15])::"memory");
    } else if (K0_TOUCH) {
      const int i0 = (pf - 142) * 256 + t;
      size_t o = WS_HB + (size_t)i0 * 65536; o = o < WS_END - 16 ? o : WS_HB;
      const int io = i0 < 40 ? i0 : 0;
      asm volatile("global_load_dwordx4 %0, %1, off" : "=v"(sink[0]) : "v"(ws + o) : "memory");
      asm volatile("global_load_dwordx4 %0, %1, off" : "=v"(sink[1]) : "v"((const char*)outp + (size_t)io * 65536) : "memory");
      asm volatile("s_waitcnt vmcnt(0)" : "+v"(sink[0]), "+v"(sink[1])::"memory");
    }
  } else {
    const int rbase = (b - K0_TILES) * 16 + w * 4;
    u32x4 xa[4], xb[4];
#pragma unroll
    for (int r = 0; r < 4; ++r) {
      xa[r] = *(const u32x4*)(x + (size_t)(rbase + r) * D + lane * 8);
      xb[r] = *(const u32x4*)(x + (size_t)(rbase + r) * D + 512 + (lane & 15) * 8);
    }
    const float mk = lane < 16 ? 1.f : 0.f;
    float mean[4], q[4];
#pragma unroll
    for (int r = 0; r < 4; ++r) {
      float s = 0.f, s2 = 0.f;
#pragma unroll
      for (int j = 0; j < 4; ++j) { s += bflo(xa[r][j]) + bfhi(xa[r][j]); s2 += bflo(xb[r][j]) + bfhi(xb[r][j]); }
      mean[r] = wave_sum(s + mk * s2) * (1.0f / D);
    }
#pragma unroll
    for (int r = 0; r < 4; ++r) {
      float s = 0.f, s2 = 0.f;
#pragma unroll
      for (int j = 0; j < 4; ++j) {
        float a = bflo(xa[r][j]) - mean[r], c = bfhi(xa[r][j]) - mean[r]; s += a * a + c * c;
        a = bflo(xb[r][j]) - mean[r]; c = bfhi(xb[r][j]) - mean[r]; s2 += a * a + c * c;
      }
      q[r] = wave_sum(s + mk * s2) * (1.0f / D);
    }
    STAMP(0, 1);
    if (lane < 2) {
      const float m0 = lane ? mean[2] : mean[0], m1 = lane ? mean[3] : mean[1], v0 = lane ? q[2] : q[0], v1 = lane ? q[3] : q[1];
      const float sd0 = rbf(sqrtf(rbf(rbf(v0) + 1e-5f))), sd1 = rbf(sqrtf(rbf(rbf(v1) + 1e-5f)));
      const f32x4 o = {rbf(m0), 1.0f / sd0, rbf(m1), 1.0f / sd1};
      ST16(rs, WS_ST1 + (size_t)(rbase + 2 * lane) * 8, __builtin_bit_cast(u32x4, o));
    }
    STAMP(0, 2);
  }
  if (b == K0_TILES + K0_STATS - 1) { ST16(rs, WS_FLG + t * 16, ((u32x4){0u, 0u, 0u, 0u})); ST16(rs, WS_FLP + t * 16, ((u32x4){0u, 0u, 0u, 0u})); }
  STAMP_FLUSH(0);
}

#define POLL_SLEEP 1
typedef __attribute__((address_space(1))) unsigned gu32;
__device__ __forceinline__ void publish(char* ws, size_t off) {
  WAIT_VM(0); BAR();
  if (threadIdx.x == 0) __hip_atomic_store((gu32*)(ws + off), 1u, __ATOMIC_RELAXED, __HIP_MEMORY_SCOPE_AGENT);
}
__device__ __forceinline__ void await_flags(char* ws, size_t off, int n, int lane) {
  const unsigned long long t0 = __builtin_amdgcn_s_memrealtime();
  for (;;) {
    bool ok = true;
#pragma unroll
    for (int i = 0; i < 4; ++i) {
      const int idx = lane + 64 * i;
      if (64 * i < n) { const unsigned v = __hip_atomic_load((gu32*)(ws + off + (size_t)(idx < n ? idx : 0) * 16), __ATOMIC_RELAXED, __HIP_MEMORY_SCOPE_AGENT); ok &= v == 1u; }
    }
    if (__all(ok)) break;
    if (__builtin_amdgcn_s_memrealtime() - t0 > 400000ull) break;
    __builtin_amdgcn_s_sleep(POLL_SLEEP);
  }
  asm volatile("" ::: "memory");
}

static constexpr int CH_BYTES = 13568, CH_F = 0, CH_U1 = 4224, CH_U0 = 4224 + 4608 + 32;
static constexpr int K1_YL = 4 * CH_BYTES;
static constexpr int K1_YP = 8192 + 64;
#define K1_DELAY 80
static constexpr int K1_CONV = 160, K1_W1 = 54, K1_W2 = 30;

template <bool W1>
__device__ __forceinline__ void retile_job(LAS char* lds, const u16* __restrict__ src, int pitch, int cg, int mat, int ks0, char* ws, int w, int lane) {
  constexpr int NU = W1 ? 10 : 9;
  const auto rs = MAKE_RSRC(ws);
#pragma unroll
  for (int i = 0; i < NU; ++i) {
    const int pc = w + 8 * i, row = 8 * pc + (lane >> 3);
    dma16(src + (size_t)(ks0 * 32 + row) * pitch + (((lane & 7) ^ (row & 7)) * 8), lds + pc * 1024);
  }
  WAIT_VM(0); BAR();
  const int g = lane >> 4, q = (lane & 15) >> 2, p = lane & 3, fb = w & 3;
  const u32 l0 = (u32)(size_t)lds;
  u16x4 lo[NU], hi[NU];
#pragma unroll
  for (int i = 0; i < NU; ++i) {
    const int ks = (w >> 2) + 2 * i, row0 = 32 * ks + 8 * g + q, row1 = row0 + 4, chk = 2 * fb + (p >> 1);
    lo[i] = tr_read(l0 + row0 * 128 + ((chk ^ (row0 & 7)) * 16) + 8 * (p & 1));
    hi[i] = tr_read(l0 + row1 * 128 + ((chk ^ (row1 & 7)) * 16) + 8 * (p & 1));
  }
#pragma unroll
  for (int i = 0; i < NU; ++i) asm volatile("s_waitcnt lgkmcnt(0)" : "+v"(lo[i]), "+v"(hi[i])::"memory");
  float m = 0.f;
  if (W1) {
    float cs = 0.f;
#pragma unroll
    for (int i = 0; i < NU; ++i) {
      const u32x2 l2 = __builtin_bit_cast(u32x2, lo[i]), h2 = __builtin_bit_cast(u32x2, hi[i]);
      cs = dot2(l2.x, 0x3f803f80u, dot2(l2.y, 0x3f803f80u, dot2(h2.x, 0x3f803f80u, dot2(h2.y, 0x3f803f80u, cs))));
    }
    cs += __shfl_xor(cs, 16); cs += __shfl_xor(cs, 32);
    LAS float* red = (LAS float*)(lds + 80 * 1024);
    if (lane < 16) red[w * 16 + lane] = cs;
    WAIT_LGKM0(); BAR();
    m = (red[fb * 16 + (lane & 15)] + red[(fb + 4) * 16 + (lane & 15)]) * (1.0f / D);
  }
#pragma unroll
  for (int i = 0; i < NU; ++i) {
    const int ks = (w >> 2) + 2 * i;
    const u32x2 l2 = __builtin_bit_cast(u32x2, lo[i]), h2 = __builtin_bit_cast(u32x2, hi[i]);
    if (W1) {
      const u32x4 o = {pack2(bflo(l2.x) - m, bfhi(l2.x) - m), pack2(bflo(l2.y) - m, bfhi(l2.y) - m), pack2(bflo(h2.x) - m, bfhi(h2.x) - m), pack2(bflo(h2.y) - m, bfhi(h2.y) - m)};
      const int blk = cg * 4 + fb;
      ST16(rs, WS_W1T + ((size_t)(blk * NKS1 + ks) * 2 + mat) * 1024 + lane * 16, o);
    } else {
      const int df = cg * 4 + fb, dt = df / 5, j = df % 5;
      ST16(rs, WS_W2T + ((size_t)(dt * NKS2P + ks0 + ks) * 5 + j) * 1024 + lane * 16, ((u32x4){l2.x, l2.y, h2.x, h2.y}));
    }
  }
  if (!W1 && ks0 == 36 && cg < 8) {
#pragma unroll
    for (int i = 0; i < 2; ++i) { const int pc = w + 8 * i; if (pc < 10) ST16(rs, WS_W2T + ((size_t)(cg * NKS2P + NKS2) * 5 + pc) * 1024 + lane * 16, ((u32x4){0u, 0u, 0u, 0u})); }
  }
  if (W1 && cg == 26 && mat == 0) {
#pragma unroll
    for (int i = 0; i < 3; ++i) { const int ks = w + 8 * i; if (ks < NKS1) ST16(rs, WS_W1T + ((size_t)(108 * NKS1 + ks) * 2) * 1024 + lane * 16, ((u32x4){0x3f803f80u, 0x3f803f80u, 0x3f803f80u, 0x3f803f80u})); }
  }
}

#define SARG
#define SPASS

__device__ __forceinline__ void conv_phase(LAS char* lds, const u16* __restrict__ x, const u16* __restrict__ fk, const u16* __restrict__ sD, char* __restrict__ ws, int b, int t, int lane, int w SARG) {
  const auto rs = MAKE_RSRC(ws);
  const int cgp = (b & 7) * 20 + (b >> 3);
  const int c = w & 3, hf = w >> 2, d = cgp * 4 + c;
  LAS char* cb = lds + c * CH_BYTES;
  {
    const char* gF = (const char*)fk + (size_t)d * S * 2 + lane * 16;
    const char* g1 = ws + WS_UR1 + (size_t)d * S * 2 + lane * 16;
#pragma unroll
    for (int i = 0; i < 2; ++i) {
      const int pc = hf * 2 + i;
      dma16(g1 + pc * 1024, cb + CH_U1 + pc * 1024);
      dma16(gF + pc * 1024, cb + CH_F + 64 + pc * 1024);
    }
    if (hf == 0) { if (lane < 32) *(LAS u32x4*)(cb + CH_U1 + 4096 + lane * 16) = (u32x4){0, 0, 0, 0}; if (lane < 4) *(LAS u32x4*)(cb + CH_F + lane * 16) = (u32x4){0, 0, 0, 0}; }
    else { if (lane < 32) *(LAS u32x4*)(cb + CH_U0 + 4096 + lane * 16) = (u32x4){0, 0, 0, 0}; }
  }
  const u32x2 dw = *(const u32x2*)(sD + cgp * 4);
  WAIT_VM(0); WAIT_LGKM0(); BAR();
#pragma unroll
  for (int i = 0; i < 2; ++i) {
    const int p0 = 1024 * hf + 512 * i + 8 * lane;
    const u32x4 in = *(LAS const u32x4*)(cb + CH_U1 + p0 * 2); const u32 nx = *(LAS const u32*)(cb + CH_U1 + p0 * 2 + 16);
    *(LAS u32x4*)(cb + CH_U0 + p0 * 2) = (u32x4){__builtin_amdgcn_alignbit(in.y, in.x, 16), __builtin_amdgcn_alignbit(in.z, in.y, 16), __builtin_amdgcn_alignbit(in.w, in.z, 16), __builtin_amdgcn_alignbit(nx, in.w, 16)};
  }
  WAIT_LGKM0(); BAR();
  STAMP(1, 1);
  u32x2 xr[4]; u32x4 stv[2];
  const char* gx = (const char*)x + (size_t)(2 * t) * D * 2 + cgp * 8;
#define C_LDX(i) do { \
      if ((i) == 0) asm volatile("global_load_dwordx2 %0, %1, off" : "=v"(xr[0]) : "v"(gx) : "memory"); \
      if ((i) == 1) asm volatile("global_load_dwordx2 %0, %1, off offset:1280" : "=v"(xr[1]) : "v"(gx) : "memory"); \
      if ((i) == 2) asm volatile("global_load_dwordx2 %0, %1, off" : "=v"(xr[2]) : "v"(gx + (size_t)1024 * D * 2) : "memory"); \
      if ((i) == 3) asm volatile("global_load_dwordx2 %0, %1, off offset:1280" : "=v"(xr[3]) : "v"(gx + (size_t)1024 * D * 2) : "memory"); \
      if ((i) == 4) GLD16(stv[0], t * 16, ws + WS_ST1, 0); \
      if ((i) == 5) GLD16(stv[1], t * 16, ws + WS_ST1 + 8192, 0); } while (0)
#define C_XWAIT() asm volatile("s_waitcnt vmcnt(0) lgkmcnt(0)" : "+v"(xr[0]), "+v"(xr[1]), "+v"(xr[2]), "+v"(xr[3]), "+v"(stv[0]), "+v"(stv[1])::"memory")
  if (hf == 0) {
    const int r = lane & 15, g = lane >> 4;
    f32x4 acc[8];
    const int a = r >> 1, e = r & 1;
    const u32 ub = (u32)(size_t)(cb + (e ? CH_U1 : CH_U0) + 448 - 64 * a + 16 * g);
    u32 fbp[4];
#pragma unroll
    for (int i = 0; i < 4; ++i) fbp[i] = (u32)(size_t)(cb + CH_F + 16 * g + 4 * r + 4) + 1024 * i;
    u32x4 U[2][8]; u32x2 Fr[2][8][2];
#define C_RDU(buf, i, cc) DS_B128(U[buf][i], ub, 512 * (7 - (i)) + 64 * (cc))
#define C_RDF(buf, i, cc, hh) asm volatile("ds_read2_b32 %0, %1 offset0:%2 offset1:%3" : "=v"(Fr[buf][i][hh]) : "v"(fbp[(i) >> 1]), "n"(128 * ((i) & 1) + 16 * (cc) + 2 * (hh)), "n"(128 * ((i) & 1) + 16 * (cc) + 2 * (hh) + 1))
#define C_F(buf, i) ((u32x4){Fr[buf][i][0].x, Fr[buf][i][0].y, Fr[buf][i][1].x, Fr[buf][i][1].y})
#define C_WAIT(buf) do { \
      asm volatile("s_waitcnt lgkmcnt(0)" : "+v"(U[buf][0]), "+v"(U[buf][1]), "+v"(U[buf][2]), "+v"(U[buf][3]), "+v"(U[buf][4]), "+v"(U[buf][5]), "+v"(U[buf][6]), "+v"(U[buf][7])::"memory"); \
      asm volatile("" : "+v"(Fr[buf][0][0]), "+v"(Fr[buf][0][1]), "+v"(Fr[buf][1][0]), "+v"(Fr[buf][1][1]), "+v"(Fr[buf][2][0]), "+v"(Fr[buf][2][1]), "+v"(Fr[buf][3][0]), "+v"(Fr[buf][3][1]), \
                        "+v"(Fr[buf][4][0]), "+v"(Fr[buf][4][1]), "+v"(Fr[buf][5][0]), "+v"(Fr[buf][5][1]), "+v"(Fr[buf][6][0]), "+v"(Fr[buf][6][1]), "+v"(Fr[buf][7][0]), "+v"(Fr[buf][7][1])); } while (0)
#pragma unroll
    for (int i = 0; i < 8; ++i) { C_RDF(0, i, 0, 0); C_RDF(0, i, 0, 1); C_RDU(0, i, 0); }
    C_WAIT(0);
#pragma unroll
    for (int cc = 0; cc < 8; ++cc) {
      const int cur = cc & 1, nxt = cur ^ 1;
      int n = 0;
#pragma unroll
      for (int c1 = 0; c1 < 8; ++c1)
#pragma unroll
        for (int i = c1; i < 8; ++i) {
          if (cc == 0 && c1 == 0) MFMA_Z(acc[i], U[cur][i - c1], C_F(cur, c1)); else MFMA_IP(acc[i], U[cur][i - c1], C_F(cur, c1));
          if (cc < 7 && n < 24) { const int j = n / 3, kk = n % 3; if (kk == 0) C_RDU(nxt, j, cc + 1); else C_RDF(nxt, j, cc + 1, kk - 1); }
          if (cc < 6 && n == 28) C_LDX(cc);
          ++n;
        }
      if (cc < 7) C_WAIT(nxt);
    }
    asm volatile("s_nop 15\n\ts_nop 15" ::: "memory");
    STAMP(1, 2);
    LAS char* yl = lds + K1_YL + c * K1_YP;
#pragma unroll
    for (int j = 0; j < 8; ++j)
#pragma unroll
      for (int hh = 0; hh < 2; ++hh) *(LAS f32x2*)(yl + (256 * j + 32 * (2 * g + hh) + 2 * r) * 4) = (f32x2){acc[j][2 * hh], acc[j][2 * hh + 1]};
    C_XWAIT();
  } else {
    C_LDX(0); C_LDX(1); C_LDX(2); C_LDX(3); C_LDX(4); C_LDX(5);
    C_XWAIT();
  }
  BAR();
  STAMP(1, 3);
#pragma unroll
  for (int i = 0; i < 2; ++i) {
    const int s = 2 * t + 1024 * i;
    f32x2 y2[4];
    const float Dd[4] = {bflo(dw.x), bfhi(dw.x), bflo(dw.y), bfhi(dw.y)};
#pragma unroll
    for (int cc = 0; cc < 4; ++cc) {
      const f32x2 y = *(LAS const f32x2*)(lds + K1_YL + cc * K1_YP + s * 4);
      const u32 uw = *(LAS const u32*)(lds + cc * CH_BYTES + CH_U1 + (2046 - s) * 2);
      y2[cc] = (f32x2){y[0] + bfhi(uw) * Dd[cc], y[1] + bflo(uw) * Dd[cc]};
    }
    const f32x4 st = __builtin_bit_cast(f32x4, stv[i]);
    const u32x2 x0 = xr[2 * i], x1 = xr[2 * i + 1];
    const float xa[4] = {bflo(x0.x), bfhi(x0.x), bflo(x0.y), bfhi(x0.y)}, xb[4] = {bflo(x1.x), bfhi(x1.x), bflo(x1.y), bfhi(x1.y)};
    float ha[4], hb[4];
#pragma unroll
    for (int cc = 0; cc < 4; ++cc) { ha[cc] = xa[cc] + (xa[cc] - st[0]) * st[1] * y2[cc][0]; hb[cc] = xb[cc] + (xb[cc] - st[2]) * st[3] * y2[cc][1]; }
    ST16(rs, WS_HB + ((size_t)cgp * S + s) * 8, ((u32x4){pack2(ha[0], ha[1]), pack2(ha[2], ha[3]), pack2(hb[0], hb[1]), pack2(hb[2], hb[3])}));
  }
  STAMP(1, 4);
}

#define DMA_SC1(m0v, voffv, sbase, off) asm volatile("s_mov_b32 m0, %0\n\ts_nop 0\n\tglobal_load_lds_dwordx4 %1, %2 offset:%3 sc1" :: "s"(m0v), "v"(voffv), "s"(sbase), "n"(off) : "memory")
#define GLD16C(dst, voff, sbase, off) asm volatile("global_load_dwordx4 %0, %1, %2 offset:%3 sc1" : "=v"(dst) : "v"(voff), "s"(sbase), "n"(off) : "memory")
static constexpr int G1_NS = 12, G1_SLOT = 8192, G1_WR = 4, G1_RING = 64 * 1024;
static constexpr int KF_CNT = 60 * 1024;
__host__ __device__ constexpr int g2_slot(int kq, int s) { return s < 3 ? kq * 15360 + s * 5120 : G1_RING + kq * 20480 + (s - 3) * 5120; }
__device__ __forceinline__ void gemm2_prefetch(LAS char* lds, char* __restrict__ ws, int dt, int lane, int first, int last, int step) {
  const u32 lds0 = (u32)(size_t)lds, voff = lane * 16;
  const char* wb = ws + WS_W2T + (size_t)dt * NKS2P * 5 * 1024;
  for (int pc = first; pc < last; pc += step) {
    int kq, o, dst;
    if (pc < 60) { kq = pc / 15; o = (pc - 15 * kq) * 1024; dst = kq * 15360 + o; }
    else { const int q = pc - 60; kq = q / 20; o = 15360 + (q - 20 * kq) * 1024; dst = G1_RING + kq * 20480 + (q - 20 * kq) * 1024; }
    const u32 m0v = __builtin_amdgcn_readfirstlane(lds0 + dst);
    const char* sb = wb + kq * 14 * 5120 + o;
    DMA_SC1(m0v, voff, sb, 0);
  }
}
__device__ __forceinline__ void gemm2_wq_issue(char* __restrict__ ws, int dt, int lane, int w, u32x4 (&wq)[7][3]) {
  const char* wsb = ws + WS_W2T + ((size_t)dt * NKS2P + (w >> 1) * 14 + 7) * 5120 + (w & 1) * 3072;
  const u32 voff = lane * 16;
  const int sh = __builtin_amdgcn_readfirstlane(w & 1);
#pragma unroll
  for (int i = 0; i < 7; ++i) {
    GLD16C(wq[i][0], voff, wsb + i * 5120, 0); GLD16C(wq[i][1], voff, wsb + i * 5120, 1024);
    asm volatile("s_cmp_lg_u32 %3, 0\n\ts_cbranch_scc1 1f\n\tglobal_load_dwordx4 %0, %1, %2 offset:2048 sc1\n1:" : "=&v"(wq[i][2]) : "v"(voff), "s"(wsb + i * 5120), "s"(sh) : "scc", "memory");
  }
}
__device__ __forceinline__ void gemm2_wq_wait(u32x4 (&wq)[7][3]) {
  asm volatile("s_waitcnt vmcnt(0)" : "+v"(wq[0][0]), "+v"(wq[0][1]), "+v"(wq[0][2]), "+v"(wq[1][0]), "+v"(wq[1][1]), "+v"(wq[1][2]), "+v"(wq[2][0]), "+v"(wq[2][1]), "+v"(wq[2][2]),
                                      "+v"(wq[3][0]), "+v"(wq[3][1]), "+v"(wq[3][2])::"memory");
  asm volatile("" : "+v"(wq[4][0]), "+v"(wq[4][1]), "+v"(wq[4][2]), "+v"(wq[5][0]), "+v"(wq[5][1]), "+v"(wq[5][2]), "+v"(wq[6][0]), "+v"(wq[6][1]), "+v"(wq[6][2]));
}

__device__ __forceinline__ void gemm1_phase(LAS char* lds, char* __restrict__ ws, int rt, int ct, int dt2, int lane, int w, unsigned tag, size_t flag_off, u32x4 (&wq)[7][3] SARG) {
  const u32 lds0 = (u32)(size_t)lds + G1_RING;
  const int s0 = rt * 128;
  const u32 voff = lane * 16;
  const auto rs = MAKE_RSRC(ws);
  if (w >= 4) {
    const int j = w - 4;
    const u32 hv = voff ^ ((j & 1) * 128);
    const char* hsb = ws + WS_HB + ((size_t)(2 * j) * S + s0) * 8;
    const u32 ldsw = lds0 + 2 * j * 1024;
#define G1_SLAB(ks) do { DMA_SC1(ldsw + ((ks) % G1_NS) * G1_SLOT, hv, hsb + (size_t)(ks) * 8 * S * 8, 0); DMA_SC1(ldsw + ((ks) % G1_NS) * G1_SLOT + 1024, hv, hsb + (size_t)(ks) * 8 * S * 8 + S * 8, 0); } while (0)
    G1_SLAB(0); G1_SLAB(1); G1_SLAB(2); G1_SLAB(3); G1_SLAB(4);
    WAIT_VM(8); BAR();
    G1_SLAB(5); G1_SLAB(6); G1_SLAB(7); G1_SLAB(8);
    WAIT_VM(8); BAR();
    gemm2_prefetch(lds, ws, dt2, lane, j, 20, 4);
    G1_SLAB(9); G1_SLAB(10); G1_SLAB(11);
    WAIT_VM(6); BAR();
    gemm2_prefetch(lds, ws, dt2, lane, 20 + j, 40, 4);
    G1_SLAB(12); G1_SLAB(13); G1_SLAB(14); G1_SLAB(15);
    WAIT_VM(6); BAR();
    gemm2_prefetch(lds, ws, dt2, lane, 40 + j, 60, 4);
    G1_SLAB(16); G1_SLAB(17); G1_SLAB(18); G1_SLAB(19);
    WAIT_VM(6); BAR();
    gemm2_wq_issue(ws, dt2, lane, w, wq);
    WAIT_VM(14); BAR();
    BAR();
    gemm2_prefetch(lds, ws, dt2, lane, 60 + j, 140, 4);
    gemm2_wq_wait(wq);
  } else {
    const int blk0 = ct * 8 + 2 * w;
    const char* wsA = ws + WS_W1T + (size_t)(blk0 > 108 ? 108 : blk0) * NKS1 * 2048;
    const char* wsB = ws + WS_W1T + (size_t)(blk0 + 1 > 108 ? 108 : blk0 + 1) * NKS1 * 2048;
    const int g = lane >> 4;
    const u32 hbase = lds0 + g * 2048 + (lane & 15) * 8;
    const u32 hoffE = hbase + (g & 1) * 128, hoffO = hbase + (1 - (g & 1)) * 128 - 128;
    const u32 hoffE2 = hoffE + 65536, hoffO2 = hoffO + 65536;
    f32x4 acc[8][4];
    const int stat = __builtin_amdgcn_readfirstlane(blk0 == 108 ? 1 : 0);
#define G1_LDW(ks, i) do { if ((i) == 0) GLD16C(wr[(ks) % G1_WR][0], voff, wsA + (ks) * 2048, 0); if ((i) == 1) GLD16C(wr[(ks) % G1_WR][1], voff, wsA + (ks) * 2048, 1024); \
      if ((i) == 2) GLD16C(wr[(ks) % G1_WR][2], voff, wsB + (ks) * 2048, 0); if ((i) == 3) GLD16C(wr[(ks) % G1_WR][3], voff, wsB + (ks) * 2048, 1024); } while (0)
#define G1_RD(sf, hh, ks) DS_B64(hf[sf][hh], ((ks) % G1_NS) >= 8 ? (((sf) & 1) ? hoffO2 : hoffE2) : (((sf) & 1) ? hoffO : hoffE), (((ks) % G1_NS) & 7) * G1_SLOT + (hh) * 1024 + (sf) * 128)
#define G1_H(sf) ((u32x4){hf[sf][0].x, hf[sf][0].y, hf[sf][1].x, hf[sf][1].y})
#define G1_MM(ks, sf, i, STAT) do { \
      if (STAT) { if ((i) == 0) { if ((ks) == 0) MFMA_Z(acc[sf][0], wr[0][0], G1_H(sf)); else MFMA_IP(acc[sf][0], wr[(ks) % G1_WR][0], G1_H(sf)); } \
                  if ((i) == 2) { if ((ks) == 0) MFMA_Z(acc[sf][2], G1_H(sf), G1_H(sf)); else MFMA_IP(acc[sf][2], G1_H(sf), G1_H(sf)); } } \
      else { if ((ks) == 0) MFMA_Z(acc[sf][i], wr[0][i], G1_H(sf)); else MFMA_IP(acc[sf][i], wr[(ks) % G1_WR][i], G1_H(sf)); } } while (0)
#define G1_BODY(STAT) { \
      u32x4 wr[G1_WR][4]; u32x2 hf[8][2]; \
      _Pragma("unroll") for (int ks = 0; ks < 3; ++ks) { G1_LDW(ks, 0); G1_LDW(ks, 1); G1_LDW(ks, 2); G1_LDW(ks, 3); } \
      BAR();                                                   \
      STAMP(1, 7); \
      _Pragma("unroll") for (int sf = 0; sf < 7; ++sf) { G1_RD(sf, 0, 0); G1_RD(sf, 1, 0); } \
      _Pragma("unroll") for (int ks = 0; ks < NKS1; ++ks) { \
        if ((ks & 3) == 0) BAR(); \
          \
        if (ks + 2 < NKS1) WAIT_VM(8); else if (ks + 1 < NKS1) WAIT_VM(4); else WAIT_VM(0); \
        asm volatile("" : "+v"(wr[ks % G1_WR][0]), "+v"(wr[ks % G1_WR][1]), "+v"(wr[ks % G1_WR][2]), "+v"(wr[ks % G1_WR][3])); \
        _Pragma("unroll") for (int sf = 0; sf < 8; ++sf) { \
            \
          if (ks + 1 < NKS1 || sf == 0) asm volatile("s_waitcnt lgkmcnt(12)" : "+v"(hf[sf][0]), "+v"(hf[sf][1])::"memory"); \
          else if (sf < 7) asm volatile("s_waitcnt lgkmcnt(%2)" : "+v"(hf[sf][0]), "+v"(hf[sf][1]) : "n"(14 - 2 * sf) : "memory"); \
          else asm volatile("s_waitcnt lgkmcnt(0)" : "+v"(hf[sf][0]), "+v"(hf[sf][1])::"memory"); \
          G1_MM(ks, sf, 0, STAT); \
          if (sf == 0) G1_RD(7, 0, ks); else if (ks + 1 < NKS1) G1_RD(sf - 1, 0, ks + 1); \
          G1_MM(ks, sf, 1, STAT); \
          if (sf == 0) G1_RD(7, 1, ks); else if (ks + 1 < NKS1) G1_RD(sf - 1, 1, ks + 1); \
          G1_MM(ks, sf, 2, STAT); \
          if ((sf & 1) && ks + 3 < NKS1) G1_LDW(ks + 3, sf >> 1); \
          G1_MM(ks, sf, 3, STAT); \
        } } }
    if (stat) G1_BODY(true) else G1_BODY(false)
    BAR();
    asm volatile("s_nop 15\n\ts_nop 15" ::: "memory");
    STAMP(1, 8);
    if (stat) {
      const int sx = lane & 15;
#pragma unroll
      for (int sp = 0; sp < 2; ++sp) {
        float r2[4];
#pragma unroll
        for (int q = 0; q < 4; ++q) {
          const int sf = 4 * sp + q;
          const float dg = (sx & 2) ? ((sx & 1) ? acc[sf][2][3] : acc[sf][2][2]) : ((sx & 1) ? acc[sf][2][1] : acc[sf][2][0]);
          const float ssq = __shfl(dg, (sx >> 2) * 16 + sx);
          const float mean = acc[sf][0][0] * (1.0f / D);
          const float var = fmaxf(ssq * (1.0f / D) - mean * mean, 0.f);
          const float r = 1.0f / rbf(sqrtf(rbf(rbf(var) + 1e-5f)));
          r2[q] = r * r;
        }
        const float mine = g == 0 ? r2[0] : (g == 1 ? r2[1] : (g == 2 ? r2[2] : r2[3]));
        const int b4 = lane & ~3;
        const f32x4 o = {__shfl(mine, b4), __shfl(mine, b4 + 1), __shfl(mine, b4 + 2), __shfl(mine, b4 + 3)};
        if ((lane & 3) == 0) ST16(rs, WS_ST2 + (size_t)(s0 + 64 * sp + lane) * 4, __builtin_bit_cast(u32x4, o));
      }
    } else {
#pragma unroll
      for (int bi = 0; bi < 2; ++bi) {
        const int blk = blk0 + bi;
        if (blk < 108) {
          const u32 obase = (u32)(WS_HF + ((size_t)(rt * 8 + (lane >> 5)) * NKS2 + (blk >> 1)) * 1024) + (blk & 1) * 512 + (lane & 31) * 16;
#pragma unroll
          for (int pr = 0; pr < 4; ++pr) {
            const f32x4 z4 = {0.f, 0.f, 0.f, 0.f};
            const f32x4 hE = __builtin_elementwise_max(acc[2 * pr][2 * bi], z4) * acc[2 * pr][2 * bi + 1], hO = __builtin_elementwise_max(acc[2 * pr + 1][2 * bi], z4) * acc[2 * pr + 1][2 * bi + 1];
            u32 e0 = pack2(hE[0], hE[1]), e1 = pack2(hE[2], hE[3]), o0 = pack2(hO[0], hO[1]), o1 = pack2(hO[2], hO[3]);
            auto r0 = __builtin_amdgcn_permlane32_swap(e0, o0, false, false); auto r0b = __builtin_amdgcn_permlane16_swap(r0[0], r0[1], false, false);
            auto r1 = __builtin_amdgcn_permlane32_swap(e1, o1, false, false); auto r1b = __builtin_amdgcn_permlane16_swap(r1[0], r1[1], false, false);
            ST16(rs, obase + pr * 2 * NKS2 * 1024, ((u32x4){r0b[0], r1b[0], r0b[1], r1b[1]}));
          }
        }
      }
    }
    WAIT_VM(0);
    unsigned old = 0;
    if (lane == 0) old = __hip_atomic_fetch_add((LAS unsigned*)(lds + KF_CNT), 1u, __ATOMIC_RELAXED, __HIP_MEMORY_SCOPE_WORKGROUP);
    if (lane == 0 && old == 3u) __hip_atomic_store((gu32*)(ws + flag_off), tag, __ATOMIC_RELAXED, __HIP_MEMORY_SCOPE_AGENT);
    STAMP(1, 9);
    gemm2_wq_issue(ws, dt2, lane, w, wq);
    gemm2_wq_wait(wq);
  }
}

__device__ __forceinline__ void gemm2_phase(LAS char* lds, char* __restrict__ ws, u16* __restrict__ out, int rt, int dt, int lane, int w, u32x4 (&wq)[7][3] SARG) {
  const int sh = w & 1, kq = w >> 1, g = lane >> 4;
  const u32 voff = lane * 16, lds0 = (u32)(size_t)lds;
  const char* hA = ws + WS_HF + ((size_t)(rt * 4 + 2 * sh) * NKS2 + kq * 14) * 1024;
  const char* hB = hA + (size_t)NKS2 * 1024;
  const int hclamp = kq == 3 ? 1 : 0;
  u32x4 hq[6][2], wf[2][5];
#define G2_LH(n) do { const int o_ = ((n) >= 12 && hclamp) ? 11 * 1024 : (n) * 1024; GLD16C(hq[(n) % 6][0], voff, hA + o_, 0); GLD16C(hq[(n) % 6][1], voff, hB + o_, 0); } while (0)
  const u32 rlo = lds0 + kq * 15360 + lane * 16, rhi = lds0 + G1_RING + kq * 20480 - 3 * 5120 + lane * 16;
#define G2_SB(n) ((((n) % 7) < 3 ? rlo : rhi))
#define G2_READ(n) do { DS_B128(wf[(n) & 1][0], G2_SB(n), ((n) % 7) * 5120); DS_B128(wf[(n) & 1][1], G2_SB(n), ((n) % 7) * 5120 + 1024); DS_B128(wf[(n) & 1][2], G2_SB(n), ((n) % 7) * 5120 + 2048); \
    DS_B128(wf[(n) & 1][3], G2_SB(n), ((n) % 7) * 5120 + 3072); DS_B128(wf[(n) & 1][4], G2_SB(n), ((n) % 7) * 5120 + 4096); } while (0)
  const int shs = __builtin_amdgcn_readfirstlane(sh);
#define G2_WST(n, i) do { if ((i) < 2) asm volatile("ds_write_b128 %0, %1 offset:%2" :: "v"(G2_SB(n) + sh * 3072), "v"(wq[(n) - 7][i]), "n"(((n) % 7) * 5120 + (i) * 1024) : "memory"); \
    else asm volatile("s_cmp_lg_u32 %3, 0\n\ts_cbranch_scc1 1f\n\tds_write_b128 %0, %1 offset:%2\n1:" :: "v"(G2_SB(n)), "v"(wq[(n) - 7][2]), "n"(((n) % 7) * 5120 + 2048), "s"(shs) : "scc", "memory"); } while (0)
  G2_LH(0); G2_LH(1);
  float r2[3]; u32x2 res[3];
#pragma unroll
  for (int j = 0; j < 3; ++j) {
    const int f = kq + 4 * j < 10 ? kq + 4 * j : kq, srow = rt * 64 + (2 * sh + f / 5) * 16 + (lane & 15);
    asm volatile("global_load_dword %0, %1, off sc1" : "=v"(r2[j]) : "v"(ws + WS_ST2 + (size_t)srow * 4) : "memory");
    asm volatile("global_load_dwordx2 %0, %1, off sc1" : "=v"(res[j]) : "v"(ws + WS_HB + ((size_t)(dt * 20 + (f % 5) * 4 + g) * S + srow) * 8) : "memory");
  }
  G2_LH(2); G2_LH(3); G2_LH(4);
  f32x4 acc[10];
  G2_READ(0);
  WAIT_VM(14);
  STAMP(1, 11);
#define G2_MM(n, m) do { if ((n) == 0) MFMA_Z(acc[m], wf[0][(m) % 5], hq[0][(m) / 5]); else MFMA_IP(acc[m], wf[(n) & 1][(m) % 5], hq[(n) % 6][(m) / 5]); } while (0)
#define G2D(k) do {} while (0)
  u32 g2d[30];
#define G2_STEP(n) do { G2D(2 * (n)); \
    if ((n) == 1) WAIT_VM(14); else if ((n) > 1) { constexpr int iss_ = 5 + (n) < 14 ? 5 + (n) : 14; WAIT_VM(2 * (iss_ - ((n) + 1))); } \
    asm volatile("s_waitcnt lgkmcnt(0)" : "+v"(wf[(n) & 1][0]), "+v"(wf[(n) & 1][1]), "+v"(wf[(n) & 1][2]), "+v"(wf[(n) & 1][3]), "+v"(wf[(n) & 1][4]), "+v"(hq[(n) % 6][0]), "+v"(hq[(n) % 6][1])::"memory"); \
    if (((n) & 1) && (n) <= 9) BAR(); \
    G2D(2 * (n) + 1); \
    if ((n) + 1 < 14) G2_READ(((n) + 1 < 14 ? (n) + 1 : 0)); \
    G2_MM(n, 0); G2_MM(n, 1); if ((n) + 5 < 14) G2_LH(((n) + 5 < 14 ? (n) + 5 : 0)); \
    G2_MM(n, 2); G2_MM(n, 3); if ((n) >= 1 && (n) <= 7) G2_WST(((n) >= 1 && (n) <= 7 ? (n) + 6 : 7), 0); \
    G2_MM(n, 4); G2_MM(n, 5); G2_MM(n, 6); if ((n) >= 1 && (n) <= 7) G2_WST(((n) >= 1 && (n) <= 7 ? (n) + 6 : 7), 1); \
    G2_MM(n, 7); G2_MM(n, 8); if ((n) >= 1 && (n) <= 7) G2_WST(((n) >= 1 && (n) <= 7 ? (n) + 6 : 7), 2); \
    G2_MM(n, 9); } while (0)
  G2_STEP(0); G2_STEP(1); G2_STEP(2); G2_STEP(3); G2_STEP(4); G2_STEP(5); G2_STEP(6);
  G2_STEP(7); G2_STEP(8); G2_STEP(9); G2_STEP(10); G2_STEP(11); G2_STEP(12); G2_STEP(13);
  asm volatile("s_nop 15\n\ts_nop 15" ::: "memory");
  asm volatile("" : "+v"(r2[0]), "+v"(r2[1]), "+v"(r2[2]), "+v"(res[0]), "+v"(res[1]), "+v"(res[2]));
  STAMP(1, 12);
  BAR();
  LAS char* xb = lds + sh * 40960 + lane * 16;
#pragma unroll
  for (int f = 0; f < 10; ++f) if (kq != (f & 3)) *(LAS f32x4*)(xb + (f * 4 + kq) * 1024) = acc[f];
  WAIT_LGKM0(); BAR();
  u32 pk[3][2];
#define G2_FIN(KQ) do { _Pragma("unroll") for (int j = 0; j < 3; ++j) { \
      constexpr int kq_ = (KQ); const int f = kq_ + 4 * j < 10 ? kq_ + 4 * j : kq_; \
      f32x4 a = acc[f]; \
      _Pragma("unroll") for (int q = 0; q < 4; ++q) if (q != kq_) a += *(LAS const f32x4*)(xb + (f * 4 + q) * 1024); \
      pk[j][0] = pack2(bflo(res[j].x) + a[0] * r2[j], bfhi(res[j].x) + a[1] * r2[j]); \
      pk[j][1] = pack2(bflo(res[j].y) + a[2] * r2[j], bfhi(res[j].y) + a[3] * r2[j]); } } while (0)
  if (kq == 0) G2_FIN(0); else if (kq == 1) G2_FIN(1); else if (kq == 2) G2_FIN(2); else G2_FIN(3);
  const auto ro = MAKE_RSRC(out);
  {
    auto r0 = __builtin_amdgcn_permlane32_swap(pk[0][0], pk[1][0], false, false); auto r0b = __builtin_amdgcn_permlane16_swap(r0[0], r0[1], false, false);
    auto r1 = __builtin_amdgcn_permlane32_swap(pk[0][1], pk[1][1], false, false); auto r1b = __builtin_amdgcn_permlane16_swap(r1[0], r1[1], false, false);
    const int f = kq + 4 * (lane >> 5);
    ST16(ro, ((size_t)(rt * 64 + (2 * sh + f / 5) * 16 + (lane & 15)) * D + dt * 80 + (f % 5) * 16 + ((lane >> 4) & 1) * 8) * 2, ((u32x4){r0b[0], r1b[0], r0b[1], r1b[1]}));
  }
  if (kq < 2) {
    auto r0 = __builtin_amdgcn_permlane32_swap(pk[2][0], pk[2][0], false, false); auto r0b = __builtin_amdgcn_permlane16_swap(r0[0], r0[1], false, false);
    auto r1 = __builtin_amdgcn_permlane32_swap(pk[2][1], pk[2][1], false, false); auto r1b = __builtin_amdgcn_permlane16_swap(r1[0], r1[1], false, false);
    const int f = kq + 8;
    if (lane < 32) ST16(ro, ((size_t)(rt * 64 + (2 * sh + f / 5) * 16 + (lane & 15)) * D + dt * 80 + (f % 5) * 16 + ((lane >> 4) & 1) * 8) * 2, ((u32x4){r0b[0], r1b[0], r0b[1], r1b[1]}));
  }
}

static constexpr int KF_LDS = 160 * 1024;
__global__ __launch_bounds__(512, 2) void kf_fused(const u16* __restrict__ x, const u16* __restrict__ fk, const u16* __restrict__ sD, const u16* __restrict__ wg,
                                                   const u16* __restrict__ wu, const u16* __restrict__ wd, char* __restrict__ ws, u16* __restrict__ out) {
  extern __shared__ __attribute__((aligned(16))) char shm[];
  LAS char* lds = (LAS char*)shm;
  const int t = threadIdx.x, lane = t & 63, w = __builtin_amdgcn_readfirstlane(t >> 6), b = blockIdx.x;
  STAMP_DECL; STAMP(1, 0);
  if (b < K1_CONV) conv_phase(lds, x, fk, sD, ws, b, t, lane, w SPASS);
  else {
    { const unsigned long long t0 = __builtin_amdgcn_s_memrealtime(); while (__builtin_amdgcn_s_memrealtime() - t0 < K1_DELAY) __builtin_amdgcn_s_sleep(4); }
    const int jb = b - K1_CONV;
    if (jb < K1_W1) retile_job<true>(lds, ((jb & 1) ? wu : wg) + (jb >> 1) * 64, F, jb >> 1, jb & 1, 0, ws, w, lane);
    else if (jb < K1_W1 + K1_W2) { const int j2 = jb - K1_W1; retile_job<false>(lds, wd + (j2 / 3) * 64, D, j2 / 3, 0, (j2 % 3) * 18, ws, w, lane); }
  }
  publish(ws, WS_FLG + (size_t)b * 16);
  STAMP(1, 5);
  const int xg = b & 7, ix = b >> 3;
  const int p = (xg & 3) * 4 + (ix & 3), j = (xg >> 2) * 8 + (ix >> 2);
  if (t == 0) *(LAS unsigned*)(lds + KF_CNT) = 0u;
  if (w == 7) await_flags(ws, WS_FLG, K1_CONV + K1_W1 + K1_W2, lane);
  WAIT_LGKM0(); BAR();
  STAMP(1, 6);
  u32x4 wq[7][3];
  if (j < 14) gemm1_phase(lds, ws, p, j, j >> 1, lane, w, 1u, WS_FLP + (size_t)(p * 16 + j) * 16, wq SPASS);
  else {
    gemm2_prefetch(lds, ws, j >> 1, lane, w, 140, 8);
    gemm2_wq_issue(ws, j >> 1, lane, w, wq);
    gemm2_wq_wait(wq);
  }
  if (w == 4) await_flags(ws, WS_FLP + (size_t)p * 256, 14, lane);
  BAR();
  STAMP(1, 10);
  gemm2_phase(lds, ws, out, p * 2 + (j & 1), j >> 1, lane, w, wq SPASS);
  STAMP(1, 13);
  STAMP_FLUSH(1);
}

extern "C" void kernel_launch(void* const* d_in, const int* in_sizes, int n_in, void* d_out, int out_size, void* d_ws, size_t ws_size, hipStream_t stream) {
  const u16* q = (const u16*)d_in[0]; const u16* k = (const u16*)d_in[1]; const u16* v = (const u16*)d_in[2];
  const u16* shk = (const u16*)d_in[3]; const u16* fk = (const u16*)d_in[4]; const u16* sD = (const u16*)d_in[5];
  const u16* wg = (const u16*)d_in[7]; const u16* wu = (const u16*)d_in[8]; const u16* wd = (const u16*)d_in[9];
  char* ws = (char*)d_ws;
  (void)hipFuncSetAttribute((const void*)k0_prep, hipFuncAttributeMaxDynamicSharedMemorySize, K0_LDS);
  (void)hipFuncSetAttribute((const void*)kf_fused, hipFuncAttributeMaxDynamicSharedMemorySize, KF_LDS);
  k0_prep<<<K0_TILES + K0_STATS + K0_NPF, 256, K0_LDS, stream>>>(q, k, v, shk, fk, wg, wu, wd, (const u16*)d_out, ws);
  kf_fused<<<256, 512, KF_LDS, stream>>>(q, fk, sD, wg, wu, wd, ws, (u16*)d_out);
}
```

```cpp
#include <hip/hip_runtime.h>
#include <hip/hip_bf16.h>

typedef unsigned short u16;
typedef unsigned int u32;
typedef __attribute__((ext_vector_type(2))) unsigned int u32x2;
typedef __attribute__((ext_vector_type(4))) unsigned int u32x4;
typedef __attribute__((ext_vector_type(8))) short bf16x8;
typedef __attribute__((ext_vector_type(2))) __bf16 bf16x2;
typedef __attribute__((ext_vector_type(2))) float f32x2;
typedef __attribute__((ext_vector_type(4))) float f32x4;
typedef __attribute__((ext_vector_type(4))) unsigned short u16x4;
#define LAS __attribute__((address_space(3)))

static constexpr int S = 2048, D = 640, F = 1728;
static constexpr int NKS1 = D / 32;
static constexpr int NKS2 = F / 32;

static constexpr size_t WS_UR1 = 0;
static constexpr size_t WS_HB  = WS_UR1 + (size_t)D * S * 2;
static constexpr size_t WS_ST1 = WS_HB  + (size_t)D * S * 2;
static constexpr size_t WS_ST2 = WS_ST1 + (size_t)S * 8;
static constexpr size_t WS_W1T = WS_ST2 + (size_t)16 * 1024;
static constexpr int NKS2P = 56;
static constexpr size_t WS_W2T = WS_W1T + (size_t)14 * 8 * NKS1 * 2048;
static constexpr size_t WS_HF  = WS_W2T + (size_t)8 * NKS2P * 5 * 1024;
static constexpr size_t WS_FLG = WS_HF  + (size_t)128 * NKS2 * 1024;
static constexpr size_t WS_FLP = WS_FLG + 4096;
static constexpr size_t WS_END = WS_FLP + 4096;

__device__ __forceinline__ float bflo(u32 w) { return __uint_as_float(w << 16); }
__device__ __forceinline__ float bfhi(u32 w) { return __uint_as_float(w & 0xffff0000u); }
__device__ __forceinline__ u32 pack2(float lo, float hi) { f32x2 v = {lo, hi}; bf16x2 b = __builtin_convertvector(v, bf16x2); return __builtin_bit_cast(u32, b); }
__device__ __forceinline__ float rbf(float x) { return bflo(pack2(x, 0.f)); }
__device__ __forceinline__ float dot2(u32 a, u32 b, float c) { return __builtin_amdgcn_fdot2_f32_bf16(__builtin_bit_cast(bf16x2, a), __builtin_bit_cast(bf16x2, b), c, false); }
__device__ __forceinline__ f32x4 mfma16(u32x4 a, u32x4 b, f32x4 c) { return __builtin_amdgcn_mfma_f32_16x16x32_bf16(__builtin_bit_cast(bf16x8, a), __builtin_bit_cast(bf16x8, b), c, 0, 0, 0); }
__device__ __forceinline__ void dma16(const void* g, LAS void* l) { __builtin_amdgcn_global_load_lds((const u32*)g, (LAS u32*)l, 16, 0, 0); }
#define WAIT_VM(n) asm volatile("s_waitcnt vmcnt(%0)" ::"n"(n) : "memory")
#define WAIT_LGKM0() asm volatile("s_waitcnt lgkmcnt(0)" ::: "memory")
#define BAR() do { asm volatile("" ::: "memory"); __builtin_amdgcn_s_barrier(); asm volatile("" ::: "memory"); } while (0)
#define SB() __builtin_amdgcn_sched_barrier(0)
#define MEMFENCE() asm volatile("" ::: "memory")
#define MAKE_RSRC(p) __builtin_amdgcn_make_buffer_rsrc((void*)(p), 0, 0x7fffffff, 0x00020000)
#define ST16(rs, off, v) __builtin_amdgcn_raw_buffer_store_b128((v), (rs), (int)(off), 0, 16)

template <int CTRL, int RM> __device__ __forceinline__ float dpp_add(float v) { return v + __builtin_bit_cast(float, __builtin_amdgcn_update_dpp(0, __builtin_bit_cast(int, v), CTRL, RM, 0xf, true)); }
__device__ __forceinline__ float wave_sum(float v) {
  v = dpp_add<0x111, 0xf>(v); v = dpp_add<0x112, 0xf>(v); v = dpp_add<0x114, 0xf>(v); v = dpp_add<0x118, 0xf>(v);
  v = dpp_add<0x142, 0xa>(v); v = dpp_add<0x143, 0xc>(v);
  return __builtin_bit_cast(float, __builtin_amdgcn_readlane(__builtin_bit_cast(int, v), 63));
}

#define STAMP(kid, i) do {} while (0)
#define STAMP_DECL do {} while (0)
#define STAMP_FLUSH(kid) do {} while (0)

typedef __attribute__((ext_vector_type(4))) short s16x4;
__device__ __forceinline__ u16x4 tr_read(u32 addr) { u16x4 r; asm volatile("ds_read_b64_tr_b16 %0, %1" : "=v"(r) : "v"(addr) : "memory"); return r; }
#define DS_TR(dst, addr, off) asm volatile("ds_read_b64_tr_b16 %0, %1 offset:%2" : "=v"(dst) : "v"(addr), "n"(off))
#define DS_B64(dst, addr, off) asm volatile("ds_read_b64 %0, %1 offset:%2" : "=v"(dst) : "v"(addr), "n"(off))
#define DS_B128(dst, addr, off) asm volatile("ds_read_b128 %0, %1 offset:%2" : "=v"(dst) : "v"(addr), "n"(off))
#define MFMA_IP(acc, a, b) asm volatile("v_mfma_f32_16x16x32_bf16 %0, %1, %2, %0" : "+v"(acc) : "v"(a), "v"(b))
#define MFMA_Z(acc, a, b) asm volatile("v_mfma_f32_16x16x32_bf16 %0, %1, %2, 0" : "=&v"(acc) : "v"(a), "v"(b))
#define GLD16(dst, voff, sbase, off) asm volatile("global_load_dwordx4 %0, %1, %2 offset:%3" : "=v"(dst) : "v"(voff), "s"(sbase), "n"(off) : "memory")
#define WAIT_LGKM(n) asm volatile("s_waitcnt lgkmcnt(%0)" ::"n"(n) : "memory")

static constexpr int K0_TILES = 32 * 10, K0_STATS = 128, K0_PF = 148;
#define K0_TOUCH 1
#define K0_NPF 0
static constexpr int K0_RAW = 17 * 1024, K0_LDS = K0_RAW + 64 * 144;
#define TLB_PF 1
#define CODE_PF 41000
__global__ __launch_bounds__(256) void k0_prep(const u16* __restrict__ x, const u16* __restrict__ k, const u16* __restrict__ v,
                                               const u16* __restrict__ shk, const u16* __restrict__ fk, const u16* __restrict__ wg, const u16* __restrict__ wu,
                                               const u16* __restrict__ wd, const u16* __restrict__ outp, char* __restrict__ ws) {
  extern __shared__ __attribute__((aligned(16))) char shm[];
  LAS char* lds = (LAS char*)shm;
  const int t = threadIdx.x, lane = t & 63, w = __builtin_amdgcn_readfirstlane(t >> 6), b = blockIdx.x;
  STAMP_DECL; STAMP(0, 0);
  const auto rs = MAKE_RSRC(ws);
  if (CODE_PF > 0 && w == 3 && (b >> 3) * 8192 < CODE_PF) {
    unsigned long long pc;
    asm volatile("s_getpc_b64 s[40:41]\n\ts_add_u32 s40, s40, _Z8kf_fusedPKtS0_S0_S0_S0_S0_PcPt@rel32@lo+4\n\ts_addc_u32 s41, s41, _Z8kf_fusedPKtS0_S0_S0_S0_S0_PcPt@rel32@hi+12\n\ts_mov_b64 %0, s[40:41]" : "=s"(pc) :: "scc", "s40", "s41");
    int o = (b >> 3) * 8192 + lane * 128; o = o < CODE_PF - 4 ? o : CODE_PF - 4;
    u32 sink;
    asm volatile("global_load_dword %0, %1, off\n\ts_waitcnt vmcnt(0)" : "=&v"(sink) : "v"((const char*)pc + (o & ~3)) : "memory");
  }
  if (TLB_PF && w == 2 && (b >> 3) < 10) {
    const int n = (b >> 3) * 64 + lane;
    const char* p;
    if (n < 400) { const size_t o = WS_HB + (size_t)n * 65536; p = ws + (o < WS_END - 4 ? o : WS_HB); }
    else if (n < 440) p = (const char*)outp + (size_t)(n - 400) * 65536;
    else if (n < 480) p = (const char*)fk + (size_t)(n - 440) * 65536;
    else if (n < 582) { const int m = (n - 480) / 34, q = (n - 480) % 34; p = (const char*)(m == 0 ? wg : (m == 1 ? wu : wd)) + (size_t)q * 65536; }
    else p = ws + WS_HB;
    u32 sink;
    asm volatile("global_load_dword %0, %1, off\n\ts_waitcnt vmcnt(0)" : "=&v"(sink) : "v"(p) : "memory");
  }
  if (b < K0_TILES) {
    const int st = b / 10, ct = b % 10, s0 = st * 64, c0 = ct * 64;
#pragma unroll
    for (int i = 0; i < 5; ++i) {
      const int pc = w + 4 * i;
      if (pc < 17) {
        const u16* src = pc < 9 ? k : v;
        int row = (pc < 9 ? s0 - 8 + 8 * pc : s0 + 8 * (pc - 9)) + (lane >> 3);
        row = row < 0 ? 0 : row;
        dma16(src + (size_t)row * D + c0 + (lane & 7) * 8, lds + pc * 1024);
      }
    }
    const int cp = t & 31, rg = t >> 5, ch = c0 + 2 * cp;
    const u32x4 sh = *(const u32x4*)(shk + ch * 4);
    WAIT_VM(0); __syncthreads();
    STAMP(0, 1);
    u32 kk[11], vv[8];
    LAS const char* rk = lds + (8 * rg + 5) * 128 + cp * 4;
#pragma unroll
    for (int i = 0; i < 11; ++i) kk[i] = *(LAS const u32*)(rk + i * 128);
#pragma unroll
    for (int i = 0; i < 8; ++i) vv[i] = *(LAS const u32*)(lds + 9 * 1024 + (8 * rg + i) * 128 + cp * 4);
    if (st == 0 && rg == 0) { kk[0] = kk[1] = kk[2] = 0u; }
    const float a0 = bflo(sh.x), a1 = bfhi(sh.x), a2 = bflo(sh.y), a3 = bfhi(sh.y);
    const float b0 = bflo(sh.z), b1 = bfhi(sh.z), b2 = bflo(sh.w), b3 = bfhi(sh.w);
    float ka[11], kb[11];
#pragma unroll
    for (int i = 0; i < 11; ++i) { ka[i] = bflo(kk[i]); kb[i] = bfhi(kk[i]); }
    float ua[8], ub[8];
#pragma unroll
    for (int i = 0; i < 8; ++i) {
      ua[i] = (ka[i] * a3 + ka[i + 1] * a2 + ka[i + 2] * a1 + ka[i + 3] * a0) * bflo(vv[i]);
      ub[i] = (kb[i] * b3 + kb[i + 1] * b2 + kb[i + 2] * b1 + kb[i + 3] * b0) * bfhi(vv[i]);
    }
    LAS char* L1 = lds + K0_RAW;
    const int o = (7 - rg) * 16;
    *(LAS u32x4*)(L1 + (2 * cp) * 144 + o) = (u32x4){pack2(ua[7], ua[6]), pack2(ua[5], ua[4]), pack2(ua[3], ua[2]), pack2(ua[1], ua[0])};
    *(LAS u32x4*)(L1 + (2 * cp + 1) * 144 + o) = (u32x4){pack2(ub[7], ub[6]), pack2(ub[5], ub[4]), pack2(ub[3], ub[2]), pack2(ub[1], ub[0])};
    __syncthreads();
    u32x4 o1[2];
#pragma unroll
    for (int i = 0; i < 2; ++i) o1[i] = *(LAS const u32x4*)(L1 + ((t >> 3) + 32 * i) * 144 + (t & 7) * 16);
#pragma unroll
    for (int i = 0; i < 2; ++i) ST16(rs, WS_UR1 + ((size_t)(c0 + (t >> 3) + 32 * i) * S + (1984 - s0)) * 2 + (t & 7) * 16, o1[i]);
    STAMP(0, 2);
  } else if (b >= K0_TILES + K0_STATS) {
    const int pf = b - K0_TILES - K0_STATS;
    u32x4 sink[16];
    if (pf < 142) {
      const char* base; size_t bytes; int idx;
      if (pf < 40) { base = (const char*)fk; bytes = (size_t)D * S * 2; idx = pf; }
      else { const int m = (pf - 40) / 34; base = (const char*)(m == 0 ? wg : (m == 1 ? wu : wd)); bytes = (size_t)D * F * 2; idx = (pf - 40) % 34; }
#pragma unroll
      for (int i = 0; i < 16; ++i) { size_t o = (size_t)idx * 65536 + i * 4096 + t * 16; o = o < bytes ? o : bytes - 16; asm volatile("global_load_dwordx4 %0, %1, off" : "=v"(sink[i]) : "v"(base + o) : "memory"); }
      asm volatile("s_waitcnt vmcnt(0)" : "+v"(sink[0]), "+v"(sink[1]), "+v"(sink[2]), "+v"(sink[3]), "+v"(sink[4]), "+v"(sink[5]), "+v"(sink[6]), "+v"(sink[7]),
                                          "+v"(sink[8]), "+v"(sink[9]), "+v"(sink[10]), "+v"(sink[11]), "+v"(sink[12]), "+v"(sink[13]), "+v"(sink[14]), "+v"(sink[15])::"memory");
    } else if (K0_TOUCH) {
      const int i0 = (pf - 142) * 256 + t;
      size_t o = WS_HB + (size_t)i0 * 65536; o = o < WS_END - 16 ? o : WS_HB;
      const int io = i0 < 40 ? i0 : 0;
      asm volatile("global_load_dwordx4 %0, %1, off" : "=v"(sink[0]) : "v"(ws + o) : "memory");
      asm volatile("global_load_dwordx4 %0, %1, off" : "=v"(sink[1]) : "v"((const char*)outp + (size_t)io * 65536) : "memory");
      asm volatile("s_waitcnt vmcnt(0)" : "+v"(sink[0]), "+v"(sink[1])::"memory");
    }
  } else {
    const int rbase = (b - K0_TILES) * 16 + w * 4;
    u32x4 xa[4], xb[4];
#pragma unroll
    for (int r = 0; r < 4; ++r) {
      xa[r] = *(const u32x4*)(x + (size_t)(rbase + r) * D + lane * 8);
      xb[r] = *(const u32x4*)(x + (size_t)(rbase + r) * D + 512 + (lane & 15) * 8);
    }
    const float mk = lane < 16 ? 1.f : 0.f;
    float mean[4], q[4];
#pragma unroll
    for (int r = 0; r < 4; ++r) {
      float s = 0.f, s2 = 0.f;
#pragma unroll
      for (int j = 0; j < 4; ++j) { s += bflo(xa[r][j]) + bfhi(xa[r][j]); s2 += bflo(xb[r][j]) + bfhi(xb[r][j]); }
      mean[r] = wave_sum(s + mk * s2) * (1.0f / D);
    }
#pragma unroll
    for (int r = 0; r < 4; ++r) {
      float s = 0.f, s2 = 0.f;
#pragma unroll
      for (int j = 0; j < 4; ++j) {
        float a = bflo(xa[r][j]) - mean[r], c = bfhi(xa[r][j]) - mean[r]; s += a * a + c * c;
        a = bflo(xb[r][j]) - mean[r]; c = bfhi(xb[r][j]) - mean[r]; s2 += a * a + c * c;
      }
      q[r] = wave_sum(s + mk * s2) * (1.0f / D);
    }
    STAMP(0, 1);
    if (lane < 2) {
      const float m0 = lane ? mean[2] : mean[0], m1 = lane ? mean[3] : mean[1], v0 = lane ? q[2] : q[0], v1 = lane ? q[3] : q[1];
      const float sd0 = rbf(sqrtf(rbf(rbf(v0) + 1e-5f))), sd1 = rbf(sqrtf(rbf(rbf(v1) + 1e-5f)));
      const f32x4 o = {rbf(m0), 1.0f / sd0, rbf(m1), 1.0f / sd1};
      ST16(rs, WS_ST1 + (size_t)(rbase + 2 * lane) * 8, __builtin_bit_cast(u32x4, o));
    }
    STAMP(0, 2);
  }
  if (b == K0_TILES + K0_STATS - 1) { ST16(rs, WS_FLG + t * 16, ((u32x4){0u, 0u, 0u, 0u})); ST16(rs, WS_FLP + t * 16, ((u32x4){0u, 0u, 0u, 0u})); }
  STAMP_FLUSH(0);
}

#define POLL_SLEEP 1
typedef __attribute__((address_space(1))) unsigned gu32;
__device__ __forceinline__ void publish(char* ws, size_t off) {
  WAIT_VM(0); BAR();
  if (threadIdx.x == 0) __hip_atomic_store((gu32*)(ws + off), 1u, __ATOMIC_RELAXED, __HIP_MEMORY_SCOPE_AGENT);
}
__device__ __forceinline__ void await_flags(char* ws, size_t off, int n, int lane) {
  const unsigned long long t0 = __builtin_amdgcn_s_memrealtime();
  for (;;) {
    bool ok = true;
#pragma unroll
    for (int i = 0; i < 4; ++i) {
      const int idx = lane + 64 * i;
      if (64 * i < n) { const unsigned v = __hip_atomic_load((gu32*)(ws + off + (size_t)(idx < n ? idx : 0) * 16), __ATOMIC_RELAXED, __HIP_MEMORY_SCOPE_AGENT); ok &= v == 1u; }
    }
    if (__all(ok)) break;
    if (__builtin_amdgcn_s_memrealtime() - t0 > 400000ull) break;
    __builtin_amdgcn_s_sleep(POLL_SLEEP);
  }
  asm volatile("" ::: "memory");
}

static constexpr int CH_BYTES = 13568, CH_F = 0, CH_U1 = 4224, CH_U0 = 4224 + 4608 + 32;
static constexpr int K1_YL = 4 * CH_BYTES;
static constexpr int K1_YP = 8192 + 64;
#define K1_DELAY 80
static constexpr int K1_CONV = 160, K1_W1 = 54, K1_W2 = 30;

template <bool W1>
__device__ __forceinline__ void retile_job(LAS char* lds, const u16* __restrict__ src, int pitch, int cg, int mat, int ks0, char* ws, int w, int lane) {
  constexpr int NU = W1 ? 10 : 9;
  const auto rs = MAKE_RSRC(ws);
#pragma unroll
  for (int i = 0; i < NU; ++i) {
    const int pc = w + 8 * i, row = 8 * pc + (lane >> 3);
    dma16(src + (size_t)(ks0 * 32 + row) * pitch + (((lane & 7) ^ (row & 7)) * 8), lds + pc * 1024);
  }
  WAIT_VM(0); BAR();
  const int g = lane >> 4, q = (lane & 15) >> 2, p = lane & 3, fb = w & 3;
  const u32 l0 = (u32)(size_t)lds;
  u16x4 lo[NU], hi[NU];
#pragma unroll
  for (int i = 0; i < NU; ++i) {
    const int ks = (w >> 2) + 2 * i, row0 = 32 * ks + 8 * g + q, row1 = row0 + 4, chk = 2 * fb + (p >> 1);
    lo[i] = tr_read(l0 + row0 * 128 + ((chk ^ (row0 & 7)) * 16) + 8 * (p & 1));
    hi[i] = tr_read(l0 + row1 * 128 + ((chk ^ (row1 & 7)) * 16) + 8 * (p & 1));
  }
#pragma unroll
  for (int i = 0; i < NU; ++i) asm volatile("s_waitcnt lgkmcnt(0)" : "+v"(lo[i]), "+v"(hi[i])::"memory");
  float m = 0.f;
  if (W1) {
    float cs = 0.f;
#pragma unroll
    for (int i = 0; i < NU; ++i) {
      const u32x2 l2 = __builtin_bit_cast(u32x2, lo[i]), h2 = __builtin_bit_cast(u32x2, hi[i]);
      cs = dot2(l2.x, 0x3f803f80u, dot2(l2.y, 0x3f803f80u, dot2(h2.x, 0x3f803f80u, dot2(h2.y, 0x3f803f80u, cs))));
    }
    cs += __shfl_xor(cs, 16); cs += __shfl_xor(cs, 32);
    LAS float* red = (LAS float*)(lds + 80 * 1024);
    if (lane < 16) red[w * 16 + lane] = cs;
    WAIT_LGKM0(); BAR();
    m = (red[fb * 16 + (lane & 15)] + red[(fb + 4) * 16 + (lane & 15)]) * (1.0f / D);
  }
#pragma unroll
  for (int i = 0; i < NU; ++i) {
    const int ks = (w >> 2) + 2 * i;
    const u32x2 l2 = __builtin_bit_cast(u32x2, lo[i]), h2 = __builtin_bit_cast(u32x2, hi[i]);
    if (W1) {
      const u32x4 o = {pack2(bflo(l2.x) - m, bfhi(l2.x) - m), pack2(bflo(l2.y) - m, bfhi(l2.y) - m), pack2(bflo(h2.x) - m, bfhi(h2.x) - m), pack2(bflo(h2.y) - m, bfhi(h2.y) - m)};
      const int blk = cg * 4 + fb;
      ST16(rs, WS_W1T + ((size_t)(blk * NKS1 + ks) * 2 + mat) * 1024 + lane * 16, o);
    } else {
      const int df = cg * 4 + fb, dt = df / 5, j = df % 5;
      ST16(rs, WS_W2T + ((size_t)(dt * NKS2P + ks0 + ks) * 5 + j) * 1024 + lane * 16, ((u32x4){l2.x, l2.y, h2.x, h2.y}));
    }
  }
  if (!W1 && ks0 == 36 && cg < 8) {
#pragma unroll
    for (int i = 0; i < 2; ++i) { const int pc = w + 8 * i; if (pc < 10) ST16(rs, WS_W2T + ((size_t)(cg * NKS2P + NKS2) * 5 + pc) * 1024 + lane * 16, ((u32x4){0u, 0u, 0u, 0u})); }
  }
  if (W1 && cg == 26 && mat == 0) {
#pragma unroll
    for (int i = 0; i < 3; ++i) { const int ks = w + 8 * i; if (ks < NKS1) ST16(rs, WS_W1T + ((size_t)(108 * NKS1 + ks) * 2) * 1024 + lane * 16, ((u32x4){0x3f803f80u, 0x3f803f80u, 0x3f803f80u, 0x3f803f80u})); }
  }
}

#define SARG
#define SPASS

__device__ __forceinline__ void conv_phase(LAS char* lds, const u16* __restrict__ x, const u16* __restrict__ fk, const u16* __restrict__ sD, char* __restrict__ ws, int b, int t, int lane, int w SARG) {
  const auto rs = MAKE_RSRC(ws);
  const int cgp = (b & 7) * 20 + (b >> 3);
  const int c = w & 3, hf = w >> 2, d = cgp * 4 + c;
  LAS char* cb = lds + c * CH_BYTES;
  {
    const char* gF = (const char*)fk + (size_t)d * S * 2 + lane * 16;
    const char* g1 = ws + WS_UR1 + (size_t)d * S * 2 + lane * 16;
#pragma unroll
    for (int i = 0; i < 2; ++i) {
      const int pc = hf * 2 + i;
      dma16(g1 + pc * 1024, cb + CH_U1 + pc * 1024);
      dma16(gF + pc * 1024, cb + CH_F + 64 + pc * 1024);
    }
    if (hf == 0) { if (lane < 32) *(LAS u32x4*)(cb + CH_U1 + 4096 + lane * 16) = (u32x4){0, 0, 0, 0}; if (lane < 4) *(LAS u32x4*)(cb + CH_F + lane * 16) = (u32x4){0, 0, 0, 0}; }
    else { if (lane < 32) *(LAS u32x4*)(cb + CH_U0 + 4096 + lane * 16) = (u32x4){0, 0, 0, 0}; }
  }
  const u32x2 dw = *(const u32x2*)(sD + cgp * 4);
  WAIT_VM(0); WAIT_LGKM0(); BAR();
#pragma unroll
  for (int i = 0; i < 2; ++i) {
    const int p0 = 1024 * hf + 512 * i + 8 * lane;
    const u32x4 in = *(LAS const u32x4*)(cb + CH_U1 + p0 * 2); const u32 nx = *(LAS const u32*)(cb + CH_U1 + p0 * 2 + 16);
    *(LAS u32x4*)(cb + CH_U0 + p0 * 2) = (u32x4){__builtin_amdgcn_alignbit(in.y, in.x, 16), __builtin_amdgcn_alignbit(in.z, in.y, 16), __builtin_amdgcn_alignbit(in.w, in.z, 16), __builtin_amdgcn_alignbit(nx, in.w, 16)};
  }
  WAIT_LGKM0(); BAR();
  STAMP(1, 1);
  u32x2 xr[4]; u32x4 stv[2];
  const char* gx = (const char*)x + (size_t)(2 * t) * D * 2 + cgp * 8;
#define C_LDX(i) do { \
      if ((i) == 0) asm volatile("global_load_dwordx2 %0, %1, off" : "=v"(xr[0]) : "v"(gx) : "memory"); \
      if ((i) == 1) asm volatile("global_load_dwordx2 %0, %1, off offset:1280" : "=v"(xr[1]) : "v"(gx) : "memory"); \
      if ((i) == 2) asm volatile("global_load_dwordx2 %0, %1, off" : "=v"(xr[2]) : "v"(gx + (size_t)1024 * D * 2) : "memory"); \
      if ((i) == 3) asm volatile("global_load_dwordx2 %0, %1, off offset:1280" : "=v"(xr[3]) : "v"(gx + (size_t)1024 * D * 2) : "memory"); \
      if ((i) == 4) GLD16(stv[0], t * 16, ws + WS_ST1, 0); \
      if ((i) == 5) GLD16(stv[1], t * 16, ws + WS_ST1 + 8192, 0); } while (0)
#define C_XWAIT() asm volatile("s_waitcnt vmcnt(0) lgkmcnt(0)" : "+v"(xr[0]), "+v"(xr[1]), "+v"(xr[2]), "+v"(xr[3]), "+v"(stv[0]), "+v"(stv[1])::"memory")
  if (hf == 0) {
    const int r = lane & 15, g = lane >> 4;
    f32x4 acc[8];
    const int a = r >> 1, e = r & 1;
    const u32 ub = (u32)(size_t)(cb + (e ? CH_U1 : CH_U0) + 448 - 64 * a + 16 * g);
    u32 fbp[4];
#pragma unroll
    for (int i = 0; i < 4; ++i) fbp[i] = (u32)(size_t)(cb + CH_F + 16 * g + 4 * r + 4) + 1024 * i;
    u32x4 U[2][8]; u32x2 Fr[2][8][2];
#define C_RDU(buf, i, cc) DS_B128(U[buf][i], ub, 512 * (7 - (i)) + 64 * (cc))
#define C_RDF(buf, i, cc, hh) asm volatile("ds_read2_b32 %0, %1 offset0:%2 offset1:%3" : "=v"(Fr[buf][i][hh]) : "v"(fbp[(i) >> 1]), "n"(128 * ((i) & 1) + 16 * (cc) + 2 * (hh)), "n"(128 * ((i) & 1) + 16 * (cc) + 2 * (hh) + 1))
#define C_F(buf, i) ((u32x4){Fr[buf][i][0].x, Fr[buf][i][0].y, Fr[buf][i][1].x, Fr[buf][i][1].y})
#define C_WAIT(buf) do { \
      asm volatile("s_waitcnt lgkmcnt(0)" : "+v"(U[buf][0]), "+v"(U[buf][1]), "+v"(U[buf][2]), "+v"(U[buf][3]), "+v"(U[buf][4]), "+v"(U[buf][5]), "+v"(U[buf][6]), "+v"(U[buf][7])::"memory"); \
      asm volatile("" : "+v"(Fr[buf][0][0]), "+v"(Fr[buf][0][1]), "+v"(Fr[buf][1][0]), "+v"(Fr[buf][1][1]), "+v"(Fr[buf][2][0]), "+v"(Fr[buf][2][1]), "+v"(Fr[buf][3][0]), "+v"(Fr[buf][3][1]), \
                        "+v"(Fr[buf][4][0]), "+v"(Fr[buf][4][1]), "+v"(Fr[buf][5][0]), "+v"(Fr[buf][5][1]), "+v"(Fr[buf][6][0]), "+v"(Fr[buf][6][1]), "+v"(Fr[buf][7][0]), "+v"(Fr[buf][7][1])); } while (0)
#pragma unroll
    for (int i = 0; i < 8; ++i) { C_RDF(0, i, 0, 0); C_RDF(0, i, 0, 1); C_RDU(0, i, 0); }
    C_WAIT(0);
#pragma unroll
    for (int cc = 0; cc < 8; ++cc) {
      const int cur = cc & 1, nxt = cur ^ 1;
      int n = 0;
#pragma unroll
      for (int c1 = 0; c1 < 8; ++c1)
#pragma unroll
        for (int i = c1; i < 8; ++i) {
          if (cc == 0 && c1 == 0) MFMA_Z(acc[i], U[cur][i - c1], C_F(cur, c1)); else MFMA_IP(acc[i], U[cur][i - c1], C_F(cur, c1));
          if (cc < 7 && n < 24) { const int j = n / 3, kk = n % 3; if (kk == 0) C_RDU(nxt, j, cc + 1); else C_RDF(nxt, j, cc + 1, kk - 1); }
          if (cc < 6 && n == 28) C_LDX(cc);
          ++n;
        }
      if (cc < 7) C_WAIT(nxt);
    }
    asm volatile("s_nop 15\n\ts_nop 15" ::: "memory");
    STAMP(1, 2);
    LAS char* yl = lds + K1_YL + c * K1_YP;
#pragma unroll
    for (int j = 0; j < 8; ++j)
#pragma unroll
      for (int hh = 0; hh < 2; ++hh) *(LAS f32x2*)(yl + (256 * j + 32 * (2 * g + hh) + 2 * r) * 4) = (f32x2){acc[j][2 * hh], acc[j][2 * hh + 1]};
    C_XWAIT();
  } else {
    C_LDX(0); C_LDX(1); C_LDX(2); C_LDX(3); C_LDX(4); C_LDX(5);
    C_XWAIT();
  }
  BAR();
  STAMP(1, 3);
#pragma unroll
  for (int i = 0; i < 2; ++i) {
    const int s = 2 * t + 1024 * i;
    f32x2 y2[4];
    const float Dd[4] = {bflo(dw.x), bfhi(dw.x), bflo(dw.y), bfhi(dw.y)};
#pragma unroll
    for (int cc = 0; cc < 4; ++cc) {
      const f32x2 y = *(LAS const f32x2*)(lds + K1_YL + cc * K1_YP + s * 4);
      const u32 uw = *(LAS const u32*)(lds + cc * CH_BYTES + CH_U1 + (2046 - s) * 2);
      y2[cc] = (f32x2){y[0] + bfhi(uw) * Dd[cc], y[1] + bflo(uw) * Dd[cc]};
    }
    const f32x4 st = __builtin_bit_cast(f32x4, stv[i]);
    const u32x2 x0 = xr[2 * i], x1 = xr[2 * i + 1];
    const float xa[4] = {bflo(x0.x), bfhi(x0.x), bflo(x0.y), bfhi(x0.y)}, xb[4] = {bflo(x1.x), bfhi(x1.x), bflo(x1.y), bfhi(x1.y)};
    float ha[4], hb[4];
#pragma unroll
    for (int cc = 0; cc < 4; ++cc) { ha[cc] = xa[cc] + (xa[cc] - st[0]) * st[1] * y2[cc][0]; hb[cc] = xb[cc] + (xb[cc] - st[2]) * st[3] * y2[cc][1]; }
    ST16(rs, WS_HB + ((size_t)cgp * S + s) * 8, ((u32x4){pack2(ha[0], ha[1]), pack2(ha[2], ha[3]), pack2(hb[0], hb[1]), pack2(hb[2], hb[3])}));
  }
  STAMP(1, 4);
}

#define DMA_SC1(m0v, voffv, sbase, off) asm volatile("s_mov_b32 m0, %0\n\ts_nop 0\n\tglobal_load_lds_dwordx4 %1, %2 offset:%3 sc1" :: "s"(m0v), "v"(voffv), "s"(sbase), "n"(off) : "memory")
#define GLD16C(dst, voff, sbase, off) asm volatile("global_load_dwordx4 %0, %1, %2 offset:%3 sc1" : "=v"(dst) : "v"(voff), "s"(sbase), "n"(off) : "memory")
static constexpr int G1_NS = 12, G1_SLOT = 8192, G1_WR = 4, G1_RING = 64 * 1024;
static constexpr int KF_CNT = 60 * 1024;
__host__ __device__ constexpr int g2_slot(int kq, int s) { return s < 3 ? kq * 15360 + s * 5120 : G1_RING + kq * 20480 + (s - 3) * 5120; }
__device__ __forceinline__ void gemm2_prefetch(LAS char* lds, char* __restrict__ ws, int dt, int lane, int first, int last, int step) {
  const u32 lds0 = (u32)(size_t)lds, voff = lane * 16;
  const char* wb = ws + WS_W2T + (size_t)dt * NKS2P * 5 * 1024;
  for (int pc = first; pc < last; pc += step) {
    int kq, o, dst;
    if (pc < 60) { kq = pc / 15; o = (pc - 15 * kq) * 1024; dst = kq * 15360 + o; }
    else { const int q = pc - 60; kq = q / 20; o = 15360 + (q - 20 * kq) * 1024; dst = G1_RING + kq * 20480 + (q - 20 * kq) * 1024; }
    const u32 m0v = __builtin_amdgcn_readfirstlane(lds0 + dst);
    const char* sb = wb + kq * 14 * 5120 + o;
    DMA_SC1(m0v, voff, sb, 0);
  }
}
__device__ __forceinline__ void gemm2_wq_issue(char* __restrict__ ws, int dt, int lane, int w, u32x4 (&wq)[7][3]) {
  const char* wsb = ws + WS_W2T + ((size_t)dt * NKS2P + (w >> 1) * 14 + 7) * 5120 + (w & 1) * 3072;
  const u32 voff = lane * 16;
  const int sh = __builtin_amdgcn_readfirstlane(w & 1);
#pragma unroll
  for (int i = 0; i < 7; ++i) {
    GLD16C(wq[i][0], voff, wsb + i * 5120, 0); GLD16C(wq[i][1], voff, wsb + i * 5120, 1024);
    asm volatile("s_cmp_lg_u32 %3, 0\n\ts_cbranch_scc1 1f\n\tglobal_load_dwordx4 %0, %1, %2 offset:2048 sc1\n1:" : "=&v"(wq[i][2]) : "v"(voff), "s"(wsb + i * 5120), "s"(sh) : "scc", "memory");
  }
}
__device__ __forceinline__ void gemm2_wq_wait(u32x4 (&wq)[7][3]) {
  asm volatile("s_waitcnt vmcnt(0)" : "+v"(wq[0][0]), "+v"(wq[0][1]), "+v"(wq[0][2]), "+v"(wq[1][0]), "+v"(wq[1][1]), "+v"(wq[1][2]), "+v"(wq[2][0]), "+v"(wq[2][1]), "+v"(wq[2][2]),
                                      "+v"(wq[3][0]), "+v"(wq[3][1]), "+v"(wq[3][2])::"memory");
  asm volatile("" : "+v"(wq[4][0]), "+v"(wq[4][1]), "+v"(wq[4][2]), "+v"(wq[5][0]), "+v"(wq[5][1]), "+v"(wq[5][2]), "+v"(wq[6][0]), "+v"(wq[6][1]), "+v"(wq[6][2]));
}

__device__ __forceinline__ void gemm1_phase(LAS char* lds, char* __restrict__ ws, int rt, int ct, int dt2, int lane, int w, unsigned tag, size_t flag_off, u32x4 (&wq)[7][3] SARG) {
  const u32 lds0 = (u32)(size_t)lds + G1_RING;
  const int s0 = rt * 128;
  const u32 voff = lane * 16;
  const auto rs = MAKE_RSRC(ws);
  if (w >= 4) {
    const int j = w - 4;
    const u32 hv = voff ^ ((j & 1) * 128);
    const char* hsb = ws + WS_HB + ((size_t)(2 * j) * S + s0) * 8;
    const u32 ldsw = lds0 + 2 * j * 1024;
#define G1_SLAB(ks) do { DMA_SC1(ldsw + ((ks) % G1_NS) * G1_SLOT, hv, hsb + (size_t)(ks) * 8 * S * 8, 0); DMA_SC1(ldsw + ((ks) % G1_NS) * G1_SLOT + 1024, hv, hsb + (size_t)(ks) * 8 * S * 8 + S * 8, 0); } while (0)
    G1_SLAB(0); G1_SLAB(1); G1_SLAB(2); G1_SLAB(3); G1_SLAB(4);
    WAIT_VM(8); BAR();
    G1_SLAB(5); G1_SLAB(6); G1_SLAB(7); G1_SLAB(8);
    WAIT_VM(8); BAR();
    gemm2_prefetch(lds, ws, dt2, lane, j, 20, 4);
    G1_SLAB(9); G1_SLAB(10); G1_SLAB(11);
    WAIT_VM(6); BAR();
    gemm2_prefetch(lds, ws, dt2, lane, 20 + j, 40, 4);
    G1_SLAB(12); G1_SLAB(13); G1_SLAB(14); G1_SLAB(15);
    WAIT_VM(6); BAR();
    gemm2_prefetch(lds, ws, dt2, lane, 40 + j, 60, 4);
    G1_SLAB(16); G1_SLAB(17); G1_SLAB(18); G1_SLAB(19);
    WAIT_VM(6); BAR();
    gemm2_wq_issue(ws, dt2, lane, w, wq);
    WAIT_VM(14); BAR();
    BAR();
    gemm2_prefetch(lds, ws, dt2, lane, 60 + j, 140, 4);
    gemm2_wq_wait(wq);
  } else {
    const int blk0 = ct * 8 + 2 * w;
    const char* wsA = ws + WS_W1T + (size_t)(blk0 > 108 ? 108 : blk0) * NKS1 * 2048;
    const char* wsB = ws + WS_W1T + (size_t)(blk0 + 1 > 108 ? 108 : blk0 + 1) * NKS1 * 2048;
    const int g = lane >> 4;
    const u32 hbase = lds0 + g * 2048 + (lane & 15) * 8;
    const u32 hoffE = hbase + (g & 1) * 128, hoffO = hbase + (1 - (g & 1)) * 128 - 128;
    const u32 hoffE2 = hoffE + 65536, hoffO2 = hoffO + 65536;
    f32x4 acc[8][4];
    const int stat = __builtin_amdgcn_readfirstlane(blk0 == 108 ? 1 : 0);
#define G1_LDW(ks, i) do { if ((i) == 0) GLD16C(wr[(ks) % G1_WR][0], voff, wsA + (ks) * 2048, 0); if ((i) == 1) GLD16C(wr[(ks) % G1_WR][1], voff, wsA + (ks) * 2048, 1024); \
      if ((i) == 2) GLD16C(wr[(ks) % G1_WR][2], voff, wsB + (ks) * 2048, 0); if ((i) == 3) GLD16C(wr[(ks) % G1_WR][3], voff, wsB + (ks) * 2048, 1024); } while (0)
#define G1_RD(sf, hh, ks) DS_B64(hf[sf][hh], ((ks) % G1_NS) >= 8 ? (((sf) & 1) ? hoffO2 : hoffE2) : (((sf) & 1) ? hoffO : hoffE), (((ks) % G1_NS) & 7) * G1_SLOT + (hh) * 1024 + (sf) * 128)
#define G1_H(sf) ((u32x4){hf[sf][0].x, hf[sf][0].y, hf[sf][1].x, hf[sf][1].y})
#define G1_MM(ks, sf, i, STAT) do { \
      if (STAT) { if ((i) == 0) { if ((ks) == 0) MFMA_Z(acc[sf][0], wr[0][0], G1_H(sf)); else MFMA_IP(acc[sf][0], wr[(ks) % G1_WR][0], G1_H(sf)); } \
                  if ((i) == 2) { if ((ks) == 0) MFMA_Z(acc[sf][2], G1_H(sf), G1_H(sf)); else MFMA_IP(acc[sf][2], G1_H(sf), G1_H(sf)); } } \
      else { if ((ks) == 0) MFMA_Z(acc[sf][i], wr[0][i], G1_H(sf)); else MFMA_IP(acc[sf][i], wr[(ks) % G1_WR][i], G1_H(sf)); } } while (0)
#define G1_BODY(STAT) { \
      u32x4 wr[G1_WR][4]; u32x2 hf[8][2]; \
      _Pragma("unroll") for (int ks = 0; ks < 3; ++ks) { G1_LDW(ks, 0); G1_LDW(ks, 1); G1_LDW(ks, 2); G1_LDW(ks, 3); } \
      BAR();                                                   \
      STAMP(1, 7); \
      _Pragma("unroll") for (int sf = 0; sf < 7; ++sf) { G1_RD(sf, 0, 0); G1_RD(sf, 1, 0); } \
      _Pragma("unroll") for (int ks = 0; ks < NKS1; ++ks) { \
        if ((ks & 3) == 0) BAR(); \
          \
        if (ks + 2 < NKS1) WAIT_VM(8); else if (ks + 1 < NKS1) WAIT_VM(4); else WAIT_VM(0); \
        asm volatile("" : "+v"(wr[ks % G1_WR][0]), "+v"(wr[ks % G1_WR][1]), "+v"(wr[ks % G1_WR][2]), "+v"(wr[ks % G1_WR][3])); \
        _Pragma("unroll") for (int sf = 0; sf < 8; ++sf) { \
            \
          if (ks + 1 < NKS1 || sf == 0) asm volatile("s_waitcnt lgkmcnt(12)" : "+v"(hf[sf][0]), "+v"(hf[sf][1])::"memory"); \
          else if (sf < 7) asm volatile("s_waitcnt lgkmcnt(%2)" : "+v"(hf[sf][0]), "+v"(hf[sf][1]) : "n"(14 - 2 * sf) : "memory"); \
          else asm volatile("s_waitcnt lgkmcnt(0)" : "+v"(hf[sf][0]), "+v"(hf[sf][1])::"memory"); \
          G1_MM(ks, sf, 0, STAT); \
          if (sf == 0) G1_RD(7, 0, ks); else if (ks + 1 < NKS1) G1_RD(sf - 1, 0, ks + 1); \
          G1_MM(ks, sf, 1, STAT); \
          if (sf == 0) G1_RD(7, 1, ks); else if (ks + 1 < NKS1) G1_RD(sf - 1, 1, ks + 1); \
          G1_MM(ks, sf, 2, STAT); \
          if ((sf & 1) && ks + 3 < NKS1) G1_LDW(ks + 3, sf >> 1); \
          G1_MM(ks, sf, 3, STAT); \
        } } }
    if (stat) G1_BODY(true) else G1_BODY(false)
    BAR();
    asm volatile("s_nop 15\n\ts_nop 15" ::: "memory");
    STAMP(1, 8);
    if (stat) {
      const int sx = lane & 15;
#pragma unroll
      for (int sp = 0; sp < 2; ++sp) {
        float r2[4];
#pragma unroll
        for (int q = 0; q < 4; ++q) {
          const int sf = 4 * sp + q;
          const float dg = (sx & 2) ? ((sx & 1) ? acc[sf][2][3] : acc[sf][2][2]) : ((sx & 1) ? acc[sf][2][1] : acc[sf][2][0]);
          const float ssq = __shfl(dg, (sx >> 2) * 16 + sx);
          const float mean = acc[sf][0][0] * (1.0f / D);
          const float var = fmaxf(ssq * (1.0f / D) - mean * mean, 0.f);
          const float r = 1.0f / rbf(sqrtf(rbf(rbf(var) + 1e-5f)));
          r2[q] = r * r;
        }
        const float mine = g == 0 ? r2[0] : (g == 1 ? r2[1] : (g == 2 ? r2[2] : r2[3]));
        const int b4 = lane & ~3;
        const f32x4 o = {__shfl(mine, b4), __shfl(mine, b4 + 1), __shfl(mine, b4 + 2), __shfl(mine, b4 + 3)};
        if ((lane & 3) == 0) ST16(rs, WS_ST2 + (size_t)(s0 + 64 * sp + lane) * 4, __builtin_bit_cast(u32x4, o));
      }
    } else {
#pragma unroll
      for (int bi = 0; bi < 2; ++bi) {
        const int blk = blk0 + bi;
        if (blk < 108) {
          const u32 obase = (u32)(WS_HF + ((size_t)(rt * 8 + (lane >> 5)) * NKS2 + (blk >> 1)) * 1024) + (blk & 1) * 512 + (lane & 31) * 16;
#pragma unroll
          for (int pr = 0; pr < 4; ++pr) {
            const f32x4 z4 = {0.f, 0.f, 0.f, 0.f};
            const f32x4 hE = __builtin_elementwise_max(acc[2 * pr][2 * bi], z4) * acc[2 * pr][2 * bi + 1], hO = __builtin_elementwise_max(acc[2 * pr + 1][2 * bi], z4) * acc[2 * pr + 1][2 * bi + 1];
            u32 e0 = pack2(hE[0], hE[1]), e1 = pack2(hE[2], hE[3]), o0 = pack2(hO[0], hO[1]), o1 = pack2(hO[2], hO[3]);
            auto r0 = __builtin_amdgcn_permlane32_swap(e0, o0, false, false); auto r0b = __builtin_amdgcn_permlane16_swap(r0[0], r0[1], false, false);
            auto r1 = __builtin_amdgcn_permlane32_swap(e1, o1, false, false); auto r1b = __builtin_amdgcn_permlane16_swap(r1[0], r1[1], false, false);
            ST16(rs, obase + pr * 2 * NKS2 * 1024, ((u32x4){r0b[0], r1b[0], r0b[1], r1b[1]}));
          }
        }
      }
    }
    WAIT_VM(0);
    unsigned old = 0;
    if (lane == 0) old = __hip_atomic_fetch_add((LAS unsigned*)(lds + KF_CNT), 1u, __ATOMIC_RELAXED, __HIP_MEMORY_SCOPE_WORKGROUP);
    if (lane == 0 && old == 3u) __hip_atomic_store((gu32*)(ws + flag_off), tag, __ATOMIC_RELAXED, __HIP_MEMORY_SCOPE_AGENT);
    STAMP(1, 9);
    gemm2_wq_issue(ws, dt2, lane, w, wq);
    gemm2_wq_wait(wq);
  }
}

__device__ __forceinline__ void gemm2_phase(LAS char* lds, char* __restrict__ ws, u16* __restrict__ out, int rt, int dt, int lane, int w, u32x4 (&wq)[7][3] SARG) {
  const int sh = w & 1, kq = w >> 1, g = lane >> 4;
  const u32 voff = lane * 16, lds0 = (u32)(size_t)lds;
  const char* hA = ws + WS_HF + ((size_t)(rt * 4 + 2 * sh) * NKS2 + kq * 14) * 1024;
  const char* hB = hA + (size_t)NKS2 * 1024;
  const int hclamp = kq == 3 ? 1 : 0;
  u32x4 hq[6][2], wf[2][5];
#define G2_LH(n) do { const int o_ = ((n) >= 12 && hclamp) ? 11 * 1024 : (n) * 1024; GLD16C(hq[(n) % 6][0], voff, hA + o_, 0); GLD16C(hq[(n) % 6][1], voff, hB + o_, 0); } while (0)
  const u32 rlo = lds0 + kq * 15360 + lane * 16, rhi = lds0 + G1_RING + kq * 20480 - 3 * 5120 + lane * 16;
#define G2_SB(n) ((((n) % 7) < 3 ? rlo : rhi))
#define G2_READ(n) do { DS_B128(wf[(n) & 1][0], G2_SB(n), ((n) % 7) * 5120); DS_B128(wf[(n) & 1][1], G2_SB(n), ((n) % 7) * 5120 + 1024); DS_B128(wf[(n) & 1][2], G2_SB(n), ((n) % 7) * 5120 + 2048); \
    DS_B128(wf[(n) & 1][3], G2_SB(n), ((n) % 7) * 5120 + 3072); DS_B128(wf[(n) & 1][4], G2_SB(n), ((n) % 7) * 5120 + 4096); } while (0)
  const int shs = __builtin_amdgcn_readfirstlane(sh);
#define G2_WST(n, i) do { if ((i) < 2) asm volatile("ds_write_b128 %0, %1 offset:%2" :: "v"(G2_SB(n) + sh * 3072), "v"(wq[(n) - 7][i]), "n"(((n) % 7) * 5120 + (i) * 1024) : "memory"); \
    else asm volatile("s_cmp_lg_u32 %3, 0\n\ts_cbranch_scc1 1f\n\tds_write_b128 %0, %1 offset:%2\n1:" :: "v"(G2_SB(n)), "v"(wq[(n) - 7][2]), "n"(((n) % 7) * 5120 + 2048), "s"(shs) : "scc", "memory"); } while (0)
  G2_LH(0); G2_LH(1);
  float r2[3]; u32x2 res[3];
#pragma unroll
  for (int j = 0; j < 3; ++j) {
    const int f = kq + 4 * j < 10 ? kq + 4 * j : kq, srow = rt * 64 + (2 * sh + f / 5) * 16 + (lane & 15);
    asm volatile("global_load_dword %0, %1, off sc1" : "=v"(r2[j]) : "v"(ws + WS_ST2 + (size_t)srow * 4) : "memory");
    asm volatile("global_load_dwordx2 %0, %1, off sc1" : "=v"(res[j]) : "v"(ws + WS_HB + ((size_t)(dt * 20 + (f % 5) * 4 + g) * S + srow) * 8) : "memory");
  }
  G2_LH(2); G2_LH(3); G2_LH(4);
  f32x4 acc[10];
  G2_READ(0);
  WAIT_VM(14);
  STAMP(1, 11);
#define G2_MM(n, m) do { if ((n) == 0) MFMA_Z(acc[m], wf[0][(m) % 5], hq[0][(m) / 5]); else MFMA_IP(acc[m], wf[(n) & 1][(m) % 5], hq[(n) % 6][(m) / 5]); } while (0)
#define G2D(k) do {} while (0)
  u32 g2d[30];
#define G2_STEP(n) do { G2D(2 * (n)); \
    if ((n) == 1) WAIT_VM(14); else if ((n) > 1) { constexpr int iss_ = 5 + (n) < 14 ? 5 + (n) : 14; WAIT_VM(2 * (iss_ - ((n) + 1))); } \
    asm volatile("s_waitcnt lgkmcnt(0)" : "+v"(wf[(n) & 1][0]), "+v"(wf[(n) & 1][1]), "+v"(wf[(n) & 1][2]), "+v"(wf[(n) & 1][3]), "+v"(wf[(n) & 1][4]), "+v"(hq[(n) % 6][0]), "+v"(hq[(n) % 6][1])::"memory"); \
    if (((n) & 1) && (n) <= 9) BAR(); \
    G2D(2 * (n) + 1); \
    if ((n) + 1 < 14) G2_READ(((n) + 1 < 14 ? (n) + 1 : 0)); \
    G2_MM(n, 0); G2_MM(n, 1); if ((n) + 5 < 14) G2_LH(((n) + 5 < 14 ? (n) + 5 : 0)); \
    G2_MM(n, 2); G2_MM(n, 3); if ((n) >= 1 && (n) <= 7) G2_WST(((n) >= 1 && (n) <= 7 ? (n) + 6 : 7), 0); \
    G2_MM(n, 4); G2_MM(n, 5); G2_MM(n, 6); if ((n) >= 1 && (n) <= 7) G2_WST(((n) >= 1 && (n) <= 7 ? (n) + 6 : 7), 1); \
    G2_MM(n, 7); G2_MM(n, 8); if ((n) >= 1 && (n) <= 7) G2_WST(((n) >= 1 && (n) <= 7 ? (n) + 6 : 7), 2); \
    G2_MM(n, 9); } while (0)
  G2_STEP(0); G2_STEP(1); G2_STEP(2); G2_STEP(3); G2_STEP(4); G2_STEP(5); G2_STEP(6);
  G2_STEP(7); G2_STEP(8); G2_STEP(9); G2_STEP(10); G2_STEP(11); G2_STEP(12); G2_STEP(13);
  asm volatile("s_nop 15\n\ts_nop 15" ::: "memory");
  asm volatile("" : "+v"(r2[0]), "+v"(r2[1]), "+v"(r2[2]), "+v"(res[0]), "+v"(res[1]), "+v"(res[2]));
  STAMP(1, 12);
  BAR();
  LAS char* xb = lds + sh * 40960 + lane * 16;
#pragma unroll
  for (int f = 0; f < 10; ++f) if (kq != (f & 3)) *(LAS f32x4*)(xb + (f * 4 + kq) * 1024) = acc[f];
  WAIT_LGKM0(); BAR();
  u32 pk[3][2];
#define G2_FIN(KQ) do { _Pragma("unroll") for (int j = 0; j < 3; ++j) { \
      constexpr int kq_ = (KQ); const int f = kq_ + 4 * j < 10 ? kq_ + 4 * j : kq_; \
      f32x4 a = acc[f]; \
      _Pragma("unroll") for (int q = 0; q < 4; ++q) if (q != kq_) a += *(LAS const f32x4*)(xb + (f * 4 + q) * 1024); \
      pk[j][0] = pack2(bflo(res[j].x) + a[0] * r2[j], bfhi(res[j].x) + a[1] * r2[j]); \
      pk[j][1] = pack2(bflo(res[j].y) + a[2] * r2[j], bfhi(res[j].y) + a[3] * r2[j]); } } while (0)
  if (kq == 0) G2_FIN(0); else if (kq == 1) G2_FIN(1); else if (kq == 2) G2_FIN(2); else G2_FIN(3);
  const auto ro = MAKE_RSRC(out);
  {
    auto r0 = __builtin_amdgcn_permlane32_swap(pk[0][0], pk[1][0], false, false); auto r0b = __builtin_amdgcn_permlane16_swap(r0[0], r0[1], false, false);
    auto r1 = __builtin_amdgcn_permlane32_swap(pk[0][1], pk[1][1], false, false); auto r1b = __builtin_amdgcn_permlane16_swap(r1[0], r1[1], false, false);
    const int f = kq + 4 * (lane >> 5);
    ST16(ro, ((size_t)(rt * 64 + (2 * sh + f / 5) * 16 + (lane & 15)) * D + dt * 80 + (f % 5) * 16 + ((lane >> 4) & 1) * 8) * 2, ((u32x4){r0b[0], r1b[0], r0b[1], r1b[1]}));
  }
  if (kq < 2) {
    auto r0 = __builtin_amdgcn_permlane32_swap(pk[2][0], pk[2][0], false, false); auto r0b = __builtin_amdgcn_permlane16_swap(r0[0], r0[1], false, false);
    auto r1 = __builtin_amdgcn_permlane32_swap(pk[2][1], pk[2][1], false, false); auto r1b = __builtin_amdgcn_permlane16_swap(r1[0], r1[1], false, false);
    const int f = kq + 8;
    if (lane < 32) ST16(ro, ((size_t)(rt * 64 + (2 * sh + f / 5) * 16 + (lane & 15)) * D + dt * 80 + (f % 5) * 16 + ((lane >> 4) & 1) * 8) * 2, ((u32x4){r0b[0], r1b[0], r0b[1], r1b[1]}));
  }
}

static constexpr int KF_LDS = 160 * 1024;
__global__ __launch_bounds__(512, 2) void kf_fused(const u16* __restrict__ x, const u16* __restrict__ fk, const u16* __restrict__ sD, const u16* __restrict__ wg,
                                                   const u16* __restrict__ wu, const u16* __restrict__ wd, char* __restrict__ ws, u16* __restrict__ out) {
  extern __shared__ __attribute__((aligned(16))) char shm[];
  LAS char* lds = (LAS char*)shm;
  const int t = threadIdx.x, lane = t & 63, w = __builtin_amdgcn_readfirstlane(t >> 6), b = blockIdx.x;
  STAMP_DECL; STAMP(1, 0);
  if (b < K1_CONV) conv_phase(lds, x, fk, sD, ws, b, t, lane, w SPASS);
  else {
    { const unsigned long long t0 = __builtin_amdgcn_s_memrealtime(); while (__builtin_amdgcn_s_memrealtime() - t0 < K1_DELAY) __builtin_amdgcn_s_sleep(4); }
    const int jb = b - K1_CONV;
    if (jb < K1_W1) retile_job<true>(lds, ((jb & 1) ? wu : wg) + (jb >> 1) * 64, F, jb >> 1, jb & 1, 0, ws, w, lane);
    else if (jb < K1_W1 + K1_W2) { const int j2 = jb - K1_W1; retile_job<false>(lds, wd + (j2 / 3) * 64, D, j2 / 3, 0, (j2 % 3) * 18, ws, w, lane); }
  }
  publish(ws, WS_FLG + (size_t)b * 16);
  STAMP(1, 5);
  const int xg = b & 7, ix = b >> 3;
  const int p = (xg & 3) * 4 + (ix & 3), j = (xg >> 2) * 8 + (ix >> 2);
  if (t == 0) *(LAS unsigned*)(lds + KF_CNT) = 0u;
  if (w == 7) await_flags(ws, WS_FLG, K1_CONV + K1_W1 + K1_W2, lane);
  WAIT_LGKM0(); BAR();
  STAMP(1, 6);
  u32x4 wq[7][3];
  if (j < 14) gemm1_phase(lds, ws, p, j, j >> 1, lane, w, 1u, WS_FLP + (size_t)(p * 16 + j) * 16, wq SPASS);
  else {
    gemm2_prefetch(lds, ws, j >> 1, lane, w, 140, 8);
    gemm2_wq_issue(ws, j >> 1, lane, w, wq);
    gemm2_wq_wait(wq);
  }
  if (w == 4) await_flags(ws, WS_FLP + (size_t)p * 256, 14, lane);
  BAR();
  STAMP(1, 10);
  gemm2_phase(lds, ws, out, p * 2 + (j & 1), j >> 1, lane, w, wq SPASS);
  STAMP(1, 13);
  STAMP_FLUSH(1);
}

extern "C" void kernel_launch(void* const* d_in, const int* in_sizes, int n_in, void* d_out, int out_size, void* d_ws, size_t ws_size, hipStream_t stream) {
  const u16* q = (const u16*)d_in[0]; const u16* k = (const u16*)d_in[1]; const u16* v = (const u16*)d_in[2];
  const u16* shk = (const u16*)d_in[3]; const u16* fk = (const u16*)d_in[4]; const u16* sD = (const u16*)d_in[5];
  const u16* wg = (const u16*)d_in[7]; const u16* wu = (const u16*)d_in[8]; const u16* wd = (const u16*)d_in[9];
  char* ws = (char*)d_ws;
  (void)hipFuncSetAttribute((const void*)k0_prep, hipFuncAttributeMaxDynamicSharedMemorySize, K0_LDS);
  (void)hipFuncSetAttribute((const void*)kf_fused, hipFuncAttributeMaxDynamicSharedMemorySize, KF_LDS);
  k0_prep<<<K0_TILES + K0_STATS + K0_NPF, 256, K0_LDS, stream>>>(q, k, v, shk, fk, wg, wu, wd, (const u16*)d_out, ws);
  kf_fused<<<256, 512, KF_LDS, stream>>>(q, fk, sD, wg, wu, wd, ws, (u16*)d_out);
}
```

```cpp
#include <hip/hip_runtime.h>
#include <hip/hip_bf16.h>

typedef unsigned short u16;
typedef unsigned int u32;
typedef __attribute__((ext_vector_type(2))) unsigned int u32x2;
typedef __attribute__((ext_vector_type(4))) unsigned int u32x4;
typedef __attribute__((ext_vector_type(8))) short bf16x8;
typedef __attribute__((ext_vector_type(2))) __bf16 bf16x2;
typedef __attribute__((ext_vector_type(2))) float f32x2;
typedef __attribute__((ext_vector_type(4))) float f32x4;
typedef __attribute__((ext_vector_type(4))) unsigned short u16x4;
#define LAS __attribute__((address_space(3)))

static constexpr int S = 2048, D = 640, F = 1728;
static constexpr int NKS1 = D / 32;
static constexpr int NKS2 = F / 32;

static constexpr size_t WS_UR1 = 0;
static constexpr size_t WS_HB  = WS_UR1 + (size_t)D * S * 2;
static constexpr size_t WS_ST1 = WS_HB  + (size_t)D * S * 2;
static constexpr size_t WS_ST2 = WS_ST1 + (size_t)S * 8;
static constexpr size_t WS_W1T = WS_ST2 + (size_t)16 * 1024;
static constexpr int NKS2P = 56;
static constexpr size_t WS_W2T = WS_W1T + (size_t)14 * 8 * NKS1 * 2048;
static constexpr size_t WS_HF  = WS_W2T + (size_t)8 * NKS2P * 5 * 1024;
static constexpr size_t WS_FLG = WS_HF  + (size_t)128 * NKS2 * 1024;
static constexpr size_t WS_FLP = WS_FLG + 4096;
static constexpr size_t WS_END = WS_FLP + 4096;

__device__ __forceinline__ float bflo(u32 w) { return __uint_as_float(w << 16); }
__device__ __forceinline__ float bfhi(u32 w) { return __uint_as_float(w & 0xffff0000u); }
__device__ __forceinline__ u32 pack2(float lo, float hi) { f32x2 v = {lo, hi}; bf16x2 b = __builtin_convertvector(v, bf16x2); return __builtin_bit_cast(u32, b); }
__device__ __forceinline__ float rbf(float x) { return bflo(pack2(x, 0.f)); }
__device__ __forceinline__ float dot2(u32 a, u32 b, float c) { return __builtin_amdgcn_fdot2_f32_bf16(__builtin_bit_cast(bf16x2, a), __builtin_bit_cast(bf16x2, b), c, false); }
__device__ __forceinline__ f32x4 mfma16(u32x4 a, u32x4 b, f32x4 c) { return __builtin_amdgcn_mfma_f32_16x16x32_bf16(__builtin_bit_cast(bf16x8, a), __builtin_bit_cast(bf16x8, b), c, 0, 0, 0); }
__device__ __forceinline__ void dma16(const void* g, LAS void* l) { __builtin_amdgcn_global_load_lds((const u32*)g, (LAS u32*)l, 16, 0, 0); }
#define WAIT_VM(n) asm volatile("s_waitcnt vmcnt(%0)" ::"n"(n) : "memory")
#define WAIT_LGKM0() asm volatile("s_waitcnt lgkmcnt(0)" ::: "memory")
#define BAR() do { asm volatile("" ::: "memory"); __builtin_amdgcn_s_barrier(); asm volatile("" ::: "memory"); } while (0)
#define SB() __builtin_amdgcn_sched_barrier(0)
#define MEMFENCE() asm volatile("" ::: "memory")
#define MAKE_RSRC(p) __builtin_amdgcn_make_buffer_rsrc((void*)(p), 0, 0x7fffffff, 0x00020000)
#define ST16(rs, off, v) __builtin_amdgcn_raw_buffer_store_b128((v), (rs), (int)(off), 0, 16)

template <int CTRL, int RM> __device__ __forceinline__ float dpp_add(float v) { return v + __builtin_bit_cast(float, __builtin_amdgcn_update_dpp(0, __builtin_bit_cast(int, v), CTRL, RM, 0xf, true)); }
__device__ __forceinline__ float wave_sum(float v) {
  v = dpp_add<0x111, 0xf>(v); v = dpp_add<0x112, 0xf>(v); v = dpp_add<0x114, 0xf>(v); v = dpp_add<0x118, 0xf>(v);
  v = dpp_add<0x142, 0xa>(v); v = dpp_add<0x143, 0xc>(v);
  return __builtin_bit_cast(float, __builtin_amdgcn_readlane(__builtin_bit_cast(int, v), 63));
}

#define STAMP(kid, i) do {} while (0)
#define STAMP_DECL do {} while (0)
#define STAMP_FLUSH(kid) do {} while (0)

typedef __attribute__((ext_vector_type(4))) short s16x4;
__device__ __forceinline__ u16x4 tr_read(u32 addr) { u16x4 r; asm volatile("ds_read_b64_tr_b16 %0, %1" : "=v"(r) : "v"(addr) : "memory"); return r; }
#define DS_TR(dst, addr, off) asm volatile("ds_read_b64_tr_b16 %0, %1 offset:%2" : "=v"(dst) : "v"(addr), "n"(off))
#define DS_B64(dst, addr, off) asm volatile("ds_read_b64 %0, %1 offset:%2" : "=v"(dst) : "v"(addr), "n"(off))
#define DS_B128(dst, addr, off) asm volatile("ds_read_b128 %0, %1 offset:%2" : "=v"(dst) : "v"(addr), "n"(off))
#define MFMA_IP(acc, a, b) asm volatile("v_mfma_f32_16x16x32_bf16 %0, %1, %2, %0" : "+v"(acc) : "v"(a), "v"(b))
#define MFMA_Z(acc, a, b) asm volatile("v_mfma_f32_16x16x32_bf16 %0, %1, %2, 0" : "=&v"(acc) : "v"(a), "v"(b))
#define GLD16(dst, voff, sbase, off) asm volatile("global_load_dwordx4 %0, %1, %2 offset:%3" : "=v"(dst) : "v"(voff), "s"(sbase), "n"(off) : "memory")
#define WAIT_LGKM(n) asm volatile("s_waitcnt lgkmcnt(%0)" ::"n"(n) : "memory")

static constexpr int K0_TILES = 32 * 10, K0_STATS = 128, K0_PF = 148;
#define K0_TOUCH 1
#define K0_NPF (8 * (2 + F_PF))
static constexpr int K0_RAW = 17 * 1024, K0_LDS = K0_RAW + 64 * 144;
#define KD_PF 1
#define F_PF 0
#define CODE_PF 10000
__global__ __launch_bounds__(256) void k0_prep(const u16* __restrict__ x, const u16* __restrict__ k, const u16* __restrict__ v,
                                               const u16* __restrict__ shk, const u16* __restrict__ fk, const u16* __restrict__ wg, const u16* __restrict__ wu,
                                               const u16* __restrict__ wd, const u16* __restrict__ outp, char* __restrict__ ws) {
  extern __shared__ __attribute__((aligned(16))) char shm[];
  LAS char* lds = (LAS char*)shm;
  const int t = threadIdx.x, lane = t & 63, w = __builtin_amdgcn_readfirstlane(t >> 6), b = blockIdx.x;
  STAMP_DECL; STAMP(0, 0);
  const auto rs = MAKE_RSRC(ws);
  if (b < K0_TILES) {
    const int st = b / 10, ct = b % 10, s0 = st * 64, c0 = ct * 64;
#pragma unroll
    for (int i = 0; i < 5; ++i) {
      const int pc = w + 4 * i;
      if (pc < 17) {
        const u16* src = pc < 9 ? k : v;
        int row = (pc < 9 ? s0 - 8 + 8 * pc : s0 + 8 * (pc - 9)) + (lane >> 3);
        row = row < 0 ? 0 : row;
        dma16(src + (size_t)row * D + c0 + (lane & 7) * 8, lds + pc * 1024);
      }
    }
    const int cp = t & 31, rg = t >> 5, ch = c0 + 2 * cp;
    const u32x4 sh = *(const u32x4*)(shk + ch * 4);
    WAIT_VM(0); __syncthreads();
    STAMP(0, 1);
    u32 kk[11], vv[8];
    LAS const char* rk = lds + (8 * rg + 5) * 128 + cp * 4;
#pragma unroll
    for (int i = 0; i < 11; ++i) kk[i] = *(LAS const u32*)(rk + i * 128);
#pragma unroll
    for (int i = 0; i < 8; ++i) vv[i] = *(LAS const u32*)(lds + 9 * 1024 + (8 * rg + i) * 128 + cp * 4);
    if (st == 0 && rg == 0) { kk[0] = kk[1] = kk[2] = 0u; }
    const float a0 = bflo(sh.x), a1 = bfhi(sh.x), a2 = bflo(sh.y), a3 = bfhi(sh.y);
    const float b0 = bflo(sh.z), b1 = bfhi(sh.z), b2 = bflo(sh.w), b3 = bfhi(sh.w);
    float ka[11], kb[11];
#pragma unroll
    for (int i = 0; i < 11; ++i) { ka[i] = bflo(kk[i]); kb[i] = bfhi(kk[i]); }
    float ua[8], ub[8];
#pragma unroll
    for (int i = 0; i < 8; ++i) {
      ua[i] = (ka[i] * a3 + ka[i + 1] * a2 + ka[i + 2] * a1 + ka[i + 3] * a0) * bflo(vv[i]);
      ub[i] = (kb[i] * b3 + kb[i + 1] * b2 + kb[i + 2] * b1 + kb[i + 3] * b0) * bfhi(vv[i]);
    }
    LAS char* L1 = lds + K0_RAW;
    const int o = (7 - rg) * 16;
    *(LAS u32x4*)(L1 + (2 * cp) * 144 + o) = (u32x4){pack2(ua[7], ua[6]), pack2(ua[5], ua[4]), pack2(ua[3], ua[2]), pack2(ua[1], ua[0])};
    *(LAS u32x4*)(L1 + (2 * cp + 1) * 144 + o) = (u32x4){pack2(ub[7], ub[6]), pack2(ub[5], ub[4]), pack2(ub[3], ub[2]), pack2(ub[1], ub[0])};
    __syncthreads();
    u32x4 o1[2];
#pragma unroll
    for (int i = 0; i < 2; ++i) o1[i] = *(LAS const u32x4*)(L1 + ((t >> 3) + 32 * i) * 144 + (t & 7) * 16);
#pragma unroll
    for (int i = 0; i < 2; ++i) ST16(rs, WS_UR1 + ((size_t)(c0 + (t >> 3) + 32 * i) * S + (1984 - s0)) * 2 + (t & 7) * 16, o1[i]);
    STAMP(0, 2);
  } else if (b >= K0_TILES + K0_STATS) {
    const int xq = b & 7, i = (b - K0_TILES - K0_STATS) >> 3;
    const char* p;
    if (i < 2) {
      unsigned long long pc;
      asm volatile("s_getpc_b64 s[40:41]\n\ts_add_u32 s40, s40, _Z7k1_convPKtS0_S0_S0_S0_S0_Pc@rel32@lo+4\n\ts_addc_u32 s41, s41, _Z7k1_convPKtS0_S0_S0_S0_S0_Pc@rel32@hi+12\n\ts_mov_b64 %0, s[40:41]" : "=s"(pc) :: "scc", "s40", "s41");
      int o = (i * 4 + w) * 8192 + lane * 128; o = o < CODE_PF - 4 ? o : CODE_PF - 4;
      p = (const char*)pc + (o & ~3);
    } else {
      const int q = (i - 2) * 4 + w;
      p = (const char*)fk + (size_t)xq * 80 * S * 2 + (size_t)q * 8192 + lane * 128;
    }
    u32 sink;
    asm volatile("global_load_dword %0, %1, off\n\ts_waitcnt vmcnt(0)" : "=&v"(sink) : "v"(p) : "memory");
  } else {
    const int rbase = (b - K0_TILES) * 16 + w * 4;
    u32x4 xa[4], xb[4];
#pragma unroll
    for (int r = 0; r < 4; ++r) {
      xa[r] = *(const u32x4*)(x + (size_t)(rbase + r) * D + lane * 8);
      xb[r] = *(const u32x4*)(x + (size_t)(rbase + r) * D + 512 + (lane & 15) * 8);
    }
    const float mk = lane < 16 ? 1.f : 0.f;
    float mean[4], q[4];
#pragma unroll
    for (int r = 0; r < 4; ++r) {
      float s = 0.f, s2 = 0.f;
#pragma unroll
      for (int j = 0; j < 4; ++j) { s += bflo(xa[r][j]) + bfhi(xa[r][j]); s2 += bflo(xb[r][j]) + bfhi(xb[r][j]); }
      mean[r] = wave_sum(s + mk * s2) * (1.0f / D);
    }
#pragma unroll
    for (int r = 0; r < 4; ++r) {
      float s = 0.f, s2 = 0.f;
#pragma unroll
      for (int j = 0; j < 4; ++j) {
        float a = bflo(xa[r][j]) - mean[r], c = bfhi(xa[r][j]) - mean[r]; s += a * a + c * c;
        a = bflo(xb[r][j]) - mean[r]; c = bfhi(xb[r][j]) - mean[r]; s2 += a * a + c * c;
      }
      q[r] = wave_sum(s + mk * s2) * (1.0f / D);
    }
    STAMP(0, 1);
    if (lane < 2) {
      const float m0 = lane ? mean[2] : mean[0], m1 = lane ? mean[3] : mean[1], v0 = lane ? q[2] : q[0], v1 = lane ? q[3] : q[1];
      const float sd0 = rbf(sqrtf(rbf(rbf(v0) + 1e-5f))), sd1 = rbf(sqrtf(rbf(rbf(v1) + 1e-5f)));
      const f32x4 o = {rbf(m0), 1.0f / sd0, rbf(m1), 1.0f / sd1};
      ST16(rs, WS_ST1 + (size_t)(rbase + 2 * lane) * 8, __builtin_bit_cast(u32x4, o));
    }
    STAMP(0, 2);
  }
  if (b == K0_TILES + K0_STATS - 1) { ST16(rs, WS_FLG + t * 16, ((u32x4){0u, 0u, 0u, 0u})); ST16(rs, WS_FLP + t * 16, ((u32x4){0u, 0u, 0u, 0u})); }
  STAMP_FLUSH(0);
}

#define POLL_SLEEP 1
typedef __attribute__((address_space(1))) unsigned gu32;
__device__ __forceinline__ void publish(char* ws, size_t off) {
  WAIT_VM(0); BAR();
  if (threadIdx.x == 0) __hip_atomic_store((gu32*)(ws + off), 1u, __ATOMIC_RELAXED, __HIP_MEMORY_SCOPE_AGENT);
}
__device__ __forceinline__ void await_flags(char* ws, size_t off, int n, int lane) {
  const unsigned long long t0 = __builtin_amdgcn_s_memrealtime();
  for (;;) {
    bool ok = true;
#pragma unroll
    for (int i = 0; i < 4; ++i) {
      const int idx = lane + 64 * i;
      if (64 * i < n) { const unsigned v = __hip_atomic_load((gu32*)(ws + off + (size_t)(idx < n ? idx : 0) * 16), __ATOMIC_RELAXED, __HIP_MEMORY_SCOPE_AGENT); ok &= v == 1u; }
    }
    if (__all(ok)) break;
    if (__builtin_amdgcn_s_memrealtime() - t0 > 400000ull) break;
    __builtin_amdgcn_s_sleep(POLL_SLEEP);
  }
  asm volatile("" ::: "memory");
}

static constexpr int CH_BYTES = 13568, CH_F = 0, CH_U1 = 4224, CH_U0 = 4224 + 4608 + 32;
static constexpr int K1_YL = 4 * CH_BYTES;
static constexpr int K1_YP = 8192 + 64;
#define K1_DELAY 80
static constexpr int K1_CONV = 160, K1_W1 = 54, K1_W2 = 30;

template <bool W1>
__device__ __forceinline__ void retile_job(LAS char* lds, const u16* __restrict__ src, int pitch, int cg, int mat, int ks0, char* ws, int w, int lane) {
  constexpr int NU = W1 ? 10 : 9;
  const auto rs = MAKE_RSRC(ws);
#pragma unroll
  for (int i = 0; i < NU; ++i) {
    const int pc = w + 8 * i, row = 8 * pc + (lane >> 3);
    dma16(src + (size_t)(ks0 * 32 + row) * pitch + (((lane & 7) ^ (row & 7)) * 8), lds + pc * 1024);
  }
  WAIT_VM(0); BAR();
  const int g = lane >> 4, q = (lane & 15) >> 2, p = lane & 3, fb = w & 3;
  const u32 l0 = (u32)(size_t)lds;
  u16x4 lo[NU], hi[NU];
#pragma unroll
  for (int i = 0; i < NU; ++i) {
    const int ks = (w >> 2) + 2 * i, row0 = 32 * ks + 8 * g + q, row1 = row0 + 4, chk = 2 * fb + (p >> 1);
    lo[i] = tr_read(l0 + row0 * 128 + ((chk ^ (row0 & 7)) * 16) + 8 * (p & 1));
    hi[i] = tr_read(l0 + row1 * 128 + ((chk ^ (row1 & 7)) * 16) + 8 * (p & 1));
  }
#pragma unroll
  for (int i = 0; i < NU; ++i) asm volatile("s_waitcnt lgkmcnt(0)" : "+v"(lo[i]), "+v"(hi[i])::"memory");
  float m = 0.f;
  if (W1) {
    float cs = 0.f;
#pragma unroll
    for (int i = 0; i < NU; ++i) {
      const u32x2 l2 = __builtin_bit_cast(u32x2, lo[i]), h2 = __builtin_bit_cast(u32x2, hi[i]);
      cs = dot2(l2.x, 0x3f803f80u, dot2(l2.y, 0x3f803f80u, dot2(h2.x, 0x3f803f80u, dot2(h2.y, 0x3f803f80u, cs))));
    }
    cs += __shfl_xor(cs, 16); cs += __shfl_xor(cs, 32);
    LAS float* red = (LAS float*)(lds + 80 * 1024);
    if (lane < 16) red[w * 16 + lane] = cs;
    WAIT_LGKM0(); BAR();
    m = (red[fb * 16 + (lane & 15)] + red[(fb + 4) * 16 + (lane & 15)]) * (1.0f / D);
  }
#pragma unroll
  for (int i = 0; i < NU; ++i) {
    const int ks = (w >> 2) + 2 * i;
    const u32x2 l2 = __builtin_bit_cast(u32x2, lo[i]), h2 = __builtin_bit_cast(u32x2, hi[i]);
    if (W1) {
      const u32x4 o = {pack2(bflo(l2.x) - m, bfhi(l2.x) - m), pack2(bflo(l2.y) - m, bfhi(l2.y) - m), pack2(bflo(h2.x) - m, bfhi(h2.x) - m), pack2(bflo(h2.y) - m, bfhi(h2.y) - m)};
      const int blk = cg * 4 + fb;
      ST16(rs, WS_W1T + ((size_t)(blk * NKS1 + ks) * 2 + mat) * 1024 + lane * 16, o);
    } else {
      const int df = cg * 4 + fb, dt = df / 5, j = df % 5;
      ST16(rs, WS_W2T + ((size_t)(dt * NKS2P + ks0 + ks) * 5 + j) * 1024 + lane * 16, ((u32x4){l2.x, l2.y, h2.x, h2.y}));
    }
  }
  if (!W1 && ks0 == 36 && cg < 8) {
#pragma unroll
    for (int i = 0; i < 2; ++i) { const int pc = w + 8 * i; if (pc < 10) ST16(rs, WS_W2T + ((size_t)(cg * NKS2P + NKS2) * 5 + pc) * 1024 + lane * 16, ((u32x4){0u, 0u, 0u, 0u})); }
  }
  if (W1 && cg == 26 && mat == 0) {
#pragma unroll
    for (int i = 0; i < 3; ++i) { const int ks = w + 8 * i; if (ks < NKS1) ST16(rs, WS_W1T + ((size_t)(108 * NKS1 + ks) * 2) * 1024 + lane * 16, ((u32x4){0x3f803f80u, 0x3f803f80u, 0x3f803f80u, 0x3f803f80u})); }
  }
}

#define SARG
#define SPASS

__device__ __forceinline__ void conv_phase(LAS char* lds, const u16* __restrict__ x, const u16* __restrict__ fk, const u16* __restrict__ sD, char* __restrict__ ws, int b, int t, int lane, int w SARG) {
  const auto rs = MAKE_RSRC(ws);
  const int cgp = (b & 7) * 20 + (b >> 3);
  const int c = w & 3, hf = w >> 2, d = cgp * 4 + c;
  LAS char* cb = lds + c * CH_BYTES;
  {
    const char* gF = (const char*)fk + (size_t)d * S * 2 + lane * 16;
    const char* g1 = ws + WS_UR1 + (size_t)d * S * 2 + lane * 16;
#pragma unroll
    for (int i = 0; i < 2; ++i) {
      const int pc = hf * 2 + i;
      dma16(g1 + pc * 1024, cb + CH_U1 + pc * 1024);
      dma16(gF + pc * 1024, cb + CH_F + 64 + pc * 1024);
    }
    if (hf == 0) { if (lane < 32) *(LAS u32x4*)(cb + CH_U1 + 4096 + lane * 16) = (u32x4){0, 0, 0, 0}; if (lane < 4) *(LAS u32x4*)(cb + CH_F + lane * 16) = (u32x4){0, 0, 0, 0}; }
    else { if (lane < 32) *(LAS u32x4*)(cb + CH_U0 + 4096 + lane * 16) = (u32x4){0, 0, 0, 0}; }
  }
  const u32x2 dw = *(const u32x2*)(sD + cgp * 4);
  WAIT_VM(0); WAIT_LGKM0(); BAR();
#pragma unroll
  for (int i = 0; i < 2; ++i) {
    const int p0 = 1024 * hf + 512 * i + 8 * lane;
    const u32x4 in = *(LAS const u32x4*)(cb + CH_U1 + p0 * 2); const u32 nx = *(LAS const u32*)(cb + CH_U1 + p0 * 2 + 16);
    *(LAS u32x4*)(cb + CH_U0 + p0 * 2) = (u32x4){__builtin_amdgcn_alignbit(in.y, in.x, 16), __builtin_amdgcn_alignbit(in.z, in.y, 16), __builtin_amdgcn_alignbit(in.w, in.z, 16), __builtin_amdgcn_alignbit(nx, in.w, 16)};
  }
  WAIT_LGKM0(); BAR();
  STAMP(1, 1);
  u32x2 xr[4]; u32x4 stv[2];
  const char* gx = (const char*)x + (size_t)(2 * t) * D * 2 + cgp * 8;
#define C_LDX(i) do { \
      if ((i) == 0) asm volatile("global_load_dwordx2 %0, %1, off" : "=v"(xr[0]) : "v"(gx) : "memory"); \
      if ((i) == 1) asm volatile("global_load_dwordx2 %0, %1, off offset:1280" : "=v"(xr[1]) : "v"(gx) : "memory"); \
      if ((i) == 2) asm volatile("global_load_dwordx2 %0, %1, off" : "=v"(xr[2]) : "v"(gx + (size_t)1024 * D * 2) : "memory"); \
      if ((i) == 3) asm volatile("global_load_dwordx2 %0, %1, off offset:1280" : "=v"(xr[3]) : "v"(gx + (size_t)1024 * D * 2) : "memory"); \
      if ((i) == 4) GLD16(stv[0], t * 16, ws + WS_ST1, 0); \
      if ((i) == 5) GLD16(stv[1], t * 16, ws + WS_ST1 + 8192, 0); } while (0)
#define C_XWAIT() asm volatile("s_waitcnt vmcnt(0) lgkmcnt(0)" : "+v"(xr[0]), "+v"(xr[1]), "+v"(xr[2]), "+v"(xr[3]), "+v"(stv[0]), "+v"(stv[1])::"memory")
  if (hf == 0) {
    const int r = lane & 15, g = lane >> 4;
    f32x4 acc[8];
    const int a = r >> 1, e = r & 1;
    const u32 ub = (u32)(size_t)(cb + (e ? CH_U1 : CH_U0) + 448 - 64 * a + 16 * g);
    u32 fbp[4];
#pragma unroll
    for (int i = 0; i < 4; ++i) fbp[i] = (u32)(size_t)(cb + CH_F + 16 * g + 4 * r + 4) + 1024 * i;
    u32x4 U[2][8]; u32x2 Fr[2][8][2];
#define C_RDU(buf, i, cc) DS_B128(U[buf][i], ub, 512 * (7 - (i)) + 64 * (cc))
#define C_RDF(buf, i, cc, hh) asm volatile("ds_read2_b32 %0, %1 offset0:%2 offset1:%3" : "=v"(Fr[buf][i][hh]) : "v"(fbp[(i) >> 1]), "n"(128 * ((i) & 1) + 16 * (cc) + 2 * (hh)), "n"(128 * ((i) & 1) + 16 * (cc) + 2 * (hh) + 1))
#define C_F(buf, i) ((u32x4){Fr[buf][i][0].x, Fr[buf][i][0].y, Fr[buf][i][1].x, Fr[buf][i][1].y})
#define C_WAIT(buf) do { \
      asm volatile("s_waitcnt lgkmcnt(0)" : "+v"(U[buf][0]), "+v"(U[buf][1]), "+v"(U[buf][2]), "+v"(U[buf][3]), "+v"(U[buf][4]), "+v"(U[buf][5]), "+v"(U[buf][6]), "+v"(U[buf][7])::"memory"); \
      asm volatile("" : "+v"(Fr[buf][0][0]), "+v"(Fr[buf][0][1]), "+v"(Fr[buf][1][0]), "+v"(Fr[buf][1][1]), "+v"(Fr[buf][2][0]), "+v"(Fr[buf][2][1]), "+v"(Fr[buf][3][0]), "+v"(Fr[buf][3][1]), \
                        "+v"(Fr[buf][4][0]), "+v"(Fr[buf][4][1]), "+v"(Fr[buf][5][0]), "+v"(Fr[buf][5][1]), "+v"(Fr[buf][6][0]), "+v"(Fr[buf][6][1]), "+v"(Fr[buf][7][0]), "+v"(Fr[buf][7][1])); } while (0)
#pragma unroll
    for (int i = 0; i < 8; ++i) { C_RDF(0, i, 0, 0); C_RDF(0, i, 0, 1); C_RDU(0, i, 0); }
    C_WAIT(0);
#pragma unroll
    for (int cc = 0; cc < 8; ++cc) {
      const int cur = cc & 1, nxt = cur ^ 1;
      int n = 0;
#pragma unroll
      for (int c1 = 0; c1 < 8; ++c1)
#pragma unroll
        for (int i = c1; i < 8; ++i) {
          if (cc == 0 && c1 == 0) MFMA_Z(acc[i], U[cur][i - c1], C_F(cur, c1)); else MFMA_IP(acc[i], U[cur][i - c1], C_F(cur, c1));
          if (cc < 7 && n < 24) { const int j = n / 3, kk = n % 3; if (kk == 0) C_RDU(nxt, j, cc + 1); else C_RDF(nxt, j, cc + 1, kk - 1); }
          if (cc < 6 && n == 28) C_LDX(cc);
          ++n;
        }
      if (cc < 7) C_WAIT(nxt);
    }
    asm volatile("s_nop 15\n\ts_nop 15" ::: "memory");
    STAMP(1, 2);
    LAS char* yl = lds + K1_YL + c * K1_YP;
#pragma unroll
    for (int j = 0; j < 8; ++j)
#pragma unroll
      for (int hh = 0; hh < 2; ++hh) *(LAS f32x2*)(yl + (256 * j + 32 * (2 * g + hh) + 2 * r) * 4) = (f32x2){acc[j][2 * hh], acc[j][2 * hh + 1]};
    C_XWAIT();
  } else {
    C_LDX(0); C_LDX(1); C_LDX(2); C_LDX(3); C_LDX(4); C_LDX(5);
    C_XWAIT();
  }
  BAR();
  STAMP(1, 3);
#pragma unroll
  for (int i = 0; i < 2; ++i) {
    const int s = 2 * t + 1024 * i;
    f32x2 y2[4];
    const float Dd[4] = {bflo(dw.x), bfhi(dw.x), bflo(dw.y), bfhi(dw.y)};
#pragma unroll
    for (int cc = 0; cc < 4; ++cc) {
      const f32x2 y = *(LAS const f32x2*)(lds + K1_YL + cc * K1_YP + s * 4);
      const u32 uw = *(LAS const u32*)(lds + cc * CH_BYTES + CH_U1 + (2046 - s) * 2);
      y2[cc] = (f32x2){y[0] + bfhi(uw) * Dd[cc], y[1] + bflo(uw) * Dd[cc]};
    }
    const f32x4 st = __builtin_bit_cast(f32x4, stv[i]);
    const u32x2 x0 = xr[2 * i], x1 = xr[2 * i + 1];
    const float xa[4] = {bflo(x0.x), bfhi(x0.x), bflo(x0.y), bfhi(x0.y)}, xb[4] = {bflo(x1.x), bfhi(x1.x), bflo(x1.y), bfhi(x1.y)};
    float ha[4], hb[4];
#pragma unroll
    for (int cc = 0; cc < 4; ++cc) { ha[cc] = xa[cc] + (xa[cc] - st[0]) * st[1] * y2[cc][0]; hb[cc] = xb[cc] + (xb[cc] - st[2]) * st[3] * y2[cc][1]; }
    ST16(rs, WS_HB + ((size_t)cgp * S + s) * 8, ((u32x4){pack2(ha[0], ha[1]), pack2(ha[2], ha[3]), pack2(hb[0], hb[1]), pack2(hb[2], hb[3])}));
  }
  STAMP(1, 4);
}

#define DMA_SC1(m0v, voffv, sbase, off) asm volatile("s_mov_b32 m0, %0\n\ts_nop 0\n\tglobal_load_lds_dwordx4 %1, %2 offset:%3 sc1" :: "s"(m0v), "v"(voffv), "s"(sbase), "n"(off) : "memory")
#define GLD16C(dst, voff, sbase, off) asm volatile("global_load_dwordx4 %0, %1, %2 offset:%3 sc1" : "=v"(dst) : "v"(voff), "s"(sbase), "n"(off) : "memory")
static constexpr int G1_NS = 12, G1_SLOT = 8192, G1_WR = 4, G1_RING = 64 * 1024;
static constexpr int KF_CNT = 60 * 1024;
__host__ __device__ constexpr int g2_slot(int kq, int s) { return s < 3 ? kq * 15360 + s * 5120 : G1_RING + kq * 20480 + (s - 3) * 5120; }
__device__ __forceinline__ void gemm2_prefetch(LAS char* lds, char* __restrict__ ws, int dt, int lane, int first, int last, int step) {
  const u32 lds0 = (u32)(size_t)lds, voff = lane * 16;
  const char* wb = ws + WS_W2T + (size_t)dt * NKS2P * 5 * 1024;
  for (int pc = first; pc < last; pc += step) {
    int kq, o, dst;
    if (pc < 60) { kq = pc / 15; o = (pc - 15 * kq) * 1024; dst = kq * 15360 + o; }
    else { const int q = pc - 60; kq = q / 20; o = 15360 + (q - 20 * kq) * 1024; dst = G1_RING + kq * 20480 + (q - 20 * kq) * 1024; }
    const u32 m0v = __builtin_amdgcn_readfirstlane(lds0 + dst);
    const char* sb = wb + kq * 14 * 5120 + o;
    DMA_SC1(m0v, voff, sb, 0);
  }
}
__device__ __forceinline__ void gemm2_wq_issue(char* __restrict__ ws, int dt, int lane, int w, u32x4 (&wq)[7][3]) {
  const char* wsb = ws + WS_W2T + ((size_t)dt * NKS2P + (w >> 1) * 14 + 7) * 5120 + (w & 1) * 3072;
  const u32 voff = lane * 16;
  const int sh = __builtin_amdgcn_readfirstlane(w & 1);
#pragma unroll
  for (int i = 0; i < 7; ++i) {
    GLD16C(wq[i][0], voff, wsb + i * 5120, 0); GLD16C(wq[i][1], voff, wsb + i * 5120, 1024);
    asm volatile("s_cmp_lg_u32 %3, 0\n\ts_cbranch_scc1 1f\n\tglobal_load_dwordx4 %0, %1, %2 offset:2048 sc1\n1:" : "=&v"(wq[i][2]) : "v"(voff), "s"(wsb + i * 5120), "s"(sh) : "scc", "memory");
  }
}
__device__ __forceinline__ void gemm2_wq_wait(u32x4 (&wq)[7][3]) {
  asm volatile("s_waitcnt vmcnt(0)" : "+v"(wq[0][0]), "+v"(wq[0][1]), "+v"(wq[0][2]), "+v"(wq[1][0]), "+v"(wq[1][1]), "+v"(wq[1][2]), "+v"(wq[2][0]), "+v"(wq[2][1]), "+v"(wq[2][2]),
                                      "+v"(wq[3][0]), "+v"(wq[3][1]), "+v"(wq[3][2])::"memory");
  asm volatile("" : "+v"(wq[4][0]), "+v"(wq[4][1]), "+v"(wq[4][2]), "+v"(wq[5][0]), "+v"(wq[5][1]), "+v"(wq[5][2]), "+v"(wq[6][0]), "+v"(wq[6][1]), "+v"(wq[6][2]));
}

__device__ __forceinline__ void gemm1_phase(LAS char* lds, char* __restrict__ ws, int rt, int ct, int dt2, int lane, int w, unsigned tag, size_t flag_off, u32x4 (&wq)[7][3] SARG) {
  const u32 lds0 = (u32)(size_t)lds + G1_RING;
  const int s0 = rt * 128;
  const u32 voff = lane * 16;
  const auto rs = MAKE_RSRC(ws);
  if (w >= 4) {
    const int j = w - 4;
    const u32 hv = voff ^ ((j & 1) * 128);
    const char* hsb = ws + WS_HB + ((size_t)(2 * j) * S + s0) * 8;
    const u32 ldsw = lds0 + 2 * j * 1024;
#define G1_SLAB(ks) do { DMA_SC1(ldsw + ((ks) % G1_NS) * G1_SLOT, hv, hsb + (size_t)(ks) * 8 * S * 8, 0); DMA_SC1(ldsw + ((ks) % G1_NS) * G1_SLOT + 1024, hv, hsb + (size_t)(ks) * 8 * S * 8 + S * 8, 0); } while (0)
    G1_SLAB(0); G1_SLAB(1); G1_SLAB(2); G1_SLAB(3); G1_SLAB(4);
    WAIT_VM(8); BAR();
    G1_SLAB(5); G1_SLAB(6); G1_SLAB(7); G1_SLAB(8);
    WAIT_VM(8); BAR();
    gemm2_prefetch(lds, ws, dt2, lane, j, 20, 4);
    G1_SLAB(9); G1_SLAB(10); G1_SLAB(11);
    WAIT_VM(6); BAR();
    gemm2_prefetch(lds, ws, dt2, lane, 20 + j, 40, 4);
    G1_SLAB(12); G1_SLAB(13); G1_SLAB(14); G1_SLAB(15);
    WAIT_VM(6); BAR();
    gemm2_prefetch(lds, ws, dt2, lane, 40 + j, 60, 4);
    G1_SLAB(16); G1_SLAB(17); G1_SLAB(18); G1_SLAB(19);
    WAIT_VM(6); BAR();
    gemm2_wq_issue(ws, dt2, lane, w, wq);
    WAIT_VM(14); BAR();
    BAR();
    gemm2_prefetch(lds, ws, dt2, lane, 60 + j, 140, 4);
    gemm2_wq_wait(wq);
  } else {
    const int blk0 = ct * 8 + 2 * w;
    const char* wsA = ws + WS_W1T + (size_t)(blk0 > 108 ? 108 : blk0) * NKS1 * 2048;
    const char* wsB = ws + WS_W1T + (size_t)(blk0 + 1 > 108 ? 108 : blk0 + 1) * NKS1 * 2048;
    const int g = lane >> 4;
    const u32 hbase = lds0 + g * 2048 + (lane & 15) * 8;
    const u32 hoffE = hbase + (g & 1) * 128, hoffO = hbase + (1 - (g & 1)) * 128 - 128;
    const u32 hoffE2 = hoffE + 65536, hoffO2 = hoffO + 65536;
    f32x4 acc[8][4];
    const int stat = __builtin_amdgcn_readfirstlane(blk0 == 108 ? 1 : 0);
#define G1_LDW(ks, i) do { if ((i) == 0) GLD16C(wr[(ks) % G1_WR][0], voff, wsA + (ks) * 2048, 0); if ((i) == 1) GLD16C(wr[(ks) % G1_WR][1], voff, wsA + (ks) * 2048, 1024); \
      if ((i) == 2) GLD16C(wr[(ks) % G1_WR][2], voff, wsB + (ks) * 2048, 0); if ((i) == 3) GLD16C(wr[(ks) % G1_WR][3], voff, wsB + (ks) * 2048, 1024); } while (0)
#define G1_RD(sf, hh, ks) DS_B64(hf[sf][hh], ((ks) % G1_NS) >= 8 ? (((sf) & 1) ? hoffO2 : hoffE2) : (((sf) & 1) ? hoffO : hoffE), (((ks) % G1_NS) & 7) * G1_SLOT + (hh) * 1024 + (sf) * 128)
#define G1_H(sf) ((u32x4){hf[sf][0].x, hf[sf][0].y, hf[sf][1].x, hf[sf][1].y})
#define G1_MM(ks, sf, i, STAT) do { \
      if (STAT) { if ((i) == 0) { if ((ks) == 0) MFMA_Z(acc[sf][0], wr[0][0], G1_H(sf)); else MFMA_IP(acc[sf][0], wr[(ks) % G1_WR][0], G1_H(sf)); } \
                  if ((i) == 2) { if ((ks) == 0) MFMA_Z(acc[sf][2], G1_H(sf), G1_H(sf)); else MFMA_IP(acc[sf][2], G1_H(sf), G1_H(sf)); } } \
      else { if ((ks) == 0) MFMA_Z(acc[sf][i], wr[0][i], G1_H(sf)); else MFMA_IP(acc[sf][i], wr[(ks) % G1_WR][i], G1_H(sf)); } } while (0)
#define G1_BODY(STAT) { \
      u32x4 wr[G1_WR][4]; u32x2 hf[8][2]; \
      _Pragma("unroll") for (int ks = 0; ks < 3; ++ks) { G1_LDW(ks, 0); G1_LDW(ks, 1); G1_LDW(ks, 2); G1_LDW(ks, 3); } \
      BAR();                                                   \
      STAMP(1, 7); \
      _Pragma("unroll") for (int sf = 0; sf < 7; ++sf) { G1_RD(sf, 0, 0); G1_RD(sf, 1, 0); } \
      _Pragma("unroll") for (int ks = 0; ks < NKS1; ++ks) { \
        if ((ks & 3) == 0) BAR(); \
          \
        if (ks + 2 < NKS1) WAIT_VM(8); else if (ks + 1 < NKS1) WAIT_VM(4); else WAIT_VM(0); \
        asm volatile("" : "+v"(wr[ks % G1_WR][0]), "+v"(wr[ks % G1_WR][1]), "+v"(wr[ks % G1_WR][2]), "+v"(wr[ks % G1_WR][3])); \
        _Pragma("unroll") for (int sf = 0; sf < 8; ++sf) { \
            \
          if (ks + 1 < NKS1 || sf == 0) asm volatile("s_waitcnt lgkmcnt(12)" : "+v"(hf[sf][0]), "+v"(hf[sf][1])::"memory"); \
          else if (sf < 7) asm volatile("s_waitcnt lgkmcnt(%2)" : "+v"(hf[sf][0]), "+v"(hf[sf][1]) : "n"(14 - 2 * sf) : "memory"); \
          else asm volatile("s_waitcnt lgkmcnt(0)" : "+v"(hf[sf][0]), "+v"(hf[sf][1])::"memory"); \
          G1_MM(ks, sf, 0, STAT); \
          if (sf == 0) G1_RD(7, 0, ks); else if (ks + 1 < NKS1) G1_RD(sf - 1, 0, ks + 1); \
          G1_MM(ks, sf, 1, STAT); \
          if (sf == 0) G1_RD(7, 1, ks); else if (ks + 1 < NKS1) G1_RD(sf - 1, 1, ks + 1); \
          G1_MM(ks, sf, 2, STAT); \
          if ((sf & 1) && ks + 3 < NKS1) G1_LDW(ks + 3, sf >> 1); \
          G1_MM(ks, sf, 3, STAT); \
        } } }
    if (stat) G1_BODY(true) else G1_BODY(false)
    BAR();
    asm volatile("s_nop 15\n\ts_nop 15" ::: "memory");
    STAMP(1, 8);
    if (stat) {
      const int sx = lane & 15;
#pragma unroll
      for (int sp = 0; sp < 2; ++sp) {
        float r2[4];
#pragma unroll
        for (int q = 0; q < 4; ++q) {
          const int sf = 4 * sp + q;
          const float dg = (sx & 2) ? ((sx & 1) ? acc[sf][2][3] : acc[sf][2][2]) : ((sx & 1) ? acc[sf][2][1] : acc[sf][2][0]);
          const float ssq = __shfl(dg, (sx >> 2) * 16 + sx);
          const float mean = acc[sf][0][0] * (1.0f / D);
          const float var = fmaxf(ssq * (1.0f / D) - mean * mean, 0.f);
          const float r = 1.0f / rbf(sqrtf(rbf(rbf(var) + 1e-5f)));
          r2[q] = r * r;
        }
        const float mine = g == 0 ? r2[0] : (g == 1 ? r2[1] : (g == 2 ? r2[2] : r2[3]));
        const int b4 = lane & ~3;
        const f32x4 o = {__shfl(mine, b4), __shfl(mine, b4 + 1), __shfl(mine, b4 + 2), __shfl(mine, b4 + 3)};
        if ((lane & 3) == 0) ST16(rs, WS_ST2 + (size_t)(s0 + 64 * sp + lane) * 4, __builtin_bit_cast(u32x4, o));
      }
    } else {
#pragma unroll
      for (int bi = 0; bi < 2; ++bi) {
        const int blk = blk0 + bi;
        if (blk < 108) {
          const u32 obase = (u32)(WS_HF + ((size_t)(rt * 8 + (lane >> 5)) * NKS2 + (blk >> 1)) * 1024) + (blk & 1) * 512 + (lane & 31) * 16;
#pragma unroll
          for (int pr = 0; pr < 4; ++pr) {
            const f32x4 z4 = {0.f, 0.f, 0.f, 0.f};
            const f32x4 hE = __builtin_elementwise_max(acc[2 * pr][2 * bi], z4) * acc[2 * pr][2 * bi + 1], hO = __builtin_elementwise_max(acc[2 * pr + 1][2 * bi], z4) * acc[2 * pr + 1][2 * bi + 1];
            u32 e0 = pack2(hE[0], hE[1]), e1 = pack2(hE[2], hE[3]), o0 = pack2(hO[0], hO[1]), o1 = pack2(hO[2], hO[3]);
            auto r0 = __builtin_amdgcn_permlane32_swap(e0, o0, false, false); auto r0b = __builtin_amdgcn_permlane16_swap(r0[0], r0[1], false, false);
            auto r1 = __builtin_amdgcn_permlane32_swap(e1, o1, false, false); auto r1b = __builtin_amdgcn_permlane16_swap(r1[0], r1[1], false, false);
            ST16(rs, obase + pr * 2 * NKS2 * 1024, ((u32x4){r0b[0], r1b[0], r0b[1], r1b[1]}));
          }
        }
      }
    }
    WAIT_VM(0);
    unsigned old = 0;
    if (lane == 0) old = __hip_atomic_fetch_add((LAS unsigned*)(lds + KF_CNT), 1u, __ATOMIC_RELAXED, __HIP_MEMORY_SCOPE_WORKGROUP);
    if (lane == 0 && old == 3u) __hip_atomic_store((gu32*)(ws + flag_off), tag, __ATOMIC_RELAXED, __HIP_MEMORY_SCOPE_AGENT);
    STAMP(1, 9);
    gemm2_wq_issue(ws, dt2, lane, w, wq);
    gemm2_wq_wait(wq);
  }
}

__device__ __forceinline__ void gemm2_phase(LAS char* lds, char* __restrict__ ws, u16* __restrict__ out, int rt, int dt, int lane, int w, u32x4 (&wq)[7][3] SARG) {
  const int sh = w & 1, kq = w >> 1, g = lane >> 4;
  const u32 voff = lane * 16, lds0 = (u32)(size_t)lds;
  const char* hA = ws + WS_HF + ((size_t)(rt * 4 + 2 * sh) * NKS2 + kq * 14) * 1024;
  const char* hB = hA + (size_t)NKS2 * 1024;
  const int hclamp = kq == 3 ? 1 : 0;
  u32x4 hq[6][2], wf[2][5];
#define G2_LH(n) do { const int o_ = ((n) >= 12 && hclamp) ? 11 * 1024 : (n) * 1024; GLD16C(hq[(n) % 6][0], voff, hA + o_, 0); GLD16C(hq[(n) % 6][1], voff, hB + o_, 0); } while (0)
  const u32 rlo = lds0 + kq * 15360 + lane * 16, rhi = lds0 + G1_RING + kq * 20480 - 3 * 5120 + lane * 16;
#define G2_SB(n) ((((n) % 7) < 3 ? rlo : rhi))
#define G2_READ(n) do { DS_B128(wf[(n) & 1][0], G2_SB(n), ((n) % 7) * 5120); DS_B128(wf[(n) & 1][1], G2_SB(n), ((n) % 7) * 5120 + 1024); DS_B128(wf[(n) & 1][2], G2_SB(n), ((n) % 7) * 5120 + 2048); \
    DS_B128(wf[(n) & 1][3], G2_SB(n), ((n) % 7) * 5120 + 3072); DS_B128(wf[(n) & 1][4], G2_SB(n), ((n) % 7) * 5120 + 4096); } while (0)
  const int shs = __builtin_amdgcn_readfirstlane(sh);
#define G2_WST(n, i) do { if ((i) < 2) asm volatile("ds_write_b128 %0, %1 offset:%2" :: "v"(G2_SB(n) + sh * 3072), "v"(wq[(n) - 7][i]), "n"(((n) % 7) * 5120 + (i) * 1024) : "memory"); \
    else asm volatile("s_cmp_lg_u32 %3, 0\n\ts_cbranch_scc1 1f\n\tds_write_b128 %0, %1 offset:%2\n1:" :: "v"(G2_SB(n)), "v"(wq[(n) - 7][2]), "n"(((n) % 7) * 5120 + 2048), "s"(shs) : "scc", "memory"); } while (0)
  G2_LH(0); G2_LH(1);
  float r2[3]; u32x2 res[3];
#pragma unroll
  for (int j = 0; j < 3; ++j) {
    const int f = kq + 4 * j < 10 ? kq + 4 * j : kq, srow = rt * 64 + (2 * sh + f / 5) * 16 + (lane & 15);
    asm volatile("global_load_dword %0, %1, off sc1" : "=v"(r2[j]) : "v"(ws + WS_ST2 + (size_t)srow * 4) : "memory");
    asm volatile("global_load_dwordx2 %0, %1, off sc1" : "=v"(res[j]) : "v"(ws + WS_HB + ((size_t)(dt * 20 + (f % 5) * 4 + g) * S + srow) * 8) : "memory");
  }
  G2_LH(2); G2_LH(3); G2_LH(4);
  f32x4 acc[10];
  G2_READ(0);
  WAIT_VM(14);
  STAMP(1, 11);
#define G2_MM(n, m) do { if ((n) == 0) MFMA_Z(acc[m], wf[0][(m) % 5], hq[0][(m) / 5]); else MFMA_IP(acc[m], wf[(n) & 1][(m) % 5], hq[(n) % 6][(m) / 5]); } while (0)
#define G2D(k) do {} while (0)
  u32 g2d[30];
#define G2_STEP(n) do { G2D(2 * (n)); \
    if ((n) == 1) WAIT_VM(14); else if ((n) > 1) { constexpr int iss_ = 5 + (n) < 14 ? 5 + (n) : 14; WAIT_VM(2 * (iss_ - ((n) + 1))); } \
    asm volatile("s_waitcnt lgkmcnt(0)" : "+v"(wf[(n) & 1][0]), "+v"(wf[(n) & 1][1]), "+v"(wf[(n) & 1][2]), "+v"(wf[(n) & 1][3]), "+v"(wf[(n) & 1][4]), "+v"(hq[(n) % 6][0]), "+v"(hq[(n) % 6][1])::"memory"); \
    if (((n) & 1) && (n) <= 9) BAR(); \
    G2D(2 * (n) + 1); \
    if ((n) + 1 < 14) G2_READ(((n) + 1 < 14 ? (n) + 1 : 0)); \
    G2_MM(n, 0); G2_MM(n, 1); if ((n) + 5 < 14) G2_LH(((n) + 5 < 14 ? (n) + 5 : 0)); \
    G2_MM(n, 2); G2_MM(n, 3); if ((n) >= 1 && (n) <= 7) G2_WST(((n) >= 1 && (n) <= 7 ? (n) + 6 : 7), 0); \
    G2_MM(n, 4); G2_MM(n, 5); G2_MM(n, 6); if ((n) >= 1 && (n) <= 7) G2_WST(((n) >= 1 && (n) <= 7 ? (n) + 6 : 7), 1); \
    G2_MM(n, 7); G2_MM(n, 8); if ((n) >= 1 && (n) <= 7) G2_WST(((n) >= 1 && (n) <= 7 ? (n) + 6 : 7), 2); \
    G2_MM(n, 9); } while (0)
  G2_STEP(0); G2_STEP(1); G2_STEP(2); G2_STEP(3); G2_STEP(4); G2_STEP(5); G2_STEP(6);
  G2_STEP(7); G2_STEP(8); G2_STEP(9); G2_STEP(10); G2_STEP(11); G2_STEP(12); G2_STEP(13);
  asm volatile("s_nop 15\n\ts_nop 15" ::: "memory");
  asm volatile("" : "+v"(r2[0]), "+v"(r2[1]), "+v"(r2[2]), "+v"(res[0]), "+v"(res[1]), "+v"(res[2]));
  STAMP(1, 12);
  BAR();
  LAS char* xb = lds + sh * 40960 + lane * 16;
#pragma unroll
  for (int f = 0; f < 10; ++f) if (kq != (f & 3)) *(LAS f32x4*)(xb + (f * 4 + kq) * 1024) = acc[f];
  WAIT_LGKM0(); BAR();
  u32 pk[3][2];
#define G2_FIN(KQ) do { _Pragma("unroll") for (int j = 0; j < 3; ++j) { \
      constexpr int kq_ = (KQ); const int f = kq_ + 4 * j < 10 ? kq_ + 4 * j : kq_; \
      f32x4 a = acc[f]; \
      _Pragma("unroll") for (int q = 0; q < 4; ++q) if (q != kq_) a += *(LAS const f32x4*)(xb + (f * 4 + q) * 1024); \
      pk[j][0] = pack2(bflo(res[j].x) + a[0] * r2[j], bfhi(res[j].x) + a[1] * r2[j]); \
      pk[j][1] = pack2(bflo(res[j].y) + a[2] * r2[j], bfhi(res[j].y) + a[3] * r2[j]); } } while (0)
  if (kq == 0) G2_FIN(0); else if (kq == 1) G2_FIN(1); else if (kq == 2) G2_FIN(2); else G2_FIN(3);
  const auto ro = MAKE_RSRC(out);
  {
    auto r0 = __builtin_amdgcn_permlane32_swap(pk[0][0], pk[1][0], false, false); auto r0b = __builtin_amdgcn_permlane16_swap(r0[0], r0[1], false, false);
    auto r1 = __builtin_amdgcn_permlane32_swap(pk[0][1], pk[1][1], false, false); auto r1b = __builtin_amdgcn_permlane16_swap(r1[0], r1[1], false, false);
    const int f = kq + 4 * (lane >> 5);
    ST16(ro, ((size_t)(rt * 64 + (2 * sh + f / 5) * 16 + (lane & 15)) * D + dt * 80 + (f % 5) * 16 + ((lane >> 4) & 1) * 8) * 2, ((u32x4){r0b[0], r1b[0], r0b[1], r1b[1]}));
  }
  if (kq < 2) {
    auto r0 = __builtin_amdgcn_permlane32_swap(pk[2][0], pk[2][0], false, false); auto r0b = __builtin_amdgcn_permlane16_swap(r0[0], r0[1], false, false);
    auto r1 = __builtin_amdgcn_permlane32_swap(pk[2][1], pk[2][1], false, false); auto r1b = __builtin_amdgcn_permlane16_swap(r1[0], r1[1], false, false);
    const int f = kq + 8;
    if (lane < 32) ST16(ro, ((size_t)(rt * 64 + (2 * sh + f / 5) * 16 + (lane & 15)) * D + dt * 80 + (f % 5) * 16 + ((lane >> 4) & 1) * 8) * 2, ((u32x4){r0b[0], r1b[0], r0b[1], r1b[1]}));
  }
}

static constexpr int KF_LDS = 160 * 1024, K1_LDS = 96 * 1024;
#define CODE_PF2 30000
__global__ __launch_bounds__(512, 2) void k1_conv(const u16* __restrict__ x, const u16* __restrict__ fk, const u16* __restrict__ sD, const u16* __restrict__ wg,
                                                  const u16* __restrict__ wu, const u16* __restrict__ wd, char* __restrict__ ws) {
  extern __shared__ __attribute__((aligned(16))) char shm[];
  LAS char* lds = (LAS char*)shm;
  const int t = threadIdx.x, lane = t & 63, w = __builtin_amdgcn_readfirstlane(t >> 6), b = blockIdx.x;
  STAMP_DECL; STAMP(1, 0);
  if (b < K1_CONV) conv_phase(lds, x, fk, sD, ws, b, t, lane, w SPASS);
  else if (b < K1_CONV + K1_W1 + K1_W2) {
    { const unsigned long long t0 = __builtin_amdgcn_s_memrealtime(); while (__builtin_amdgcn_s_memrealtime() - t0 < K1_DELAY) __builtin_amdgcn_s_sleep(4); }
    const int jb = b - K1_CONV;
    if (jb < K1_W1) retile_job<true>(lds, ((jb & 1) ? wu : wg) + (jb >> 1) * 64, F, jb >> 1, jb & 1, 0, ws, w, lane);
    else { const int j2 = jb - K1_W1; retile_job<false>(lds, wd + (j2 / 3) * 64, D, j2 / 3, 0, (j2 % 3) * 18, ws, w, lane); }
  } else if (CODE_PF2 > 0 && w * 8192 < CODE_PF2) {
    unsigned long long pc;
    asm volatile("s_getpc_b64 s[40:41]\n\ts_add_u32 s40, s40, _Z6k2_ffnPcPt@rel32@lo+4\n\ts_addc_u32 s41, s41, _Z6k2_ffnPcPt@rel32@hi+12\n\ts_mov_b64 %0, s[40:41]" : "=s"(pc) :: "scc", "s40", "s41");
    int o = w * 8192 + lane * 128; o = o < CODE_PF2 - 4 ? o : CODE_PF2 - 4;
    u32 sink;
    asm volatile("global_load_dword %0, %1, off\n\ts_waitcnt vmcnt(0)" : "=&v"(sink) : "v"((const char*)pc + (o & ~3)) : "memory");
  }
  STAMP(1, 5);
  STAMP_FLUSH(1);
}

__global__ __launch_bounds__(512, 2) void k2_ffn(char* __restrict__ ws, u16* __restrict__ out) {
  extern __shared__ __attribute__((aligned(16))) char shm[];
  LAS char* lds = (LAS char*)shm;
  const int t = threadIdx.x, lane = t & 63, w = __builtin_amdgcn_readfirstlane(t >> 6), b = blockIdx.x;
  STAMP_DECL; STAMP(2, 0);
  const int xg = b & 7, ix = b >> 3;
  const int p = (xg & 3) * 4 + (ix & 3), j = (xg >> 2) * 8 + (ix >> 2);
  if (t == 0) *(LAS unsigned*)(lds + KF_CNT) = 0u;
  WAIT_LGKM0(); BAR();
  u32x4 wq[7][3];
  if (j < 14) gemm1_phase(lds, ws, p, j, j >> 1, lane, w, 1u, WS_FLP + (size_t)(p * 16 + j) * 16, wq SPASS);
  else {
    gemm2_prefetch(lds, ws, j >> 1, lane, w, 140, 8);
    gemm2_wq_issue(ws, j >> 1, lane, w, wq);
    gemm2_wq_wait(wq);
  }
  if (w == 4) await_flags(ws, WS_FLP + (size_t)p * 256, 14, lane);
  BAR();
  STAMP(2, 10);
  gemm2_phase(lds, ws, out, p * 2 + (j & 1), j >> 1, lane, w, wq SPASS);
  STAMP(2, 13);
  STAMP_FLUSH(2);
}

extern "C" void kernel_launch(void* const* d_in, const int* in_sizes, int n_in, void* d_out, int out_size, void* d_ws, size_t ws_size, hipStream_t stream) {
  const u16* q = (const u16*)d_in[0]; const u16* k = (const u16*)d_in[1]; const u16* v = (const u16*)d_in[2];
  const u16* shk = (const u16*)d_in[3]; const u16* fk = (const u16*)d_in[4]; const u16* sD = (const u16*)d_in[5];
  const u16* wg = (const u16*)d_in[7]; const u16* wu = (const u16*)d_in[8]; const u16* wd = (const u16*)d_in[9];
  char* ws = (char*)d_ws;
  (void)hipFuncSetAttribute((const void*)k0_prep, hipFuncAttributeMaxDynamicSharedMemorySize, K0_LDS);
  (void)hipFuncSetAttribute((const void*)k1_conv, hipFuncAttributeMaxDynamicSharedMemorySize, K1_LDS);
  (void)hipFuncSetAttribute((const void*)k2_ffn, hipFuncAttributeMaxDynamicSharedMemorySize, KF_LDS);
  k0_prep<<<K0_TILES + K0_STATS + K0_NPF, 256, K0_LDS, stream>>>(q, k, v, shk, fk, wg, wu, wd, (const u16*)d_out, ws);
  k1_conv<<<K1_CONV + K1_W1 + K1_W2 + 12, 512, K1_LDS, stream>>>(q, fk, sD, wg, wu, wd, ws);
  k2_ffn<<<256, 512, KF_LDS, stream>>>(ws, (u16*)d_out);
}
```

```cpp
#include <hip/hip_runtime.h>
#include <hip/hip_bf16.h>

typedef unsigned short u16;
typedef unsigned int u32;
typedef __attribute__((ext_vector_type(2))) unsigned int u32x2;
typedef __attribute__((ext_vector_type(4))) unsigned int u32x4;
typedef __attribute__((ext_vector_type(8))) short bf16x8;
typedef __attribute__((ext_vector_type(2))) __bf16 bf16x2;
typedef __attribute__((ext_vector_type(2))) float f32x2;
typedef __attribute__((ext_vector_type(4))) float f32x4;
typedef __attribute__((ext_vector_type(4))) unsigned short u16x4;
#define LAS __attribute__((address_space(3)))

static constexpr int S = 2048, D = 640, F = 1728;
static constexpr int NKS1 = D / 32;
static constexpr int NKS2 = F / 32;

static constexpr size_t WS_UR1 = 0;
static constexpr size_t WS_HB  = WS_UR1 + (size_t)D * S * 2;
static constexpr size_t WS_ST1 = WS_HB  + (size_t)D * S * 2;
static constexpr size_t WS_ST2 = WS_ST1 + (size_t)S * 8;
static constexpr size_t WS_W1T = WS_ST2 + (size_t)16 * 1024;
static constexpr int NKS2P = 56;
static constexpr size_t WS_W2T = WS_W1T + (size_t)14 * 8 * NKS1 * 2048;
static constexpr size_t WS_HF  = WS_W2T + (size_t)8 * NKS2P * 5 * 1024;
static constexpr size_t WS_FLG = WS_HF  + (size_t)128 * NKS2 * 1024;
static constexpr size_t WS_FLP = WS_FLG + 4096;
static constexpr size_t WS_END = WS_FLP + 4096;

__device__ __forceinline__ float bflo(u32 w) { return __uint_as_float(w << 16); }
__device__ __forceinline__ float bfhi(u32 w) { return __uint_as_float(w & 0xffff0000u); }
__device__ __forceinline__ u32 pack2(float lo, float hi) { f32x2 v = {lo, hi}; bf16x2 b = __builtin_convertvector(v, bf16x2); return __builtin_bit_cast(u32, b); }
__device__ __forceinline__ float relu1(float x) { float r; asm("v_max_f32 %0, 0, %1" : "=v"(r) : "v"(x)); return r; }
__device__ __forceinline__ f32x4 relu4(f32x4 x) { return (f32x4){relu1(x[0]), relu1(x[1]), relu1(x[2]), relu1(x[3])}; }
__device__ __forceinline__ float rbf(float x) { return bflo(pack2(x, 0.f)); }
__device__ __forceinline__ float dot2(u32 a, u32 b, float c) { return __builtin_amdgcn_fdot2_f32_bf16(__builtin_bit_cast(bf16x2, a), __builtin_bit_cast(bf16x2, b), c, false); }
__device__ __forceinline__ f32x4 mfma16(u32x4 a, u32x4 b, f32x4 c) { return __builtin_amdgcn_mfma_f32_16x16x32_bf16(__builtin_bit_cast(bf16x8, a), __builtin_bit_cast(bf16x8, b), c, 0, 0, 0); }
__device__ __forceinline__ void dma16(const void* g, LAS void* l) { __builtin_amdgcn_global_load_lds((const u32*)g, (LAS u32*)l, 16, 0, 0); }
#define WAIT_VM(n) asm volatile("s_waitcnt vmcnt(%0)" ::"n"(n) : "memory")
#define WAIT_LGKM0() asm volatile("s_waitcnt lgkmcnt(0)" ::: "memory")
#define BAR() do { asm volatile("" ::: "memory"); __builtin_amdgcn_s_barrier(); asm volatile("" ::: "memory"); } while (0)
#define SB() __builtin_amdgcn_sched_barrier(0)
#define MEMFENCE() asm volatile("" ::: "memory")
#define MAKE_RSRC(p) __builtin_amdgcn_make_buffer_rsrc((void*)(p), 0, 0x7fffffff, 0x00020000)
#define ST16(rs, off, v) __builtin_amdgcn_raw_buffer_store_b128((v), (rs), (int)(off), 0, 16)

template <int CTRL, int RM> __device__ __forceinline__ float dpp_add(float v) { return v + __builtin_bit_cast(float, __builtin_amdgcn_update_dpp(0, __builtin_bit_cast(int, v), CTRL, RM, 0xf, true)); }
__device__ __forceinline__ float wave_sum(float v) {
  v = dpp_add<0x111, 0xf>(v); v = dpp_add<0x112, 0xf>(v); v = dpp_add<0x114, 0xf>(v); v = dpp_add<0x118, 0xf>(v);
  v = dpp_add<0x142, 0xa>(v); v = dpp_add<0x143, 0xc>(v);
  return __builtin_bit_cast(float, __builtin_amdgcn_readlane(__builtin_bit_cast(int, v), 63));
}

#define STAMP(kid, i) do {} while (0)
#define STAMP_DECL do {} while (0)
#define STAMP_FLUSH(kid) do {} while (0)

typedef __attribute__((ext_vector_type(4))) short s16x4;
__device__ __forceinline__ u16x4 tr_read(u32 addr) { u16x4 r; asm volatile("ds_read_b64_tr_b16 %0, %1" : "=v"(r) : "v"(addr) : "memory"); return r; }
#define DS_TR(dst, addr, off) asm volatile("ds_read_b64_tr_b16 %0, %1 offset:%2" : "=v"(dst) : "v"(addr), "n"(off))
#define DS_B64(dst, addr, off) asm volatile("ds_read_b64 %0, %1 offset:%2" : "=v"(dst) : "v"(addr), "n"(off))
#define DS_B128(dst, addr, off) asm volatile("ds_read_b128 %0, %1 offset:%2" : "=v"(dst) : "v"(addr), "n"(off))
#define MFMA_IP(acc, a, b) asm volatile("v_mfma_f32_16x16x32_bf16 %0, %1, %2, %0" : "+v"(acc) : "v"(a), "v"(b))
#define MFMA_Z(acc, a, b) asm volatile("v_mfma_f32_16x16x32_bf16 %0, %1, %2, 0" : "=&v"(acc) : "v"(a), "v"(b))
#define GLD16(dst, voff, sbase, off) asm volatile("global_load_dwordx4 %0, %1, %2 offset:%3" : "=v"(dst) : "v"(voff), "s"(sbase), "n"(off) : "memory")
#define WAIT_LGKM(n) asm volatile("s_waitcnt lgkmcnt(%0)" ::"n"(n) : "memory")

static constexpr int K0_TILES = 32 * 10, K0_STATS = 128, K0_PF = 148;
#define K0_TOUCH 1
#define K0_NPF (8 * (2 + F_PF))
static constexpr int K0_RAW = 17 * 1024, K0_LDS = K0_RAW + 64 * 144;
#define KD_PF 1
#define F_PF 0
#define CODE_PF 10000
__global__ __launch_bounds__(256) void k0_prep(const u16* __restrict__ x, const u16* __restrict__ k, const u16* __restrict__ v,
                                               const u16* __restrict__ shk, const u16* __restrict__ fk, const u16* __restrict__ wg, const u16* __restrict__ wu,
                                               const u16* __restrict__ wd, const u16* __restrict__ outp, char* __restrict__ ws) {
  extern __shared__ __attribute__((aligned(16))) char shm[];
  LAS char* lds = (LAS char*)shm;
  const int t = threadIdx.x, lane = t & 63, w = __builtin_amdgcn_readfirstlane(t >> 6), b = blockIdx.x;
  STAMP_DECL; STAMP(0, 0);
  const auto rs = MAKE_RSRC(ws);
  if (b < K0_TILES) {
    const int st = b / 10, ct = b % 10, s0 = st * 64, c0 = ct * 64;
#pragma unroll
    for (int i = 0; i < 5; ++i) {
      const int pc = w + 4 * i;
      if (pc < 17) {
        const u16* src = pc < 9 ? k : v;
        int row = (pc < 9 ? s0 - 8 + 8 * pc : s0 + 8 * (pc - 9)) + (lane >> 3);
        row = row < 0 ? 0 : row;
        dma16(src + (size_t)row * D + c0 + (lane & 7) * 8, lds + pc * 1024);
      }
    }
    const int cp = t & 31, rg = t >> 5, ch = c0 + 2 * cp;
    const u32x4 sh = *(const u32x4*)(shk + ch * 4);
    WAIT_VM(0); __syncthreads();
    STAMP(0, 1);
    u32 kk[11], vv[8];
    LAS const char* rk = lds + (8 * rg + 5) * 128 + cp * 4;
#pragma unroll
    for (int i = 0; i < 11; ++i) kk[i] = *(LAS const u32*)(rk + i * 128);
#pragma unroll
    for (int i = 0; i < 8; ++i) vv[i] = *(LAS const u32*)(lds + 9 * 1024 + (8 * rg + i) * 128 + cp * 4);
    if (st == 0 && rg == 0) { kk[0] = kk[1] = kk[2] = 0u; }
    const float a0 = bflo(sh.x), a1 = bfhi(sh.x), a2 = bflo(sh.y), a3 = bfhi(sh.y);
    const float b0 = bflo(sh.z), b1 = bfhi(sh.z), b2 = bflo(sh.w), b3 = bfhi(sh.w);
    float ka[11], kb[11];
#pragma unroll
    for (int i = 0; i < 11; ++i) { ka[i] = bflo(kk[i]); kb[i] = bfhi(kk[i]); }
    float ua[8], ub[8];
#pragma unroll
    for (int i = 0; i < 8; ++i) {
      ua[i] = (ka[i] * a3 + ka[i + 1] * a2 + ka[i + 2] * a1 + ka[i + 3] * a0) * bflo(vv[i]);
      ub[i] = (kb[i] * b3 + kb[i + 1] * b2 + kb[i + 2] * b1 + kb[i + 3] * b0) * bfhi(vv[i]);
    }
    LAS char* L1 = lds + K0_RAW;
    const int o = (7 - rg) * 16;
    *(LAS u32x4*)(L1 + (2 * cp) * 144 + o) = (u32x4){pack2(ua[7], ua[6]), pack2(ua[5], ua[4]), pack2(ua[3], ua[2]), pack2(ua[1], ua[0])};
    *(LAS u32x4*)(L1 + (2 * cp + 1) * 144 + o) = (u32x4){pack2(ub[7], ub[6]), pack2(ub[5], ub[4]), pack2(ub[3], ub[2]), pack2(ub[1], ub[0])};
    __syncthreads();
    u32x4 o1[2];
#pragma unroll
    for (int i = 0; i < 2; ++i) o1[i] = *(LAS const u32x4*)(L1 + ((t >> 3) + 32 * i) * 144 + (t & 7) * 16);
#pragma unroll
    for (int i = 0; i < 2; ++i) ST16(rs, WS_UR1 + ((size_t)(c0 + (t >> 3) + 32 * i) * S + (1984 - s0)) * 2 + (t & 7) * 16, o1[i]);
    STAMP(0, 2);
  } else if (b >= K0_TILES + K0_STATS) {
    const int xq = b & 7, i = (b - K0_TILES - K0_STATS) >> 3;
    const char* p;
    if (i < 2) {
      unsigned long long pc;
      asm volatile("s_getpc_b64 s[40:41]\n\ts_add_u32 s40, s40, _Z7k1_convPKtS0_S0_S0_S0_S0_Pc@rel32@lo+4\n\ts_addc_u32 s41, s41, _Z7k1_convPKtS0_S0_S0_S0_S0_Pc@rel32@hi+12\n\ts_mov_b64 %0, s[40:41]" : "=s"(pc) :: "scc", "s40", "s41");
      int o = (i * 4 + w) * 8192 + lane * 128; o = o < CODE_PF - 4 ? o : CODE_PF - 4;
      p = (const char*)pc + (o & ~3);
    } else {
      const int q = (i - 2) * 4 + w;
      p = (const char*)fk + (size_t)xq * 80 * S * 2 + (size_t)q * 8192 + lane * 128;
    }
    u32 sink;
    asm volatile("global_load_dword %0, %1, off\n\ts_waitcnt vmcnt(0)" : "=&v"(sink) : "v"(p) : "memory");
  } else {
    const int rbase = (b - K0_TILES) * 16 + w * 4;
    u32x4 xa[4], xb[4];
#pragma unroll
    for (int r = 0; r < 4; ++r) {
      xa[r] = *(const u32x4*)(x + (size_t)(rbase + r) * D + lane * 8);
      xb[r] = *(const u32x4*)(x + (size_t)(rbase + r) * D + 512 + (lane & 15) * 8);
    }
    const float mk = lane < 16 ? 1.f : 0.f;
    float mean[4], q[4];
#pragma unroll
    for (int r = 0; r < 4; ++r) {
      float s = 0.f, s2 = 0.f;
#pragma unroll
      for (int j = 0; j < 4; ++j) { s += bflo(xa[r][j]) + bfhi(xa[r][j]); s2 += bflo(xb[r][j]) + bfhi(xb[r][j]); }
      mean[r] = wave_sum(s + mk * s2) * (1.0f / D);
    }
#pragma unroll
    for (int r = 0; r < 4; ++r) {
      float s = 0.f, s2 = 0.f;
#pragma unroll
      for (int j = 0; j < 4; ++j) {
        float a = bflo(xa[r][j]) - mean[r], c = bfhi(xa[r][j]) - mean[r]; s += a * a + c * c;
        a = bflo(xb[r][j]) - mean[r]; c = bfhi(xb[r][j]) - mean[r]; s2 += a * a + c * c;
      }
      q[r] = wave_sum(s + mk * s2) * (1.0f / D);
    }
    STAMP(0, 1);
    if (lane < 2) {
      const float m0 = lane ? mean[2] : mean[0], m1 = lane ? mean[3] : mean[1], v0 = lane ? q[2] : q[0], v1 = lane ? q[3] : q[1];
      const float sd0 = rbf(sqrtf(rbf(rbf(v0) + 1e-5f))), sd1 = rbf(sqrtf(rbf(rbf(v1) + 1e-5f)));
      const f32x4 o = {rbf(m0), 1.0f / sd0, rbf(m1), 1.0f / sd1};
      ST16(rs, WS_ST1 + (size_t)(rbase + 2 * lane) * 8, __builtin_bit_cast(u32x4, o));
    }
    STAMP(0, 2);
  }
  if (b == K0_TILES + K0_STATS - 1) { ST16(rs, WS_FLG + t * 16, ((u32x4){0u, 0u, 0u, 0u})); ST16(rs, WS_FLP + t * 16, ((u32x4){0u, 0u, 0u, 0u})); }
  STAMP_FLUSH(0);
}

#define POLL_SLEEP 1
typedef __attribute__((address_space(1))) unsigned gu32;
__device__ __forceinline__ void publish(char* ws, size_t off) {
  WAIT_VM(0); BAR();
  if (threadIdx.x == 0) __hip_atomic_store((gu32*)(ws + off), 1u, __ATOMIC_RELAXED, __HIP_MEMORY_SCOPE_AGENT);
}
__device__ __forceinline__ void await_flags(char* ws, size_t off, int n, int lane) {
  const unsigned long long t0 = __builtin_amdgcn_s_memrealtime();
  for (;;) {
    bool ok = true;
#pragma unroll
    for (int i = 0; i < 4; ++i) {
      const int idx = lane + 64 * i;
      if (64 * i < n) { const unsigned v = __hip_atomic_load((gu32*)(ws + off + (size_t)(idx < n ? idx : 0) * 16), __ATOMIC_RELAXED, __HIP_MEMORY_SCOPE_AGENT); ok &= v == 1u; }
    }
    if (__all(ok)) break;
    if (__builtin_amdgcn_s_memrealtime() - t0 > 400000ull) break;
    __builtin_amdgcn_s_sleep(POLL_SLEEP);
  }
  asm volatile("" ::: "memory");
}

static constexpr int CH_BYTES = 13568, CH_F = 0, CH_U1 = 4224, CH_U0 = 4224 + 4608 + 32;
static constexpr int K1_YL = 4 * CH_BYTES;
static constexpr int K1_YP = 8192 + 64;
#define K1_DELAY 80
static constexpr int K1_CONV = 160, K1_W1 = 54, K1_W2 = 30;

template <bool W1>
__device__ __forceinline__ void retile_job(LAS char* lds, const u16* __restrict__ src, int pitch, int cg, int mat, int ks0, char* ws, int w, int lane) {
  constexpr int NU = W1 ? 10 : 9;
  const auto rs = MAKE_RSRC(ws);
#pragma unroll
  for (int i = 0; i < NU; ++i) {
    const int pc = w + 8 * i, row = 8 * pc + (lane >> 3);
    dma16(src + (size_t)(ks0 * 32 + row) * pitch + (((lane & 7) ^ (row & 7)) * 8), lds + pc * 1024);
  }
  WAIT_VM(0); BAR();
  const int g = lane >> 4, q = (lane & 15) >> 2, p = lane & 3, fb = w & 3;
  const u32 l0 = (u32)(size_t)lds;
  u16x4 lo[NU], hi[NU];
#pragma unroll
  for (int i = 0; i < NU; ++i) {
    const int ks = (w >> 2) + 2 * i, row0 = 32 * ks + 8 * g + q, row1 = row0 + 4, chk = 2 * fb + (p >> 1);
    lo[i] = tr_read(l0 + row0 * 128 + ((chk ^ (row0 & 7)) * 16) + 8 * (p & 1));
    hi[i] = tr_read(l0 + row1 * 128 + ((chk ^ (row1 & 7)) * 16) + 8 * (p & 1));
  }
#pragma unroll
  for (int i = 0; i < NU; ++i) asm volatile("s_waitcnt lgkmcnt(0)" : "+v"(lo[i]), "+v"(hi[i])::"memory");
  float m = 0.f;
  if (W1) {
    float cs = 0.f;
#pragma unroll
    for (int i = 0; i < NU; ++i) {
      const u32x2 l2 = __builtin_bit_cast(u32x2, lo[i]), h2 = __builtin_bit_cast(u32x2, hi[i]);
      cs = dot2(l2.x, 0x3f803f80u, dot2(l2.y, 0x3f803f80u, dot2(h2.x, 0x3f803f80u, dot2(h2.y, 0x3f803f80u, cs))));
    }
    cs += __shfl_xor(cs, 16); cs += __shfl_xor(cs, 32);
    LAS float* red = (LAS float*)(lds + 80 * 1024);
    if (lane < 16) red[w * 16 + lane] = cs;
    WAIT_LGKM0(); BAR();
    m = (red[fb * 16 + (lane & 15)] + red[(fb + 4) * 16 + (lane & 15)]) * (1.0f / D);
  }
#pragma unroll
  for (int i = 0; i < NU; ++i) {
    const int ks = (w >> 2) + 2 * i;
    const u32x2 l2 = __builtin_bit_cast(u32x2, lo[i]), h2 = __builtin_bit_cast(u32x2, hi[i]);
    if (W1) {
      const u32x4 o = {pack2(bflo(l2.x) - m, bfhi(l2.x) - m), pack2(bflo(l2.y) - m, bfhi(l2.y) - m), pack2(bflo(h2.x) - m, bfhi(h2.x) - m), pack2(bflo(h2.y) - m, bfhi(h2.y) - m)};
      const int blk = cg * 4 + fb;
      ST16(rs, WS_W1T + ((size_t)(blk * NKS1 + ks) * 2 + mat) * 1024 + lane * 16, o);
    } else {
      const int df = cg * 4 + fb, dt = df / 5, j = df % 5;
      ST16(rs, WS_W2T + ((size_t)(dt * NKS2P + ks0 + ks) * 5 + j) * 1024 + lane * 16, ((u32x4){l2.x, l2.y, h2.x, h2.y}));
    }
  }
  if (!W1 && ks0 == 36 && cg < 8) {
#pragma unroll
    for (int i = 0; i < 2; ++i) { const int pc = w + 8 * i; if (pc < 10) ST16(rs, WS_W2T + ((size_t)(cg * NKS2P + NKS2) * 5 + pc) * 1024 + lane * 16, ((u32x4){0u, 0u, 0u, 0u})); }
  }
  if (W1 && cg == 26 && mat == 0) {
#pragma unroll
    for (int i = 0; i < 3; ++i) { const int ks = w + 8 * i; if (ks < NKS1) ST16(rs, WS_W1T + ((size_t)(108 * NKS1 + ks) * 2) * 1024 + lane * 16, ((u32x4){0x3f803f80u, 0x3f803f80u, 0x3f803f80u, 0x3f803f80u})); }
  }
}

#define SARG
#define SPASS

__device__ __forceinline__ void conv_phase(LAS char* lds, const u16* __restrict__ x, const u16* __restrict__ fk, const u16* __restrict__ sD, char* __restrict__ ws, int b, int t, int lane, int w SARG) {
  const auto rs = MAKE_RSRC(ws);
  const int cgp = (b & 7) * 20 + (b >> 3);
  const int c = w & 3, hf = w >> 2, d = cgp * 4 + c;
  LAS char* cb = lds + c * CH_BYTES;
  {
    const char* gF = (const char*)fk + (size_t)d * S * 2 + lane * 16;
    const char* g1 = ws + WS_UR1 + (size_t)d * S * 2 + lane * 16;
#pragma unroll
    for (int i = 0; i < 2; ++i) {
      const int pc = hf * 2 + i;
      dma16(g1 + pc * 1024, cb + CH_U1 + pc * 1024);
      dma16(gF + pc * 1024, cb + CH_F + 64 + pc * 1024);
    }
    if (hf == 0) { if (lane < 32) *(LAS u32x4*)(cb + CH_U1 + 4096 + lane * 16) = (u32x4){0, 0, 0, 0}; if (lane < 4) *(LAS u32x4*)(cb + CH_F + lane * 16) = (u32x4){0, 0, 0, 0}; }
    else { if (lane < 32) *(LAS u32x4*)(cb + CH_U0 + 4096 + lane * 16) = (u32x4){0, 0, 0, 0}; }
  }
  const u32x2 dw = *(const u32x2*)(sD + cgp * 4);
  WAIT_VM(0); WAIT_LGKM0(); BAR();
#pragma unroll
  for (int i = 0; i < 2; ++i) {
    const int p0 = 1024 * hf + 512 * i + 8 * lane;
    const u32x4 in = *(LAS const u32x4*)(cb + CH_U1 + p0 * 2); const u32 nx = *(LAS const u32*)(cb + CH_U1 + p0 * 2 + 16);
    *(LAS u32x4*)(cb + CH_U0 + p0 * 2) = (u32x4){__builtin_amdgcn_alignbit(in.y, in.x, 16), __builtin_amdgcn_alignbit(in.z, in.y, 16), __builtin_amdgcn_alignbit(in.w, in.z, 16), __builtin_amdgcn_alignbit(nx, in.w, 16)};
  }
  WAIT_LGKM0(); BAR();
  STAMP(1, 1);
  u32x2 xr[4]; u32x4 stv[2];
  const char* gx = (const char*)x + (size_t)(2 * t) * D * 2 + cgp * 8;
#define C_LDX(i) do { \
      if ((i) == 0) asm volatile("global_load_dwordx2 %0, %1, off" : "=v"(xr[0]) : "v"(gx) : "memory"); \
      if ((i) == 1) asm volatile("global_load_dwordx2 %0, %1, off offset:1280" : "=v"(xr[1]) : "v"(gx) : "memory"); \
      if ((i) == 2) asm volatile("global_load_dwordx2 %0, %1, off" : "=v"(xr[2]) : "v"(gx + (size_t)1024 * D * 2) : "memory"); \
      if ((i) == 3) asm volatile("global_load_dwordx2 %0, %1, off offset:1280" : "=v"(xr[3]) : "v"(gx + (size_t)1024 * D * 2) : "memory"); \
      if ((i) == 4) GLD16(stv[0], t * 16, ws + WS_ST1, 0); \
      if ((i) == 5) GLD16(stv[1], t * 16, ws + WS_ST1 + 8192, 0); } while (0)
#define C_XWAIT() asm volatile("s_waitcnt vmcnt(0) lgkmcnt(0)" : "+v"(xr[0]), "+v"(xr[1]), "+v"(xr[2]), "+v"(xr[3]), "+v"(stv[0]), "+v"(stv[1])::"memory")
  if (hf == 0) {
    const int r = lane & 15, g = lane >> 4;
    f32x4 acc[8];
    const int a = r >> 1, e = r & 1;
    const u32 ub = (u32)(size_t)(cb + (e ? CH_U1 : CH_U0) + 448 - 64 * a + 16 * g);
    u32 fbp[4];
#pragma unroll
    for (int i = 0; i < 4; ++i) fbp[i] = (u32)(size_t)(cb + CH_F + 16 * g + 4 * r + 4) + 1024 * i;
    u32x4 U[2][8]; u32x2 Fr[2][8][2];
#define C_RDU(buf, i, cc) DS_B128(U[buf][i], ub, 512 * (7 - (i)) + 64 * (cc))
#define C_RDF(buf, i, cc, hh) asm volatile("ds_read2_b32 %0, %1 offset0:%2 offset1:%3" : "=v"(Fr[buf][i][hh]) : "v"(fbp[(i) >> 1]), "n"(128 * ((i) & 1) + 16 * (cc) + 2 * (hh)), "n"(128 * ((i) & 1) + 16 * (cc) + 2 * (hh) + 1))
#define C_F(buf, i) ((u32x4){Fr[buf][i][0].x, Fr[buf][i][0].y, Fr[buf][i][1].x, Fr[buf][i][1].y})
#define C_WAIT(buf) do { \
      asm volatile("s_waitcnt lgkmcnt(0)" : "+v"(U[buf][0]), "+v"(U[buf][1]), "+v"(U[buf][2]), "+v"(U[buf][3]), "+v"(U[buf][4]), "+v"(U[buf][5]), "+v"(U[buf][6]), "+v"(U[buf][7])::"memory"); \
      asm volatile("" : "+v"(Fr[buf][0][0]), "+v"(Fr[buf][0][1]), "+v"(Fr[buf][1][0]), "+v"(Fr[buf][1][1]), "+v"(Fr[buf][2][0]), "+v"(Fr[buf][2][1]), "+v"(Fr[buf][3][0]), "+v"(Fr[buf][3][1]), \
                        "+v"(Fr[buf][4][0]), "+v"(Fr[buf][4][1]), "+v"(Fr[buf][5][0]), "+v"(Fr[buf][5][1]), "+v"(Fr[buf][6][0]), "+v"(Fr[buf][6][1]), "+v"(Fr[buf][7][0]), "+v"(Fr[buf][7][1])); } while (0)
#pragma unroll
    for (int i = 0; i < 8; ++i) { C_RDF(0, i, 0, 0); C_RDF(0, i, 0, 1); C_RDU(0, i, 0); }
    C_WAIT(0);
#pragma unroll
    for (int cc = 0; cc < 8; ++cc) {
      const int cur = cc & 1, nxt = cur ^ 1;
      int n = 0;
#pragma unroll
      for (int c1 = 0; c1 < 8; ++c1)
#pragma unroll
        for (int i = c1; i < 8; ++i) {
          if (cc == 0 && c1 == 0) MFMA_Z(acc[i], U[cur][i - c1], C_F(cur, c1)); else MFMA_IP(acc[i], U[cur][i - c1], C_F(cur, c1));
          if (cc < 7 && n < 24) { const int j = n / 3, kk = n % 3; if (kk == 0) C_RDU(nxt, j, cc + 1); else C_RDF(nxt, j, cc + 1, kk - 1); }
          if (cc < 6 && n == 28) C_LDX(cc);
          ++n;
        }
      if (cc < 7) C_WAIT(nxt);
    }
    asm volatile("s_nop 15\n\ts_nop 15" ::: "memory");
    STAMP(1, 2);
    LAS char* yl = lds + K1_YL + c * K1_YP;
#pragma unroll
    for (int j = 0; j < 8; ++j)
#pragma unroll
      for (int hh = 0; hh < 2; ++hh) *(LAS f32x2*)(yl + (256 * j + 32 * (2 * g + hh) + 2 * r) * 4) = (f32x2){acc[j][2 * hh], acc[j][2 * hh + 1]};
    C_XWAIT();
  } else {
    C_LDX(0); C_LDX(1); C_LDX(2); C_LDX(3); C_LDX(4); C_LDX(5);
    C_XWAIT();
  }
  BAR();
  STAMP(1, 3);
#pragma unroll
  for (int i = 0; i < 2; ++i) {
    const int s = 2 * t + 1024 * i;
    f32x2 y2[4];
    const float Dd[4] = {bflo(dw.x), bfhi(dw.x), bflo(dw.y), bfhi(dw.y)};
#pragma unroll
    for (int cc = 0; cc < 4; ++cc) {
      const f32x2 y = *(LAS const f32x2*)(lds + K1_YL + cc * K1_YP + s * 4);
      const u32 uw = *(LAS const u32*)(lds + cc * CH_BYTES + CH_U1 + (2046 - s) * 2);
      y2[cc] = (f32x2){y[0] + bfhi(uw) * Dd[cc], y[1] + bflo(uw) * Dd[cc]};
    }
    const f32x4 st = __builtin_bit_cast(f32x4, stv[i]);
    const u32x2 x0 = xr[2 * i], x1 = xr[2 * i + 1];
    const float xa[4] = {bflo(x0.x), bfhi(x0.x), bflo(x0.y), bfhi(x0.y)}, xb[4] = {bflo(x1.x), bfhi(x1.x), bflo(x1.y), bfhi(x1.y)};
    float ha[4], hb[4];
#pragma unroll
    for (int cc = 0; cc < 4; ++cc) { ha[cc] = xa[cc] + (xa[cc] - st[0]) * st[1] * y2[cc][0]; hb[cc] = xb[cc] + (xb[cc] - st[2]) * st[3] * y2[cc][1]; }
    ST16(rs, WS_HB + ((size_t)cgp * S + s) * 8, ((u32x4){pack2(ha[0], ha[1]), pack2(ha[2], ha[3]), pack2(hb[0], hb[1]), pack2(hb[2], hb[3])}));
  }
  STAMP(1, 4);
}

#define DMA_SC1(m0v, voffv, sbase, off) asm volatile("s_mov_b32 m0, %0\n\ts_nop 0\n\tglobal_load_lds_dwordx4 %1, %2 offset:%3 sc1" :: "s"(m0v), "v"(voffv), "s"(sbase), "n"(off) : "memory")
#define DMA_PL(m0v, voffv, sbase, off) asm volatile("s_mov_b32 m0, %0\n\ts_nop 0\n\tglobal_load_lds_dwordx4 %1, %2 offset:%3" :: "s"(m0v), "v"(voffv), "s"(sbase), "n"(off) : "memory")
#define GLD16C(dst, voff, sbase, off) asm volatile("global_load_dwordx4 %0, %1, %2 offset:%3 sc1" : "=v"(dst) : "v"(voff), "s"(sbase), "n"(off) : "memory")
static constexpr int G1_NS = 12, G1_SLOT = 8192, G1_WR = 4, G1_RING = 64 * 1024;
#define G1_EARLY 1
#define G1_PACE 0
static constexpr int KF_CNT = 60 * 1024;
__host__ __device__ constexpr int g2_slot(int kq, int s) { return s < 3 ? kq * 15360 + s * 5120 : G1_RING + kq * 20480 + (s - 3) * 5120; }
template <int PACE>
__device__ __forceinline__ void gemm2_prefetch(LAS char* lds, char* __restrict__ ws, int dt, int lane, int first, int last, int step) {
  const u32 lds0 = (u32)(size_t)lds, voff = lane * 16;
  const char* wb = ws + WS_W2T + (size_t)dt * NKS2P * 5 * 1024;
  for (int pc = first; pc < last; pc += step) {
    int kq, o, dst;
    if (pc < 60) { kq = pc / 15; o = (pc - 15 * kq) * 1024; dst = kq * 15360 + o; }
    else { const int q = pc - 60; kq = q / 20; o = 15360 + (q - 20 * kq) * 1024; dst = G1_RING + kq * 20480 + (q - 20 * kq) * 1024; }
    const u32 m0v = __builtin_amdgcn_readfirstlane(lds0 + dst);
    const char* sb = wb + kq * 14 * 5120 + o;
    DMA_PL(m0v, voff, sb, 0);
    if (PACE) __builtin_amdgcn_s_sleep(PACE);
  }
}
template <int PACE>
__device__ __forceinline__ void gemm2_wq_issue(char* __restrict__ ws, int dt, int lane, int w, u32x4 (&wq)[7][3]) {
  const char* wsb = ws + WS_W2T + ((size_t)dt * NKS2P + (w >> 1) * 14 + 7) * 5120 + (w & 1) * 3072;
  const u32 voff = lane * 16;
  const int sh = __builtin_amdgcn_readfirstlane(w & 1);
#pragma unroll
  for (int i = 0; i < 7; ++i) {
    GLD16(wq[i][0], voff, wsb + i * 5120, 0); GLD16(wq[i][1], voff, wsb + i * 5120, 1024);
    asm volatile("s_cmp_lg_u32 %3, 0\n\ts_cbranch_scc1 1f\n\tglobal_load_dwordx4 %0, %1, %2 offset:2048\n1:" : "=&v"(wq[i][2]) : "v"(voff), "s"(wsb + i * 5120), "s"(sh) : "scc", "memory");
    if (PACE) __builtin_amdgcn_s_sleep(PACE);
  }
}
__device__ __forceinline__ void gemm2_wq_wait(u32x4 (&wq)[7][3]) {
  asm volatile("s_waitcnt vmcnt(0)" : "+v"(wq[0][0]), "+v"(wq[0][1]), "+v"(wq[0][2]), "+v"(wq[1][0]), "+v"(wq[1][1]), "+v"(wq[1][2]), "+v"(wq[2][0]), "+v"(wq[2][1]), "+v"(wq[2][2]),
                                      "+v"(wq[3][0]), "+v"(wq[3][1]), "+v"(wq[3][2])::"memory");
  asm volatile("" : "+v"(wq[4][0]), "+v"(wq[4][1]), "+v"(wq[4][2]), "+v"(wq[5][0]), "+v"(wq[5][1]), "+v"(wq[5][2]), "+v"(wq[6][0]), "+v"(wq[6][1]), "+v"(wq[6][2]));
}

__device__ __forceinline__ void gemm1_phase(LAS char* lds, char* __restrict__ ws, int rt, int ct, int dt2, int lane, int w, unsigned tag, size_t flag_off, u32x4 (&wq)[7][3] SARG) {
  const u32 lds0 = (u32)(size_t)lds + G1_RING;
  const int s0 = rt * 128;
  const u32 voff = lane * 16;
  const auto rs = MAKE_RSRC(ws);
  if (w >= 4) {
    const int j = w - 4;
    const u32 hv = voff ^ ((j & 1) * 128);
    const char* hsb = ws + WS_HB + ((size_t)(2 * j) * S + s0) * 8;
    const u32 ldsw = lds0 + 2 * j * 1024;
#define G1_SLAB(ks) do { DMA_PL(ldsw + ((ks) % G1_NS) * G1_SLOT, hv, hsb + (size_t)(ks) * 8 * S * 8, 0); DMA_PL(ldsw + ((ks) % G1_NS) * G1_SLOT + 1024, hv, hsb + (size_t)(ks) * 8 * S * 8 + S * 8, 0); if ((ks) > 8 && G1_PACE) __builtin_amdgcn_s_sleep(G1_PACE); } while (0)
    G1_SLAB(0); G1_SLAB(1); G1_SLAB(2); G1_SLAB(3); G1_SLAB(4);
    WAIT_VM(8); BAR();
    G1_SLAB(5); G1_SLAB(6); G1_SLAB(7); G1_SLAB(8);
    WAIT_VM(8); BAR();
    if (G1_EARLY) gemm2_prefetch<G1_PACE>(lds, ws, dt2, lane, j, 20, 4);
    G1_SLAB(9); G1_SLAB(10); G1_SLAB(11);
    WAIT_VM(6); BAR();
    if (G1_EARLY) gemm2_prefetch<G1_PACE>(lds, ws, dt2, lane, 20 + j, 40, 4);
    G1_SLAB(12); G1_SLAB(13); G1_SLAB(14); G1_SLAB(15);
    WAIT_VM(6); BAR();
    if (G1_EARLY) gemm2_prefetch<G1_PACE>(lds, ws, dt2, lane, 40 + j, 60, 4);
    G1_SLAB(16); G1_SLAB(17); G1_SLAB(18); G1_SLAB(19);
    WAIT_VM(6); BAR();
    gemm2_wq_issue<G1_PACE>(ws, dt2, lane, w, wq);
    WAIT_VM(14); BAR();
    BAR();
    gemm2_prefetch<0>(lds, ws, dt2, lane, (G1_EARLY ? 60 : 0) + j, 140, 4);
    gemm2_wq_wait(wq);
  } else {
    const int blk0 = ct * 8 + 2 * w;
    const char* wsA = ws + WS_W1T + (size_t)(blk0 > 108 ? 108 : blk0) * NKS1 * 2048;
    const char* wsB = ws + WS_W1T + (size_t)(blk0 + 1 > 108 ? 108 : blk0 + 1) * NKS1 * 2048;
    const int g = lane >> 4;
    const u32 hbase = lds0 + g * 2048 + (lane & 15) * 8;
    const u32 hoffE = hbase + (g & 1) * 128, hoffO = hbase + (1 - (g & 1)) * 128 - 128;
    const u32 hoffE2 = hoffE + 65536, hoffO2 = hoffO + 65536;
    f32x4 acc[8][4];
    const int stat = __builtin_amdgcn_readfirstlane(blk0 == 108 ? 1 : 0);
#define G1_LDW(ks, i) do { if ((i) == 0) GLD16(wr[(ks) % G1_WR][0], voff, wsA + (ks) * 2048, 0); if ((i) == 1) GLD16(wr[(ks) % G1_WR][1], voff, wsA + (ks) * 2048, 1024); \
      if ((i) == 2) GLD16(wr[(ks) % G1_WR][2], voff, wsB + (ks) * 2048, 0); if ((i) == 3) GLD16(wr[(ks) % G1_WR][3], voff, wsB + (ks) * 2048, 1024); } while (0)
#define G1_RD(sf, hh, ks) DS_B64(hf[sf][hh], ((ks) % G1_NS) >= 8 ? (((sf) & 1) ? hoffO2 : hoffE2) : (((sf) & 1) ? hoffO : hoffE), (((ks) % G1_NS) & 7) * G1_SLOT + (hh) * 1024 + (sf) * 128)
#define G1_H(sf) ((u32x4){hf[sf][0].x, hf[sf][0].y, hf[sf][1].x, hf[sf][1].y})
#define G1_MM(ks, sf, i, STAT) do { \
      if (STAT) { if ((i) == 0) { if ((ks) == 0) MFMA_Z(acc[sf][0], wr[0][0], G1_H(sf)); else MFMA_IP(acc[sf][0], wr[(ks) % G1_WR][0], G1_H(sf)); } \
                  if ((i) == 2) { if ((ks) == 0) MFMA_Z(acc[sf][2], G1_H(sf), G1_H(sf)); else MFMA_IP(acc[sf][2], G1_H(sf), G1_H(sf)); } } \
      else { if ((ks) == 0) MFMA_Z(acc[sf][i], wr[0][i], G1_H(sf)); else MFMA_IP(acc[sf][i], wr[(ks) % G1_WR][i], G1_H(sf)); } } while (0)
#define G1_BODY(STAT) { \
      u32x4 wr[G1_WR][4]; u32x2 hf[8][2]; \
      _Pragma("unroll") for (int ks = 0; ks < 3; ++ks) { G1_LDW(ks, 0); G1_LDW(ks, 1); G1_LDW(ks, 2); G1_LDW(ks, 3); } \
      BAR();                                                   \
      STAMP(1, 7); \
      _Pragma("unroll") for (int sf = 0; sf < 7; ++sf) { G1_RD(sf, 0, 0); G1_RD(sf, 1, 0); } \
      _Pragma("unroll") for (int ks = 0; ks < NKS1; ++ks) { \
        if ((ks & 3) == 0) BAR(); \
          \
        if (ks + 2 < NKS1) WAIT_VM(8); else if (ks + 1 < NKS1) WAIT_VM(4); else WAIT_VM(0); \
        asm volatile("" : "+v"(wr[ks % G1_WR][0]), "+v"(wr[ks % G1_WR][1]), "+v"(wr[ks % G1_WR][2]), "+v"(wr[ks % G1_WR][3])); \
        _Pragma("unroll") for (int sf = 0; sf < 8; ++sf) { \
            \
          if (ks + 1 < NKS1 || sf == 0) asm volatile("s_waitcnt lgkmcnt(12)" : "+v"(hf[sf][0]), "+v"(hf[sf][1])::"memory"); \
          else if (sf < 7) asm volatile("s_waitcnt lgkmcnt(%2)" : "+v"(hf[sf][0]), "+v"(hf[sf][1]) : "n"(14 - 2 * sf) : "memory"); \
          else asm volatile("s_waitcnt lgkmcnt(0)" : "+v"(hf[sf][0]), "+v"(hf[sf][1])::"memory"); \
          G1_MM(ks, sf, 0, STAT); \
          if (sf == 0) G1_RD(7, 0, ks); else if (ks + 1 < NKS1) G1_RD(sf - 1, 0, ks + 1); \
          G1_MM(ks, sf, 1, STAT); \
          if (sf == 0) G1_RD(7, 1, ks); else if (ks + 1 < NKS1) G1_RD(sf - 1, 1, ks + 1); \
          G1_MM(ks, sf, 2, STAT); \
          if ((sf & 1) && ks + 3 < NKS1) G1_LDW(ks + 3, sf >> 1); \
          G1_MM(ks, sf, 3, STAT); \
        } } }
    if (stat) G1_BODY(true) else G1_BODY(false)
    BAR();
    asm volatile("s_nop 15\n\ts_nop 15" ::: "memory");
    STAMP(1, 8);
    if (stat) {
      const int sx = lane & 15;
#pragma unroll
      for (int sp = 0; sp < 2; ++sp) {
        float r2[4];
#pragma unroll
        for (int q = 0; q < 4; ++q) {
          const int sf = 4 * sp + q;
          const float dg = (sx & 2) ? ((sx & 1) ? acc[sf][2][3] : acc[sf][2][2]) : ((sx & 1) ? acc[sf][2][1] : acc[sf][2][0]);
          const float ssq = __shfl(dg, (sx >> 2) * 16 + sx);
          const float mean = acc[sf][0][0] * (1.0f / D);
          const float var = fmaxf(ssq * (1.0f / D) - mean * mean, 0.f);
          const float r = 1.0f / rbf(sqrtf(rbf(rbf(var) + 1e-5f)));
          r2[q] = r * r;
        }
        const float mine = g == 0 ? r2[0] : (g == 1 ? r2[1] : (g == 2 ? r2[2] : r2[3]));
        const int b4 = lane & ~3;
        const f32x4 o = {__shfl(mine, b4), __shfl(mine, b4 + 1), __shfl(mine, b4 + 2), __shfl(mine, b4 + 3)};
        if ((lane & 3) == 0) ST16(rs, WS_ST2 + (size_t)(s0 + 64 * sp + lane) * 4, __builtin_bit_cast(u32x4, o));
      }
    } else {
#pragma unroll
      for (int bi = 0; bi < 2; ++bi) {
        const int blk = blk0 + bi;
        if (blk < 108) {
          const u32 obase = (u32)(WS_HF + ((size_t)(rt * 8 + (lane >> 5)) * NKS2 + (blk >> 1)) * 1024) + (blk & 1) * 512 + (lane & 31) * 16;
#pragma unroll
          for (int pr = 0; pr < 4; ++pr) {
            const f32x4 hE = relu4(acc[2 * pr][2 * bi]) * acc[2 * pr][2 * bi + 1], hO = relu4(acc[2 * pr + 1][2 * bi]) * acc[2 * pr + 1][2 * bi + 1];
            u32 e0 = pack2(hE[0], hE[1]), e1 = pack2(hE[2], hE[3]), o0 = pack2(hO[0], hO[1]), o1 = pack2(hO[2], hO[3]);
            auto r0 = __builtin_amdgcn_permlane32_swap(e0, o0, false, false); auto r0b = __builtin_amdgcn_permlane16_swap(r0[0], r0[1], false, false);
            auto r1 = __builtin_amdgcn_permlane32_swap(e1, o1, false, false); auto r1b = __builtin_amdgcn_permlane16_swap(r1[0], r1[1], false, false);
            ST16(rs, obase + pr * 2 * NKS2 * 1024, ((u32x4){r0b[0], r1b[0], r0b[1], r1b[1]}));
          }
        }
      }
    }
    WAIT_VM(0);
    unsigned old = 0;
    if (lane == 0) old = __hip_atomic_fetch_add((LAS unsigned*)(lds + KF_CNT), 1u, __ATOMIC_RELAXED, __HIP_MEMORY_SCOPE_WORKGROUP);
    if (lane == 0 && old == 3u) __hip_atomic_store((gu32*)(ws + flag_off), tag, __ATOMIC_RELAXED, __HIP_MEMORY_SCOPE_AGENT);
    STAMP(1, 9);
    gemm2_wq_issue<0>(ws, dt2, lane, w, wq);
    gemm2_wq_wait(wq);
  }
}

__device__ __forceinline__ void gemm2_phase(LAS char* lds, char* __restrict__ ws, u16* __restrict__ out, int rt, int dt, int lane, int w, u32x4 (&wq)[7][3] SARG) {
  const int sh = w & 1, kq = w >> 1, g = lane >> 4;
  const u32 voff = lane * 16, lds0 = (u32)(size_t)lds;
  const char* hA = ws + WS_HF + ((size_t)(rt * 4 + 2 * sh) * NKS2 + kq * 14) * 1024;
  const char* hB = hA + (size_t)NKS2 * 1024;
  const int hclamp = kq == 3 ? 1 : 0;
  u32x4 hq[6][2], wf[2][5];
#define G2_LH(n) do { const int o_ = ((n) >= 12 && hclamp) ? 11 * 1024 : (n) * 1024; GLD16C(hq[(n) % 6][0], voff, hA + o_, 0); GLD16C(hq[(n) % 6][1], voff, hB + o_, 0); } while (0)
  const u32 rlo = lds0 + kq * 15360 + lane * 16, rhi = lds0 + G1_RING + kq * 20480 - 3 * 5120 + lane * 16;
#define G2_SB(n) ((((n) % 7) < 3 ? rlo : rhi))
#define G2_READ(n) do { DS_B128(wf[(n) & 1][0], G2_SB(n), ((n) % 7) * 5120); DS_B128(wf[(n) & 1][1], G2_SB(n), ((n) % 7) * 5120 + 1024); DS_B128(wf[(n) & 1][2], G2_SB(n), ((n) % 7) * 5120 + 2048); \
    DS_B128(wf[(n) & 1][3], G2_SB(n), ((n) % 7) * 5120 + 3072); DS_B128(wf[(n) & 1][4], G2_SB(n), ((n) % 7) * 5120 + 4096); } while (0)
  const int shs = __builtin_amdgcn_readfirstlane(sh);
#define G2_WST(n, i) do { if ((i) < 2) asm volatile("ds_write_b128 %0, %1 offset:%2" :: "v"(G2_SB(n) + sh * 3072), "v"(wq[(n) - 7][i]), "n"(((n) % 7) * 5120 + (i) * 1024) : "memory"); \
    else asm volatile("s_cmp_lg_u32 %3, 0\n\ts_cbranch_scc1 1f\n\tds_write_b128 %0, %1 offset:%2\n1:" :: "v"(G2_SB(n)), "v"(wq[(n) - 7][2]), "n"(((n) % 7) * 5120 + 2048), "s"(shs) : "scc", "memory"); } while (0)
  G2_LH(0); G2_LH(1);
  float r2[3]; u32x2 res[3];
#pragma unroll
  for (int j = 0; j < 3; ++j) {
    const int f = kq + 4 * j < 10 ? kq + 4 * j : kq, srow = rt * 64 + (2 * sh + f / 5) * 16 + (lane & 15);
    asm volatile("global_load_dword %0, %1, off sc1" : "=v"(r2[j]) : "v"(ws + WS_ST2 + (size_t)srow * 4) : "memory");
    asm volatile("global_load_dwordx2 %0, %1, off" : "=v"(res[j]) : "v"(ws + WS_HB + ((size_t)(dt * 20 + (f % 5) * 4 + g) * S + srow) * 8) : "memory");
  }
  G2_LH(2); G2_LH(3); G2_LH(4);
  f32x4 acc[10];
  G2_READ(0);
  WAIT_VM(14);
  STAMP(1, 11);
#define G2_MM(n, m) do { if ((n) == 0) MFMA_Z(acc[m], wf[0][(m) % 5], hq[0][(m) / 5]); else MFMA_IP(acc[m], wf[(n) & 1][(m) % 5], hq[(n) % 6][(m) / 5]); } while (0)
#define G2D(k) do {} while (0)
  u32 g2d[30];
#define G2_STEP(n) do { G2D(2 * (n)); \
    if ((n) == 1) WAIT_VM(14); else if ((n) > 1) { constexpr int iss_ = 5 + (n) < 14 ? 5 + (n) : 14; WAIT_VM(2 * (iss_ - ((n) + 1))); } \
    asm volatile("s_waitcnt lgkmcnt(0)" : "+v"(wf[(n) & 1][0]), "+v"(wf[(n) & 1][1]), "+v"(wf[(n) & 1][2]), "+v"(wf[(n) & 1][3]), "+v"(wf[(n) & 1][4]), "+v"(hq[(n) % 6][0]), "+v"(hq[(n) % 6][1])::"memory"); \
    if (((n) & 1) && (n) <= 9) BAR(); \
    G2D(2 * (n) + 1); \
    if ((n) + 1 < 14) G2_READ(((n) + 1 < 14 ? (n) + 1 : 0)); \
    G2_MM(n, 0); G2_MM(n, 1); if ((n) + 5 < 14) G2_LH(((n) + 5 < 14 ? (n) + 5 : 0)); \
    G2_MM(n, 2); G2_MM(n, 3); if ((n) >= 1 && (n) <= 7) G2_WST(((n) >= 1 && (n) <= 7 ? (n) + 6 : 7), 0); \
    G2_MM(n, 4); G2_MM(n, 5); G2_MM(n, 6); if ((n) >= 1 && (n) <= 7) G2_WST(((n) >= 1 && (n) <= 7 ? (n) + 6 : 7), 1); \
    G2_MM(n, 7); G2_MM(n, 8); if ((n) >= 1 && (n) <= 7) G2_WST(((n) >= 1 && (n) <= 7 ? (n) + 6 : 7), 2); \
    G2_MM(n, 9); } while (0)
  G2_STEP(0); G2_STEP(1); G2_STEP(2); G2_STEP(3); G2_STEP(4); G2_STEP(5); G2_STEP(6);
  G2_STEP(7); G2_STEP(8); G2_STEP(9); G2_STEP(10); G2_STEP(11); G2_STEP(12); G2_STEP(13);
  asm volatile("s_nop 15\n\ts_nop 15" ::: "memory");
  asm volatile("" : "+v"(r2[0]), "+v"(r2[1]), "+v"(r2[2]), "+v"(res[0]), "+v"(res[1]), "+v"(res[2]));
  STAMP(1, 12);
  BAR();
  LAS char* xb = lds + sh * 40960 + lane * 16;
#pragma unroll
  for (int f = 0; f < 10; ++f) *(LAS f32x4*)(xb + kq * 1024 + f * 4096) = acc[f];
  WAIT_LGKM0(); BAR();
  u32 pk[3][2];
#pragma unroll
  for (int j = 0; j < 3; ++j) {
    LAS const char* sl = xb + (kq + 4 * j < 10 ? kq + 4 * j : kq) * 4096;
    const f32x4 a = (*(LAS const f32x4*)(sl) + *(LAS const f32x4*)(sl + 1024)) + (*(LAS const f32x4*)(sl + 2048) + *(LAS const f32x4*)(sl + 3072));
    pk[j][0] = pack2(bflo(res[j].x) + a[0] * r2[j], bfhi(res[j].x) + a[1] * r2[j]);
    pk[j][1] = pack2(bflo(res[j].y) + a[2] * r2[j], bfhi(res[j].y) + a[3] * r2[j]);
  }
  const auto ro = MAKE_RSRC(out);
  {
    auto r0 = __builtin_amdgcn_permlane32_swap(pk[0][0], pk[1][0], false, false); auto r0b = __builtin_amdgcn_permlane16_swap(r0[0], r0[1], false, false);
    auto r1 = __builtin_amdgcn_permlane32_swap(pk[0][1], pk[1][1], false, false); auto r1b = __builtin_amdgcn_permlane16_swap(r1[0], r1[1], false, false);
    const int f = kq + 4 * (lane >> 5);
    ST16(ro, ((size_t)(rt * 64 + (2 * sh + f / 5) * 16 + (lane & 15)) * D + dt * 80 + (f % 5) * 16 + ((lane >> 4) & 1) * 8) * 2, ((u32x4){r0b[0], r1b[0], r0b[1], r1b[1]}));
  }
  if (kq < 2) {
    auto r0 = __builtin_amdgcn_permlane32_swap(pk[2][0], pk[2][0], false, false); auto r0b = __builtin_amdgcn_permlane16_swap(r0[0], r0[1], false, false);
    auto r1 = __builtin_amdgcn_permlane32_swap(pk[2][1], pk[2][1], false, false); auto r1b = __builtin_amdgcn_permlane16_swap(r1[0], r1[1], false, false);
    const int f = kq + 8;
    if (lane < 32) ST16(ro, ((size_t)(rt * 64 + (2 * sh + f / 5) * 16 + (lane & 15)) * D + dt * 80 + (f % 5) * 16 + ((lane >> 4) & 1) * 8) * 2, ((u32x4){r0b[0], r1b[0], r0b[1], r1b[1]}));
  }
}

static constexpr int KF_LDS = 160 * 1024, K1_LDS = 96 * 1024;
#define CODE_PF2 29000
__global__ __launch_bounds__(512, 2) void k1_conv(const u16* __restrict__ x, const u16* __restrict__ fk, const u16* __restrict__ sD, const u16* __restrict__ wg,
                                                  const u16* __restrict__ wu, const u16* __restrict__ wd, char* __restrict__ ws) {
  extern __shared__ __attribute__((aligned(16))) char shm[];
  LAS char* lds = (LAS char*)shm;
  const int t = threadIdx.x, lane = t & 63, w = __builtin_amdgcn_readfirstlane(t >> 6), b = blockIdx.x;
  STAMP_DECL; STAMP(1, 0);
  if (b < K1_CONV) conv_phase(lds, x, fk, sD, ws, b, t, lane, w SPASS);
  else if (b < K1_CONV + K1_W1 + K1_W2) {
    { const unsigned long long t0 = __builtin_amdgcn_s_memrealtime(); while (__builtin_amdgcn_s_memrealtime() - t0 < K1_DELAY) __builtin_amdgcn_s_sleep(4); }
    const int jb = b - K1_CONV;
    if (jb < K1_W1) retile_job<true>(lds, ((jb & 1) ? wu : wg) + (jb >> 1) * 64, F, jb >> 1, jb & 1, 0, ws, w, lane);
    else { const int j2 = jb - K1_W1; retile_job<false>(lds, wd + (j2 / 3) * 64, D, j2 / 3, 0, (j2 % 3) * 18, ws, w, lane); }
  } else if (CODE_PF2 > 0 && w * 8192 < CODE_PF2) {
    unsigned long long pc;
    asm volatile("s_getpc_b64 s[40:41]\n\ts_add_u32 s40, s40, _Z6k2_ffnPcPt@rel32@lo+4\n\ts_addc_u32 s41, s41, _Z6k2_ffnPcPt@rel32@hi+12\n\ts_mov_b64 %0, s[40:41]" : "=s"(pc) :: "scc", "s40", "s41");
    int o = w * 8192 + lane * 128; o = o < CODE_PF2 - 4 ? o : CODE_PF2 - 4;
    u32 sink;
    asm volatile("global_load_dword %0, %1, off\n\ts_waitcnt vmcnt(0)" : "=&v"(sink) : "v"((const char*)pc + (o & ~3)) : "memory");
  }
  STAMP(1, 5);
  STAMP_FLUSH(1);
}

__global__ __launch_bounds__(512, 2) void k2_ffn(char* __restrict__ ws, u16* __restrict__ out) {
  extern __shared__ __attribute__((aligned(16))) char shm[];
  LAS char* lds = (LAS char*)shm;
  const int t = threadIdx.x, lane = t & 63, w = __builtin_amdgcn_readfirstlane(t >> 6), b = blockIdx.x;
  STAMP_DECL; STAMP(2, 0);
  const int xg = b & 7, ix = b >> 3;
  const int p = (xg & 3) * 4 + (ix & 3), j = (xg >> 2) * 8 + (ix >> 2);
  if (t == 0) *(LAS unsigned*)(lds + KF_CNT) = 0u;
  u32x4 wq[7][3];
  if (j < 14) gemm1_phase(lds, ws, p, j, j >> 1, lane, w, 1u, WS_FLP + (size_t)(p * 16 + j) * 16, wq SPASS);
  else {
    gemm2_prefetch<0>(lds, ws, j >> 1, lane, w, 140, 8);
    gemm2_wq_issue<0>(ws, j >> 1, lane, w, wq);
    gemm2_wq_wait(wq);
  }
  if (w == 4) await_flags(ws, WS_FLP + (size_t)p * 256, 14, lane);
  BAR();
  STAMP(2, 10);
  gemm2_phase(lds, ws, out, p * 2 + (j & 1), j >> 1, lane, w, wq SPASS);
  STAMP(2, 13);
  STAMP_FLUSH(2);
}

extern "C" void kernel_launch(void* const* d_in, const int* in_sizes, int n_in, void* d_out, int out_size, void* d_ws, size_t ws_size, hipStream_t stream) {
  const u16* q = (const u16*)d_in[0]; const u16* k = (const u16*)d_in[1]; const u16* v = (const u16*)d_in[2];
  const u16* shk = (const u16*)d_in[3]; const u16* fk = (const u16*)d_in[4]; const u16* sD = (const u16*)d_in[5];
  const u16* wg = (const u16*)d_in[7]; const u16* wu = (const u16*)d_in[8]; const u16* wd = (const u16*)d_in[9];
  char* ws = (char*)d_ws;
  (void)hipFuncSetAttribute((const void*)k0_prep, hipFuncAttributeMaxDynamicSharedMemorySize, K0_LDS);
  (void)hipFuncSetAttribute((const void*)k1_conv, hipFuncAttributeMaxDynamicSharedMemorySize, K1_LDS);
  (void)hipFuncSetAttribute((const void*)k2_ffn, hipFuncAttributeMaxDynamicSharedMemorySize, KF_LDS);
  k0_prep<<<K0_TILES + K0_STATS + K0_NPF, 256, K0_LDS, stream>>>(q, k, v, shk, fk, wg, wu, wd, (const u16*)d_out, ws);
  k1_conv<<<K1_CONV + K1_W1 + K1_W2 + 12, 512, K1_LDS, stream>>>(q, fk, sD, wg, wu, wd, ws);
  k2_ffn<<<256, 512, KF_LDS, stream>>>(ws, (u16*)d_out);
}
```

```cpp
#include <hip/hip_runtime.h>
#include <hip/hip_bf16.h>

typedef unsigned short u16;
typedef unsigned int u32;
typedef __attribute__((ext_vector_type(2))) unsigned int u32x2;
typedef __attribute__((ext_vector_type(4))) unsigned int u32x4;
typedef __attribute__((ext_vector_type(8))) short bf16x8;
typedef __attribute__((ext_vector_type(2))) __bf16 bf16x2;
typedef __attribute__((ext_vector_type(2))) float f32x2;
typedef __attribute__((ext_vector_type(4))) float f32x4;
typedef __attribute__((ext_vector_type(4))) unsigned short u16x4;
#define LAS __attribute__((address_space(3)))

static constexpr int S = 2048, D = 640, F = 1728;
static constexpr int NKS1 = D / 32;
static constexpr int NKS2 = F / 32;

static constexpr size_t WS_UR1 = 0;
static constexpr size_t WS_HB  = WS_UR1 + (size_t)D * S * 2;
static constexpr size_t WS_ST1 = WS_HB  + (size_t)D * S * 2;
static constexpr size_t WS_ST2 = WS_ST1 + (size_t)S * 8;
static constexpr size_t WS_W1T = WS_ST2 + (size_t)16 * 1024;
static constexpr int NKS2P = 56;
static constexpr size_t WS_W2T = WS_W1T + (size_t)14 * 8 * NKS1 * 2048;
static constexpr size_t WS_HF  = WS_W2T + (size_t)8 * NKS2P * 5 * 1024;
static constexpr size_t WS_FLG = WS_HF  + (size_t)128 * NKS2 * 1024;
static constexpr size_t WS_FLP = WS_FLG + 4096;
static constexpr size_t WS_FL0 = WS_FLP + 4096;
static constexpr size_t WS_EPO = WS_FL0 + 4096;
static constexpr size_t WS_END = WS_EPO + 256;

__device__ __forceinline__ float bflo(u32 w) { return __uint_as_float(w << 16); }
__device__ __forceinline__ float bfhi(u32 w) { return __uint_as_float(w & 0xffff0000u); }
__device__ __forceinline__ u32 pack2(float lo, float hi) { f32x2 v = {lo, hi}; bf16x2 b = __builtin_convertvector(v, bf16x2); return __builtin_bit_cast(u32, b); }
__device__ __forceinline__ float rbf(float x) { return bflo(pack2(x, 0.f)); }
__device__ __forceinline__ float dot2(u32 a, u32 b, float c) { return __builtin_amdgcn_fdot2_f32_bf16(__builtin_bit_cast(bf16x2, a), __builtin_bit_cast(bf16x2, b), c, false); }
__device__ __forceinline__ f32x4 mfma16(u32x4 a, u32x4 b, f32x4 c) { return __builtin_amdgcn_mfma_f32_16x16x32_bf16(__builtin_bit_cast(bf16x8, a), __builtin_bit_cast(bf16x8, b), c, 0, 0, 0); }
__device__ __forceinline__ void dma16(const void* g, LAS void* l) { __builtin_amdgcn_global_load_lds((const u32*)g, (LAS u32*)l, 16, 0, 0); }
#define WAIT_VM(n) asm volatile("s_waitcnt vmcnt(%0)" ::"n"(n) : "memory")
#define WAIT_LGKM0() asm volatile("s_waitcnt lgkmcnt(0)" ::: "memory")
#define BAR() do { asm volatile("" ::: "memory"); __builtin_amdgcn_s_barrier(); asm volatile("" ::: "memory"); } while (0)
#define SB() __builtin_amdgcn_sched_barrier(0)
#define MEMFENCE() asm volatile("" ::: "memory")
#define MAKE_RSRC(p) __builtin_amdgcn_make_buffer_rsrc((void*)(p), 0, 0x7fffffff, 0x00020000)
#define ST16(rs, off, v) __builtin_amdgcn_raw_buffer_store_b128((v), (rs), (int)(off), 0, 16)

template <int CTRL, int RM> __device__ __forceinline__ float dpp_add(float v) { return v + __builtin_bit_cast(float, __builtin_amdgcn_update_dpp(0, __builtin_bit_cast(int, v), CTRL, RM, 0xf, true)); }
__device__ __forceinline__ float wave_sum(float v) {
  v = dpp_add<0x111, 0xf>(v); v = dpp_add<0x112, 0xf>(v); v = dpp_add<0x114, 0xf>(v); v = dpp_add<0x118, 0xf>(v);
  v = dpp_add<0x142, 0xa>(v); v = dpp_add<0x143, 0xc>(v);
  return __builtin_bit_cast(float, __builtin_amdgcn_readlane(__builtin_bit_cast(int, v), 63));
}

#define STAMP(kid, i) do {} while (0)
#define STAMP_DECL do {} while (0)
#define STAMP_FLUSH(kid) do {} while (0)

typedef __attribute__((ext_vector_type(4))) short s16x4;
__device__ __forceinline__ u16x4 tr_read(u32 addr) { u16x4 r; asm volatile("ds_read_b64_tr_b16 %0, %1" : "=v"(r) : "v"(addr) : "memory"); return r; }
#define DS_TR(dst, addr, off) asm volatile("ds_read_b64_tr_b16 %0, %1 offset:%2" : "=v"(dst) : "v"(addr), "n"(off))
#define DS_B64(dst, addr, off) asm volatile("ds_read_b64 %0, %1 offset:%2" : "=v"(dst) : "v"(addr), "n"(off))
#define DS_B128(dst, addr, off) asm volatile("ds_read_b128 %0, %1 offset:%2" : "=v"(dst) : "v"(addr), "n"(off))
#define MFMA_IP(acc, a, b) asm volatile("v_mfma_f32_16x16x32_bf16 %0, %1, %2, %0" : "+v"(acc) : "v"(a), "v"(b))
#define MFMA_Z(acc, a, b) asm volatile("v_mfma_f32_16x16x32_bf16 %0, %1, %2, 0" : "=&v"(acc) : "v"(a), "v"(b))
#define GLD16(dst, voff, sbase, off) asm volatile("global_load_dwordx4 %0, %1, %2 offset:%3" : "=v"(dst) : "v"(voff), "s"(sbase), "n"(off) : "memory")
#define WAIT_LGKM(n) asm volatile("s_waitcnt lgkmcnt(%0)" ::"n"(n) : "memory")

#define POLL_SLEEP 1
typedef __attribute__((address_space(1))) unsigned gu32;
__device__ __forceinline__ void publish(char* ws, size_t off, unsigned tag) {
  WAIT_VM(0); BAR();
  if (threadIdx.x == 0) __hip_atomic_store((gu32*)(ws + off), tag, __ATOMIC_RELAXED, __HIP_MEMORY_SCOPE_AGENT);
}
__device__ __forceinline__ void await_flags(char* ws, size_t off, int n, int lane, unsigned tag) {
  const unsigned long long t0 = __builtin_amdgcn_s_memrealtime();
  for (;;) {
    bool ok = true;
#pragma unroll
    for (int i = 0; i < 4; ++i) {
      const int idx = lane + 64 * i;
      if (64 * i < n) { const unsigned v = __hip_atomic_load((gu32*)(ws + off + (size_t)(idx < n ? idx : 0) * 16), __ATOMIC_RELAXED, __HIP_MEMORY_SCOPE_AGENT); ok &= v == tag; }
    }
    if (__all(ok)) break;
    if (__builtin_amdgcn_s_memrealtime() - t0 > 400000ull) break;
    __builtin_amdgcn_s_sleep(POLL_SLEEP);
  }
  asm volatile("" ::: "memory");
}

#define SARG
#define SPASS
#define DMA_SC1(m0v, voffv, sbase, off) asm volatile("s_mov_b32 m0, %0\n\ts_nop 0\n\tglobal_load_lds_dwordx4 %1, %2 offset:%3 sc1" :: "s"(m0v), "v"(voffv), "s"(sbase), "n"(off) : "memory")
#define GLD16C(dst, voff, sbase, off) asm volatile("global_load_dwordx4 %0, %1, %2 offset:%3 sc1" : "=v"(dst) : "v"(voff), "s"(sbase), "n"(off) : "memory")
static constexpr int P0_RAW = 4 * 13568, P0_V = P0_RAW + 72 * 160, P0_STG = P0_RAW + 23 * 960;
__device__ __forceinline__ void prep_phase(LAS char* lds, const u16* __restrict__ x, const u16* __restrict__ k, const u16* __restrict__ v, const u16* __restrict__ shk,
                                           const u16* __restrict__ fk, char* __restrict__ ws, int b, int t, int lane, int w SARG) {
  const auto rs = MAKE_RSRC(ws);
  const int xq = b & 7, st = b >> 3, s0 = st * 64, c0 = xq * 80;
#pragma unroll
  for (int i = 0; i < 3; ++i) {
    const int pc = w + 8 * i;
    if (pc < 23 && lane < 60) {
      const u16* src = pc < 12 ? k : v;
      int row = (pc < 12 ? s0 - 8 + 6 * pc : s0 + 6 * (pc - 12)) + lane / 10;
      row = row < 0 ? 0 : (row > S - 1 ? S - 1 : row);
      dma16(src + (size_t)row * D + c0 + (lane % 10) * 8, lds + P0_RAW + pc * 960);
    }
  }
  const int srow = b * 8 + w;
  const u32x4 xa = *(const u32x4*)(x + (size_t)srow * D + lane * 8), xb = *(const u32x4*)(x + (size_t)srow * D + 512 + (lane & 15) * 8);
  const int cp = t % 40, rg = t / 40, ch = c0 + 2 * cp;
  const u32x4 sh = *(const u32x4*)(shk + ch * 4);
  WAIT_VM(0); BAR();
  STAMP(1, 14);
  if (b < 160) {
    const int d = ((b & 7) * 20 + (b >> 3)) * 4 + (w & 3), hf = w >> 2;
    const char* gF = (const char*)fk + (size_t)d * S * 2 + lane * 16;
    dma16(gF + (hf * 2) * 1024, lds + (w & 3) * 13568 + 64 + (hf * 2) * 1024);
    dma16(gF + (hf * 2 + 1) * 1024, lds + (w & 3) * 13568 + 64 + (hf * 2 + 1) * 1024);
  }
  if (t < 320) {
    u32 kk[11], vv[8];
    LAS const char* rk = lds + P0_RAW + (8 * rg + 5) * 160 + cp * 4;
#pragma unroll
    for (int i = 0; i < 11; ++i) kk[i] = *(LAS const u32*)(rk + i * 160);
#pragma unroll
    for (int i = 0; i < 8; ++i) vv[i] = *(LAS const u32*)(lds + P0_V + (8 * rg + i) * 160 + cp * 4);
    if (st == 0 && rg == 0) { kk[0] = kk[1] = kk[2] = 0u; }
    const float a0 = bflo(sh.x), a1 = bfhi(sh.x), a2 = bflo(sh.y), a3 = bfhi(sh.y);
    const float b0 = bflo(sh.z), b1 = bfhi(sh.z), b2 = bflo(sh.w), b3 = bfhi(sh.w);
    float ka[11], kb[11];
#pragma unroll
    for (int i = 0; i < 11; ++i) { ka[i] = bflo(kk[i]); kb[i] = bfhi(kk[i]); }
    float ua[8], ub[8];
#pragma unroll
    for (int i = 0; i < 8; ++i) {
      ua[i] = (ka[i] * a3 + ka[i + 1] * a2 + ka[i + 2] * a1 + ka[i + 3] * a0) * bflo(vv[i]);
      ub[i] = (kb[i] * b3 + kb[i + 1] * b2 + kb[i + 2] * b1 + kb[i + 3] * b0) * bfhi(vv[i]);
    }
    LAS char* L1 = lds + P0_STG;
    const int o = (7 - rg) * 16;
    *(LAS u32x4*)(L1 + (2 * cp) * 144 + o) = (u32x4){pack2(ua[7], ua[6]), pack2(ua[5], ua[4]), pack2(ua[3], ua[2]), pack2(ua[1], ua[0])};
    *(LAS u32x4*)(L1 + (2 * cp + 1) * 144 + o) = (u32x4){pack2(ub[7], ub[6]), pack2(ub[5], ub[4]), pack2(ub[3], ub[2]), pack2(ub[1], ub[0])};
  }
  {
    const float mk = lane < 16 ? 1.f : 0.f;
    float s = 0.f, s2 = 0.f;
#pragma unroll
    for (int j = 0; j < 4; ++j) { s += bflo(xa[j]) + bfhi(xa[j]); s2 += bflo(xb[j]) + bfhi(xb[j]); }
    const float mean = wave_sum(s + mk * s2) * (1.0f / D);
    s = 0.f; s2 = 0.f;
#pragma unroll
    for (int j = 0; j < 4; ++j) {
      float a = bflo(xa[j]) - mean, c = bfhi(xa[j]) - mean; s += a * a + c * c;
      a = bflo(xb[j]) - mean; c = bfhi(xb[j]) - mean; s2 += a * a + c * c;
    }
    const float var = wave_sum(s + mk * s2) * (1.0f / D);
    const float sd = rbf(sqrtf(rbf(rbf(var) + 1e-5f)));
    if (lane == 0) __hip_atomic_store((__attribute__((address_space(1))) unsigned long long*)(ws + WS_ST1 + (size_t)srow * 8),
                                      (unsigned long long)__float_as_uint(rbf(mean)) | ((unsigned long long)__float_as_uint(1.0f / sd) << 32), __ATOMIC_RELAXED, __HIP_MEMORY_SCOPE_AGENT);
  }
  WAIT_LGKM0(); BAR();
  {
    LAS const char* L1 = lds + P0_STG;
    const u32x4 o0 = *(LAS const u32x4*)(L1 + (t >> 3) * 144 + (t & 7) * 16);
    ST16(rs, WS_UR1 + ((size_t)(c0 + (t >> 3)) * S + (1984 - s0)) * 2 + (t & 7) * 16, o0);
    if (t < 128) { const u32x4 o1 = *(LAS const u32x4*)(L1 + (64 + (t >> 3)) * 144 + (t & 7) * 16); ST16(rs, WS_UR1 + ((size_t)(c0 + 64 + (t >> 3)) * S + (1984 - s0)) * 2 + (t & 7) * 16, o1); }
  }
}

static constexpr int CH_BYTES = 13568, CH_F = 0, CH_U1 = 4224, CH_U0 = 4224 + 4608 + 32;
static constexpr int K1_YL = 4 * CH_BYTES;
static constexpr int K1_YP = 8192 + 64;
static constexpr int K1_ST = K1_YL + 4 * K1_YP;
#define K1_DELAY 80
static constexpr int K1_CONV = 160, K1_W1 = 54, K1_W2 = 30;

template <bool W1>
__device__ __forceinline__ void retile_job(LAS char* lds, const u16* __restrict__ src, int pitch, int cg, int mat, int ks0, char* ws, int w, int lane) {
  constexpr int NU = W1 ? 10 : 9;
  const auto rs = MAKE_RSRC(ws);
#pragma unroll
  for (int i = 0; i < NU; ++i) {
    const int pc = w + 8 * i, row = 8 * pc + (lane >> 3);
    dma16(src + (size_t)(ks0 * 32 + row) * pitch + (((lane & 7) ^ (row & 7)) * 8), lds + pc * 1024);
  }
  WAIT_VM(0); BAR();
  const int g = lane >> 4, q = (lane & 15) >> 2, p = lane & 3, fb = w & 3;
  const u32 l0 = (u32)(size_t)lds;
  u16x4 lo[NU], hi[NU];
#pragma unroll
  for (int i = 0; i < NU; ++i) {
    const int ks = (w >> 2) + 2 * i, row0 = 32 * ks + 8 * g + q, row1 = row0 + 4, chk = 2 * fb + (p >> 1);
    lo[i] = tr_read(l0 + row0 * 128 + ((chk ^ (row0 & 7)) * 16) + 8 * (p & 1));
    hi[i] = tr_read(l0 + row1 * 128 + ((chk ^ (row1 & 7)) * 16) + 8 * (p & 1));
  }
#pragma unroll
  for (int i = 0; i < NU; ++i) asm volatile("s_waitcnt lgkmcnt(0)" : "+v"(lo[i]), "+v"(hi[i])::"memory");
  float m = 0.f;
  if (W1) {
    float cs = 0.f;
#pragma unroll
    for (int i = 0; i < NU; ++i) {
      const u32x2 l2 = __builtin_bit_cast(u32x2, lo[i]), h2 = __builtin_bit_cast(u32x2, hi[i]);
      cs = dot2(l2.x, 0x3f803f80u, dot2(l2.y, 0x3f803f80u, dot2(h2.x, 0x3f803f80u, dot2(h2.y, 0x3f803f80u, cs))));
    }
    cs += __shfl_xor(cs, 16); cs += __shfl_xor(cs, 32);
    LAS float* red = (LAS float*)(lds + 80 * 1024);
    if (lane < 16) red[w * 16 + lane] = cs;
    WAIT_LGKM0(); BAR();
    m = (red[fb * 16 + (lane & 15)] + red[(fb + 4) * 16 + (lane & 15)]) * (1.0f / D);
  }
#pragma unroll
  for (int i = 0; i < NU; ++i) {
    const int ks = (w >> 2) + 2 * i;
    const u32x2 l2 = __builtin_bit_cast(u32x2, lo[i]), h2 = __builtin_bit_cast(u32x2, hi[i]);
    if (W1) {
      const u32x4 o = {pack2(bflo(l2.x) - m, bfhi(l2.x) - m), pack2(bflo(l2.y) - m, bfhi(l2.y) - m), pack2(bflo(h2.x) - m, bfhi(h2.x) - m), pack2(bflo(h2.y) - m, bfhi(h2.y) - m)};
      const int blk = cg * 4 + fb;
      ST16(rs, WS_W1T + ((size_t)(blk * NKS1 + ks) * 2 + mat) * 1024 + lane * 16, o);
    } else {
      const int df = cg * 4 + fb, dt = df / 5, j = df % 5;
      ST16(rs, WS_W2T + ((size_t)(dt * NKS2P + ks0 + ks) * 5 + j) * 1024 + lane * 16, ((u32x4){l2.x, l2.y, h2.x, h2.y}));
    }
  }
  if (!W1 && ks0 == 36 && cg < 8) {
#pragma unroll
    for (int i = 0; i < 2; ++i) { const int pc = w + 8 * i; if (pc < 10) ST16(rs, WS_W2T + ((size_t)(cg * NKS2P + NKS2) * 5 + pc) * 1024 + lane * 16, ((u32x4){0u, 0u, 0u, 0u})); }
  }
  if (W1 && cg == 26 && mat == 0) {
#pragma unroll
    for (int i = 0; i < 3; ++i) { const int ks = w + 8 * i; if (ks < NKS1) ST16(rs, WS_W1T + ((size_t)(108 * NKS1 + ks) * 2) * 1024 + lane * 16, ((u32x4){0x3f803f80u, 0x3f803f80u, 0x3f803f80u, 0x3f803f80u})); }
  }
}

__device__ __forceinline__ void conv_phase(LAS char* lds, const u16* __restrict__ x, const u16* __restrict__ sD, char* __restrict__ ws, int b, int t, int lane, int w, unsigned tag SARG) {
  const auto rs = MAKE_RSRC(ws);
  const int cgp = (b & 7) * 20 + (b >> 3);
  const int c = w & 3, hf = w >> 2, d = cgp * 4 + c;
  LAS char* cb = lds + c * CH_BYTES;
  if (w == 7) await_flags(ws, WS_FL0 + (size_t)(b & 7) * 512, 32, lane, tag);
  BAR();
  STAMP(1, 15);
  {
    const u32 m0b = __builtin_amdgcn_readfirstlane((u32)(size_t)(cb + CH_U1 + hf * 2048));
    const char* g1 = ws + WS_UR1 + (size_t)d * S * 2 + hf * 2048;
    DMA_SC1(m0b, lane * 16, g1, 0); DMA_SC1(m0b, lane * 16, g1, 1024);
    if (hf == 0) { if (lane < 32) *(LAS u32x4*)(cb + CH_U1 + 4096 + lane * 16) = (u32x4){0, 0, 0, 0}; if (lane < 4) *(LAS u32x4*)(cb + CH_F + lane * 16) = (u32x4){0, 0, 0, 0}; }
    else { if (lane < 32) *(LAS u32x4*)(cb + CH_U0 + 4096 + lane * 16) = (u32x4){0, 0, 0, 0}; }
  }
  const u32x2 dw = *(const u32x2*)(sD + cgp * 4);
  WAIT_VM(0); WAIT_LGKM0(); BAR();
#pragma unroll
  for (int i = 0; i < 2; ++i) {
    const int p0 = 1024 * hf + 512 * i + 8 * lane;
    const u32x4 in = *(LAS const u32x4*)(cb + CH_U1 + p0 * 2); const u32 nx = *(LAS const u32*)(cb + CH_U1 + p0 * 2 + 16);
    *(LAS u32x4*)(cb + CH_U0 + p0 * 2) = (u32x4){__builtin_amdgcn_alignbit(in.y, in.x, 16), __builtin_amdgcn_alignbit(in.z, in.y, 16), __builtin_amdgcn_alignbit(in.w, in.z, 16), __builtin_amdgcn_alignbit(nx, in.w, 16)};
  }
  WAIT_LGKM0(); BAR();
  STAMP(1, 1);
  u32x2 xr[4];
  const char* gx = (const char*)x + (size_t)(2 * t) * D * 2 + cgp * 8;
#define C_LDX(i) do { \
      if ((i) == 0) asm volatile("global_load_dwordx2 %0, %1, off" : "=v"(xr[0]) : "v"(gx) : "memory"); \
      if ((i) == 1) asm volatile("global_load_dwordx2 %0, %1, off offset:1280" : "=v"(xr[1]) : "v"(gx) : "memory"); \
      if ((i) == 2) asm volatile("global_load_dwordx2 %0, %1, off" : "=v"(xr[2]) : "v"(gx + (size_t)1024 * D * 2) : "memory"); \
      if ((i) == 3) asm volatile("global_load_dwordx2 %0, %1, off offset:1280" : "=v"(xr[3]) : "v"(gx + (size_t)1024 * D * 2) : "memory"); \
      } while (0)
#define C_XWAIT() asm volatile("s_waitcnt vmcnt(0) lgkmcnt(0)" : "+v"(xr[0]), "+v"(xr[1]), "+v"(xr[2]), "+v"(xr[3])::"memory")
  if (hf == 0) {
    const int r = lane & 15, g = lane >> 4;
    f32x4 acc[8];
    const int a = r >> 1, e = r & 1;
    const u32 ub = (u32)(size_t)(cb + (e ? CH_U1 : CH_U0) + 448 - 64 * a + 16 * g);
    u32 fbp[4];
#pragma unroll
    for (int i = 0; i < 4; ++i) fbp[i] = (u32)(size_t)(cb + CH_F + 16 * g + 4 * r + 4) + 1024 * i;
    u32x4 U[2][8]; u32x2 Fr[2][8][2];
#define C_RDU(buf, i, cc) DS_B128(U[buf][i], ub, 512 * (7 - (i)) + 64 * (cc))
#define C_RDF(buf, i, cc, hh) asm volatile("ds_read2_b32 %0, %1 offset0:%2 offset1:%3" : "=v"(Fr[buf][i][hh]) : "v"(fbp[(i) >> 1]), "n"(128 * ((i) & 1) + 16 * (cc) + 2 * (hh)), "n"(128 * ((i) & 1) + 16 * (cc) + 2 * (hh) + 1))
#define C_F(buf, i) ((u32x4){Fr[buf][i][0].x, Fr[buf][i][0].y, Fr[buf][i][1].x, Fr[buf][i][1].y})
#define C_WAIT(buf) do { \
      asm volatile("s_waitcnt lgkmcnt(0)" : "+v"(U[buf][0]), "+v"(U[buf][1]), "+v"(U[buf][2]), "+v"(U[buf][3]), "+v"(U[buf][4]), "+v"(U[buf][5]), "+v"(U[buf][6]), "+v"(U[buf][7])::"memory"); \
      asm volatile("" : "+v"(Fr[buf][0][0]), "+v"(Fr[buf][0][1]), "+v"(Fr[buf][1][0]), "+v"(Fr[buf][1][1]), "+v"(Fr[buf][2][0]), "+v"(Fr[buf][2][1]), "+v"(Fr[buf][3][0]), "+v"(Fr[buf][3][1]), \
                        "+v"(Fr[buf][4][0]), "+v"(Fr[buf][4][1]), "+v"(Fr[buf][5][0]), "+v"(Fr[buf][5][1]), "+v"(Fr[buf][6][0]), "+v"(Fr[buf][6][1]), "+v"(Fr[buf][7][0]), "+v"(Fr[buf][7][1])); } while (0)
#pragma unroll
    for (int i = 0; i < 8; ++i) { C_RDF(0, i, 0, 0); C_RDF(0, i, 0, 1); C_RDU(0, i, 0); }
    C_WAIT(0);
#pragma unroll
    for (int cc = 0; cc < 8; ++cc) {
      const int cur = cc & 1, nxt = cur ^ 1;
      int n = 0;
#pragma unroll
      for (int c1 = 0; c1 < 8; ++c1)
#pragma unroll
        for (int i = c1; i < 8; ++i) {
          if (cc == 0 && c1 == 0) MFMA_Z(acc[i], U[cur][i - c1], C_F(cur, c1)); else MFMA_IP(acc[i], U[cur][i - c1], C_F(cur, c1));
          if (cc < 7 && n < 24) { const int j = n / 3, kk = n % 3; if (kk == 0) C_RDU(nxt, j, cc + 1); else C_RDF(nxt, j, cc + 1, kk - 1); }
          if (cc < 4 && n == 28) C_LDX(cc);
          ++n;
        }
      if (cc < 7) C_WAIT(nxt);
    }
    asm volatile("s_nop 15\n\ts_nop 15" ::: "memory");
    STAMP(1, 2);
    LAS char* yl = lds + K1_YL + c * K1_YP;
#pragma unroll
    for (int j = 0; j < 8; ++j)
#pragma unroll
      for (int hh = 0; hh < 2; ++hh) *(LAS f32x2*)(yl + (256 * j + 32 * (2 * g + hh) + 2 * r) * 4) = (f32x2){acc[j][2 * hh], acc[j][2 * hh + 1]};
    C_XWAIT();
  } else {
    C_LDX(0); C_LDX(1); C_LDX(2); C_LDX(3);
    if (w == 4) {
      await_flags(ws, WS_FL0, 256, lane, tag);
      const u32 m0b = __builtin_amdgcn_readfirstlane((u32)(size_t)lds + K1_ST);
      const char* sb = ws + WS_ST1;
#pragma unroll
      for (int i = 0; i < 4; ++i) { DMA_SC1(m0b + i * 4096, lane * 16, sb + i * 4096, 0); DMA_SC1(m0b + i * 4096, lane * 16, sb + i * 4096, 1024); DMA_SC1(m0b + i * 4096, lane * 16, sb + i * 4096, 2048); DMA_SC1(m0b + i * 4096, lane * 16, sb + i * 4096, 3072); }
    }
    C_XWAIT();
  }
  BAR();
  STAMP(1, 3);
#pragma unroll
  for (int i = 0; i < 2; ++i) {
    const int s = 2 * t + 1024 * i;
    f32x2 y2[4];
    const float Dd[4] = {bflo(dw.x), bfhi(dw.x), bflo(dw.y), bfhi(dw.y)};
#pragma unroll
    for (int cc = 0; cc < 4; ++cc) {
      const f32x2 y = *(LAS const f32x2*)(lds + K1_YL + cc * K1_YP + s * 4);
      const u32 uw = *(LAS const u32*)(lds + cc * CH_BYTES + CH_U1 + (2046 - s) * 2);
      y2[cc] = (f32x2){y[0] + bfhi(uw) * Dd[cc], y[1] + bflo(uw) * Dd[cc]};
    }
    const f32x4 st = *(LAS const f32x4*)(lds + K1_ST + s * 8);
    const u32x2 x0 = xr[2 * i], x1 = xr[2 * i + 1];
    const float xa[4] = {bflo(x0.x), bfhi(x0.x), bflo(x0.y), bfhi(x0.y)}, xb[4] = {bflo(x1.x), bfhi(x1.x), bflo(x1.y), bfhi(x1.y)};
    float ha[4], hb[4];
#pragma unroll
    for (int cc = 0; cc < 4; ++cc) { ha[cc] = xa[cc] + (xa[cc] - st[0]) * st[1] * y2[cc][0]; hb[cc] = xb[cc] + (xb[cc] - st[2]) * st[3] * y2[cc][1]; }
    ST16(rs, WS_HB + ((size_t)cgp * S + s) * 8, ((u32x4){pack2(ha[0], ha[1]), pack2(ha[2], ha[3]), pack2(hb[0], hb[1]), pack2(hb[2], hb[3])}));
  }
  STAMP(1, 4);
}

static constexpr int G1_NS = 12, G1_SLOT = 8192, G1_WR = 4, G1_RING = 64 * 1024;
static constexpr int KF_CNT = 60 * 1024;
__host__ __device__ constexpr int g2_slot(int kq, int s) { return s < 3 ? kq * 15360 + s * 5120 : G1_RING + kq * 20480 + (s - 3) * 5120; }
__device__ __forceinline__ void gemm2_prefetch(LAS char* lds, char* __restrict__ ws, int dt, int lane, int first, int last, int step) {
  const u32 lds0 = (u32)(size_t)lds, voff = lane * 16;
  const char* wb = ws + WS_W2T + (size_t)dt * NKS2P * 5 * 1024;
  for (int pc = first; pc < last; pc += step) {
    int kq, o, dst;
    if (pc < 60) { kq = pc / 15; o = (pc - 15 * kq) * 1024; dst = kq * 15360 + o; }
    else { const int q = pc - 60; kq = q / 20; o = 15360 + (q - 20 * kq) * 1024; dst = G1_RING + kq * 20480 + (q - 20 * kq) * 1024; }
    const u32 m0v = __builtin_amdgcn_readfirstlane(lds0 + dst);
    const char* sb = wb + kq * 14 * 5120 + o;
    DMA_SC1(m0v, voff, sb, 0);
  }
}
__device__ __forceinline__ void gemm2_wq_issue(char* __restrict__ ws, int dt, int lane, int w, u32x4 (&wq)[7][3]) {
  const char* wsb = ws + WS_W2T + ((size_t)dt * NKS2P + (w >> 1) * 14 + 7) * 5120 + (w & 1) * 3072;
  const u32 voff = lane * 16;
  const int sh = __builtin_amdgcn_readfirstlane(w & 1);
#pragma unroll
  for (int i = 0; i < 7; ++i) {
    GLD16C(wq[i][0], voff, wsb + i * 5120, 0); GLD16C(wq[i][1], voff, wsb + i * 5120, 1024);
    asm volatile("s_cmp_lg_u32 %3, 0\n\ts_cbranch_scc1 1f\n\tglobal_load_dwordx4 %0, %1, %2 offset:2048 sc1\n1:" : "=&v"(wq[i][2]) : "v"(voff), "s"(wsb + i * 5120), "s"(sh) : "scc", "memory");
  }
}
__device__ __forceinline__ void gemm2_wq_wait(u32x4 (&wq)[7][3]) {
  asm volatile("s_waitcnt vmcnt(0)" : "+v"(wq[0][0]), "+v"(wq[0][1]), "+v"(wq[0][2]), "+v"(wq[1][0]), "+v"(wq[1][1]), "+v"(wq[1][2]), "+v"(wq[2][0]), "+v"(wq[2][1]), "+v"(wq[2][2]),
                                      "+v"(wq[3][0]), "+v"(wq[3][1]), "+v"(wq[3][2])::"memory");
  asm volatile("" : "+v"(wq[4][0]), "+v"(wq[4][1]), "+v"(wq[4][2]), "+v"(wq[5][0]), "+v"(wq[5][1]), "+v"(wq[5][2]), "+v"(wq[6][0]), "+v"(wq[6][1]), "+v"(wq[6][2]));
}

__device__ __forceinline__ void gemm1_phase(LAS char* lds, char* __restrict__ ws, int rt, int ct, int dt2, int lane, int w, unsigned tag, size_t flag_off, u32x4 (&wq)[7][3] SARG) {
  const u32 lds0 = (u32)(size_t)lds + G1_RING;
  const int s0 = rt * 128;
  const u32 voff = lane * 16;
  const auto rs = MAKE_RSRC(ws);
  if (w >= 4) {
    const int j = w - 4;
    const u32 hv = voff ^ ((j & 1) * 128);
    const char* hsb = ws + WS_HB + ((size_t)(2 * j) * S + s0) * 8;
    const u32 ldsw = lds0 + 2 * j * 1024;
#define G1_SLAB(ks) do { DMA_SC1(ldsw + ((ks) % G1_NS) * G1_SLOT, hv, hsb + (size_t)(ks) * 8 * S * 8, 0); DMA_SC1(ldsw + ((ks) % G1_NS) * G1_SLOT + 1024, hv, hsb + (size_t)(ks) * 8 * S * 8 + S * 8, 0); } while (0)
    G1_SLAB(0); G1_SLAB(1); G1_SLAB(2); G1_SLAB(3); G1_SLAB(4);
    WAIT_VM(8); BAR();
    G1_SLAB(5); G1_SLAB(6); G1_SLAB(7); G1_SLAB(8);
    WAIT_VM(8); BAR();
    gemm2_prefetch(lds, ws, dt2, lane, j, 20, 4);
    G1_SLAB(9); G1_SLAB(10); G1_SLAB(11);
    WAIT_VM(6); BAR();
    gemm2_prefetch(lds, ws, dt2, lane, 20 + j, 40, 4);
    G1_SLAB(12); G1_SLAB(13); G1_SLAB(14); G1_SLAB(15);
    WAIT_VM(6); BAR();
    gemm2_prefetch(lds, ws, dt2, lane, 40 + j, 60, 4);
    G1_SLAB(16); G1_SLAB(17); G1_SLAB(18); G1_SLAB(19);
    WAIT_VM(6); BAR();
    gemm2_wq_issue(ws, dt2, lane, w, wq);
    WAIT_VM(14); BAR();
    BAR();
    gemm2_prefetch(lds, ws, dt2, lane, 60 + j, 140, 4);
    gemm2_wq_wait(wq);
  } else {
    const int blk0 = ct * 8 + 2 * w;
    const char* wsA = ws + WS_W1T + (size_t)(blk0 > 108 ? 108 : blk0) * NKS1 * 2048;
    const char* wsB = ws + WS_W1T + (size_t)(blk0 + 1 > 108 ? 108 : blk0 + 1) * NKS1 * 2048;
    const int g = lane >> 4;
    const u32 hbase = lds0 + g * 2048 + (lane & 15) * 8;
    const u32 hoffE = hbase + (g & 1) * 128, hoffO = hbase + (1 - (g & 1)) * 128 - 128;
    const u32 hoffE2 = hoffE + 65536, hoffO2 = hoffO + 65536;
    f32x4 acc[8][4];
    const int stat = __builtin_amdgcn_readfirstlane(blk0 == 108 ? 1 : 0);
#define G1_LDW(ks, i) do { if ((i) == 0) GLD16C(wr[(ks) % G1_WR][0], voff, wsA + (ks) * 2048, 0); if ((i) == 1) GLD16C(wr[(ks) % G1_WR][1], voff, wsA + (ks) * 2048, 1024); \
      if ((i) == 2) GLD16C(wr[(ks) % G1_WR][2], voff, wsB + (ks) * 2048, 0); if ((i) == 3) GLD16C(wr[(ks) % G1_WR][3], voff, wsB + (ks) * 2048, 1024); } while (0)
#define G1_RD(sf, hh, ks) DS_B64(hf[sf][hh], ((ks) % G1_NS) >= 8 ? (((sf) & 1) ? hoffO2 : hoffE2) : (((sf) & 1) ? hoffO : hoffE), (((ks) % G1_NS) & 7) * G1_SLOT + (hh) * 1024 + (sf) * 128)
#define G1_H(sf) ((u32x4){hf[sf][0].x, hf[sf][0].y, hf[sf][1].x, hf[sf][1].y})
#define G1_MM(ks, sf, i, STAT) do { \
      if (STAT) { if ((i) == 0) { if ((ks) == 0) MFMA_Z(acc[sf][0], wr[0][0], G1_H(sf)); else MFMA_IP(acc[sf][0], wr[(ks) % G1_WR][0], G1_H(sf)); } \
                  if ((i) == 2) { if ((ks) == 0) MFMA_Z(acc[sf][2], G1_H(sf), G1_H(sf)); else MFMA_IP(acc[sf][2], G1_H(sf), G1_H(sf)); } } \
      else { if ((ks) == 0) MFMA_Z(acc[sf][i], wr[0][i], G1_H(sf)); else MFMA_IP(acc[sf][i], wr[(ks) % G1_WR][i], G1_H(sf)); } } while (0)
#define G1_BODY(STAT) { \
      u32x4 wr[G1_WR][4]; u32x2 hf[8][2]; \
      _Pragma("unroll") for (int ks = 0; ks < 3; ++ks) { G1_LDW(ks, 0); G1_LDW(ks, 1); G1_LDW(ks, 2); G1_LDW(ks, 3); } \
      BAR();                                                   \
      STAMP(1, 7); \
      _Pragma("unroll") for (int sf = 0; sf < 7; ++sf) { G1_RD(sf, 0, 0); G1_RD(sf, 1, 0); } \
      _Pragma("unroll") for (int ks = 0; ks < NKS1; ++ks) { \
        if ((ks & 3) == 0) BAR(); \
          \
        if (ks + 2 < NKS1) WAIT_VM(8); else if (ks + 1 < NKS1) WAIT_VM(4); else WAIT_VM(0); \
        asm volatile("" : "+v"(wr[ks % G1_WR][0]), "+v"(wr[ks % G1_WR][1]), "+v"(wr[ks % G1_WR][2]), "+v"(wr[ks % G1_WR][3])); \
        _Pragma("unroll") for (int sf = 0; sf < 8; ++sf) { \
            \
          if (ks + 1 < NKS1 || sf == 0) asm volatile("s_waitcnt lgkmcnt(12)" : "+v"(hf[sf][0]), "+v"(hf[sf][1])::"memory"); \
          else if (sf < 7) asm volatile("s_waitcnt lgkmcnt(%2)" : "+v"(hf[sf][0]), "+v"(hf[sf][1]) : "n"(14 - 2 * sf) : "memory"); \
          else asm volatile("s_waitcnt lgkmcnt(0)" : "+v"(hf[sf][0]), "+v"(hf[sf][1])::"memory"); \
          G1_MM(ks, sf, 0, STAT); \
          if (sf == 0) G1_RD(7, 0, ks); else if (ks + 1 < NKS1) G1_RD(sf - 1, 0, ks + 1); \
          G1_MM(ks, sf, 1, STAT); \
          if (sf == 0) G1_RD(7, 1, ks); else if (ks + 1 < NKS1) G1_RD(sf - 1, 1, ks + 1); \
          G1_MM(ks, sf, 2, STAT); \
          if ((sf & 1) && ks + 3 < NKS1) G1_LDW(ks + 3, sf >> 1); \
          G1_MM(ks, sf, 3, STAT); \
        } } }
    if (stat) G1_BODY(true) else G1_BODY(false)
    BAR();
    asm volatile("s_nop 15\n\ts_nop 15" ::: "memory");
    STAMP(1, 8);
    if (stat) {
      const int sx = lane & 15;
#pragma unroll
      for (int sp = 0; sp < 2; ++sp) {
        float r2[4];
#pragma unroll
        for (int q = 0; q < 4; ++q) {
          const int sf = 4 * sp + q;
          const float dg = (sx & 2) ? ((sx & 1) ? acc[sf][2][3] : acc[sf][2][2]) : ((sx & 1) ? acc[sf][2][1] : acc[sf][2][0]);
          const float ssq = __shfl(dg, (sx >> 2) * 16 + sx);
          const float mean = acc[sf][0][0] * (1.0f / D);
          const float var = fmaxf(ssq * (1.0f / D) - mean * mean, 0.f);
          const float r = 1.0f / rbf(sqrtf(rbf(rbf(var) + 1e-5f)));
          r2[q] = r * r;
        }
        const float mine = g == 0 ? r2[0] : (g == 1 ? r2[1] : (g == 2 ? r2[2] : r2[3]));
        const int b4 = lane & ~3;
        const f32x4 o = {__shfl(mine, b4), __shfl(mine, b4 + 1), __shfl(mine, b4 + 2), __shfl(mine, b4 + 3)};
        if ((lane & 3) == 0) ST16(rs, WS_ST2 + (size_t)(s0 + 64 * sp + lane) * 4, __builtin_bit_cast(u32x4, o));
      }
    } else {
#pragma unroll
      for (int bi = 0; bi < 2; ++bi) {
        const int blk = blk0 + bi;
        if (blk < 108) {
          const u32 obase = (u32)(WS_HF + ((size_t)(rt * 8 + (lane >> 5)) * NKS2 + (blk >> 1)) * 1024) + (blk & 1) * 512 + (lane & 31) * 16;
#pragma unroll
          for (int pr = 0; pr < 4; ++pr) {
            const f32x4 z4 = {0.f, 0.f, 0.f, 0.f};
            const f32x4 hE = __builtin_elementwise_max(acc[2 * pr][2 * bi], z4) * acc[2 * pr][2 * bi + 1], hO = __builtin_elementwise_max(acc[2 * pr + 1][2 * bi], z4) * acc[2 * pr + 1][2 * bi + 1];
            u32 e0 = pack2(hE[0], hE[1]), e1 = pack2(hE[2], hE[3]), o0 = pack2(hO[0], hO[1]), o1 = pack2(hO[2], hO[3]);
            auto r0 = __builtin_amdgcn_permlane32_swap(e0, o0, false, false); auto r0b = __builtin_amdgcn_permlane16_swap(r0[0], r0[1], false, false);
            auto r1 = __builtin_amdgcn_permlane32_swap(e1, o1, false, false); auto r1b = __builtin_amdgcn_permlane16_swap(r1[0], r1[1], false, false);
            ST16(rs, obase + pr * 2 * NKS2 * 1024, ((u32x4){r0b[0], r1b[0], r0b[1], r1b[1]}));
          }
        }
      }
    }
    WAIT_VM(0);
    unsigned old = 0;
    if (lane == 0) old = __hip_atomic_fetch_add((LAS unsigned*)(lds + KF_CNT), 1u, __ATOMIC_RELAXED, __HIP_MEMORY_SCOPE_WORKGROUP);
    if (lane == 0 && old == 3u) __hip_atomic_store((gu32*)(ws + flag_off), tag, __ATOMIC_RELAXED, __HIP_MEMORY_SCOPE_AGENT);
    STAMP(1, 9);
    gemm2_wq_issue(ws, dt2, lane, w, wq);
    gemm2_wq_wait(wq);
  }
}

__device__ __forceinline__ void gemm2_phase(LAS char* lds, char* __restrict__ ws, u16* __restrict__ out, int rt, int dt, int lane, int w, u32x4 (&wq)[7][3] SARG) {
  const int sh = w & 1, kq = w >> 1, g = lane >> 4;
  const u32 voff = lane * 16, lds0 = (u32)(size_t)lds;
  const char* hA = ws + WS_HF + ((size_t)(rt * 4 + 2 * sh) * NKS2 + kq * 14) * 1024;
  const char* hB = hA + (size_t)NKS2 * 1024;
  const int hclamp = kq == 3 ? 1 : 0;
  u32x4 hq[6][2], wf[2][5];
#define G2_LH(n) do { const int o_ = ((n) >= 12 && hclamp) ? 11 * 1024 : (n) * 1024; GLD16C(hq[(n) % 6][0], voff, hA + o_, 0); GLD16C(hq[(n) % 6][1], voff, hB + o_, 0); } while (0)
  const u32 rlo = lds0 + kq * 15360 + lane * 16, rhi = lds0 + G1_RING + kq * 20480 - 3 * 5120 + lane * 16;
#define G2_SB(n) ((((n) % 7) < 3 ? rlo : rhi))
#define G2_READ(n) do { DS_B128(wf[(n) & 1][0], G2_SB(n), ((n) % 7) * 5120); DS_B128(wf[(n) & 1][1], G2_SB(n), ((n) % 7) * 5120 + 1024); DS_B128(wf[(n) & 1][2], G2_SB(n), ((n) % 7) * 5120 + 2048); \
    DS_B128(wf[(n) & 1][3], G2_SB(n), ((n) % 7) * 5120 + 3072); DS_B128(wf[(n) & 1][4], G2_SB(n), ((n) % 7) * 5120 + 4096); } while (0)
  const int shs = __builtin_amdgcn_readfirstlane(sh);
#define G2_WST(n, i) do { if ((i) < 2) asm volatile("ds_write_b128 %0, %1 offset:%2" :: "v"(G2_SB(n) + sh * 3072), "v"(wq[(n) - 7][i]), "n"(((n) % 7) * 5120 + (i) * 1024) : "memory"); \
    else asm volatile("s_cmp_lg_u32 %3, 0\n\ts_cbranch_scc1 1f\n\tds_write_b128 %0, %1 offset:%2\n1:" :: "v"(G2_SB(n)), "v"(wq[(n) - 7][2]), "n"(((n) % 7) * 5120 + 2048), "s"(shs) : "scc", "memory"); } while (0)
  G2_LH(0); G2_LH(1);
  float r2[3]; u32x2 res[3];
#pragma unroll
  for (int j = 0; j < 3; ++j) {
    const int f = kq + 4 * j < 10 ? kq + 4 * j : kq, srow = rt * 64 + (2 * sh + f / 5) * 16 + (lane & 15);
    asm volatile("global_load_dword %0, %1, off sc1" : "=v"(r2[j]) : "v"(ws + WS_ST2 + (size_t)srow * 4) : "memory");
    asm volatile("global_load_dwordx2 %0, %1, off sc1" : "=v"(res[j]) : "v"(ws + WS_HB + ((size_t)(dt * 20 + (f % 5) * 4 + g) * S + srow) * 8) : "memory");
  }
  G2_LH(2); G2_LH(3); G2_LH(4);
  f32x4 acc[10];
  G2_READ(0);
  WAIT_VM(14);
  STAMP(1, 11);
#define G2_MM(n, m) do { if ((n) == 0) MFMA_Z(acc[m], wf[0][(m) % 5], hq[0][(m) / 5]); else MFMA_IP(acc[m], wf[(n) & 1][(m) % 5], hq[(n) % 6][(m) / 5]); } while (0)
#define G2D(k) do {} while (0)
  u32 g2d[30];
#define G2_STEP(n) do { G2D(2 * (n)); \
    if ((n) == 1) WAIT_VM(14); else if ((n) > 1) { constexpr int iss_ = 5 + (n) < 14 ? 5 + (n) : 14; WAIT_VM(2 * (iss_ - ((n) + 1))); } \
    asm volatile("s_waitcnt lgkmcnt(0)" : "+v"(wf[(n) & 1][0]), "+v"(wf[(n) & 1][1]), "+v"(wf[(n) & 1][2]), "+v"(wf[(n) & 1][3]), "+v"(wf[(n) & 1][4]), "+v"(hq[(n) % 6][0]), "+v"(hq[(n) % 6][1])::"memory"); \
    if (((n) & 1) && (n) <= 9) BAR(); \
    G2D(2 * (n) + 1); \
    if ((n) + 1 < 14) G2_READ(((n) + 1 < 14 ? (n) + 1 : 0)); \
    G2_MM(n, 0); G2_MM(n, 1); if ((n) + 5 < 14) G2_LH(((n) + 5 < 14 ? (n) + 5 : 0)); \
    G2_MM(n, 2); G2_MM(n, 3); if ((n) >= 1 && (n) <= 7) G2_WST(((n) >= 1 && (n) <= 7 ? (n) + 6 : 7), 0); \
    G2_MM(n, 4); G2_MM(n, 5); G2_MM(n, 6); if ((n) >= 1 && (n) <= 7) G2_WST(((n) >= 1 && (n) <= 7 ? (n) + 6 : 7), 1); \
    G2_MM(n, 7); G2_MM(n, 8); if ((n) >= 1 && (n) <= 7) G2_WST(((n) >= 1 && (n) <= 7 ? (n) + 6 : 7), 2); \
    G2_MM(n, 9); } while (0)
  G2_STEP(0); G2_STEP(1); G2_STEP(2); G2_STEP(3); G2_STEP(4); G2_STEP(5); G2_STEP(6);
  G2_STEP(7); G2_STEP(8); G2_STEP(9); G2_STEP(10); G2_STEP(11); G2_STEP(12); G2_STEP(13);
  asm volatile("s_nop 15\n\ts_nop 15" ::: "memory");
  asm volatile("" : "+v"(r2[0]), "+v"(r2[1]), "+v"(r2[2]), "+v"(res[0]), "+v"(res[1]), "+v"(res[2]));
  STAMP(1, 12);
  BAR();
  LAS char* xb = lds + sh * 40960 + lane * 16;
#pragma unroll
  for (int f = 0; f < 10; ++f) if (kq != (f & 3)) *(LAS f32x4*)(xb + (f * 4 + kq) * 1024) = acc[f];
  WAIT_LGKM0(); BAR();
  u32 pk[3][2];
#define G2_FIN(KQ) do { _Pragma("unroll") for (int j = 0; j < 3; ++j) { \
      constexpr int kq_ = (KQ); const int f = kq_ + 4 * j < 10 ? kq_ + 4 * j : kq_; \
      f32x4 a = acc[f]; \
      _Pragma("unroll") for (int q = 0; q < 4; ++q) if (q != kq_) a += *(LAS const f32x4*)(xb + (f * 4 + q) * 1024); \
      pk[j][0] = pack2(bflo(res[j].x) + a[0] * r2[j], bfhi(res[j].x) + a[1] * r2[j]); \
      pk[j][1] = pack2(bflo(res[j].y) + a[2] * r2[j], bfhi(res[j].y) + a[3] * r2[j]); } } while (0)
  if (kq == 0) G2_FIN(0); else if (kq == 1) G2_FIN(1); else if (kq == 2) G2_FIN(2); else G2_FIN(3);
  const auto ro = MAKE_RSRC(out);
  {
    auto r0 = __builtin_amdgcn_permlane32_swap(pk[0][0], pk[1][0], false, false); auto r0b = __builtin_amdgcn_permlane16_swap(r0[0], r0[1], false, false);
    auto r1 = __builtin_amdgcn_permlane32_swap(pk[0][1], pk[1][1], false, false); auto r1b = __builtin_amdgcn_permlane16_swap(r1[0], r1[1], false, false);
    const int f = kq + 4 * (lane >> 5);
    ST16(ro, ((size_t)(rt * 64 + (2 * sh + f / 5) * 16 + (lane & 15)) * D + dt * 80 + (f % 5) * 16 + ((lane >> 4) & 1) * 8) * 2, ((u32x4){r0b[0], r1b[0], r0b[1], r1b[1]}));
  }
  if (kq < 2) {
    auto r0 = __builtin_amdgcn_permlane32_swap(pk[2][0], pk[2][0], false, false); auto r0b = __builtin_amdgcn_permlane16_swap(r0[0], r0[1], false, false);
    auto r1 = __builtin_amdgcn_permlane32_swap(pk[2][1], pk[2][1], false, false); auto r1b = __builtin_amdgcn_permlane16_swap(r1[0], r1[1], false, false);
    const int f = kq + 8;
    if (lane < 32) ST16(ro, ((size_t)(rt * 64 + (2 * sh + f / 5) * 16 + (lane & 15)) * D + dt * 80 + (f % 5) * 16 + ((lane >> 4) & 1) * 8) * 2, ((u32x4){r0b[0], r1b[0], r0b[1], r1b[1]}));
  }
}

static constexpr int KF_LDS = 160 * 1024;
#define CODE_PF 44000
__global__ __launch_bounds__(512, 2) void kf_fused(const u16* __restrict__ x, const u16* __restrict__ k, const u16* __restrict__ v, const u16* __restrict__ shk,
                                                   const u16* __restrict__ fk, const u16* __restrict__ sD, const u16* __restrict__ wg, const u16* __restrict__ wu,
                                                   const u16* __restrict__ wd, char* __restrict__ ws, u16* __restrict__ out) {
  extern __shared__ __attribute__((aligned(16))) char shm[];
  LAS char* lds = (LAS char*)shm;
  const int t = threadIdx.x, lane = t & 63, w = __builtin_amdgcn_readfirstlane(t >> 6), b = blockIdx.x;
  STAMP_DECL; STAMP(1, 0);
  const unsigned tag = __builtin_amdgcn_readfirstlane(__hip_atomic_load((gu32*)(ws + WS_EPO), __ATOMIC_RELAXED, __HIP_MEMORY_SCOPE_AGENT)) + 1u;
  if (CODE_PF > 0 && b >= 248 && w * 8192 < CODE_PF) {
    unsigned long long pc;
    asm volatile("s_getpc_b64 s[40:41]\n\ts_add_u32 s40, s40, _Z8kf_fusedPKtS0_S0_S0_S0_S0_S0_S0_S0_PcPt@rel32@lo+4\n\ts_addc_u32 s41, s41, _Z8kf_fusedPKtS0_S0_S0_S0_S0_S0_S0_S0_PcPt@rel32@hi+12\n\ts_mov_b64 %0, s[40:41]" : "=s"(pc) :: "scc", "s40", "s41");
    int o = w * 8192 + lane * 128; o = o < CODE_PF - 4 ? o : CODE_PF - 4;
    u32 sink;
    asm volatile("global_load_dword %0, %1, off\n\ts_waitcnt vmcnt(0)" : "=&v"(sink) : "v"((const char*)pc + (o & ~3)) : "memory");
  }
  prep_phase(lds, x, k, v, shk, fk, ws, b, t, lane, w SPASS);
  {
    publish(ws, WS_FL0 + (size_t)((b & 7) * 32 + (b >> 3)) * 16, tag);
  }
  STAMP(1, 1);
  if (b < K1_CONV) conv_phase(lds, x, sD, ws, b, t, lane, w, tag SPASS);
  else {
    const int jb = b - K1_CONV;
    if (jb < K1_W1) retile_job<true>(lds, ((jb & 1) ? wu : wg) + (jb >> 1) * 64, F, jb >> 1, jb & 1, 0, ws, w, lane);
    else if (jb < K1_W1 + K1_W2) { const int j2 = jb - K1_W1; retile_job<false>(lds, wd + (j2 / 3) * 64, D, j2 / 3, 0, (j2 % 3) * 18, ws, w, lane); }
  }
  publish(ws, WS_FLG + (size_t)b * 16, tag);
  STAMP(1, 5);
  const int xg = b & 7, ix = b >> 3;
  const int p = (xg & 3) * 4 + (ix & 3), j = (xg >> 2) * 8 + (ix >> 2);
  if (t == 0) *(LAS unsigned*)(lds + KF_CNT) = 0u;
  if (w == 7) await_flags(ws, WS_FLG, 256, lane, tag);
  WAIT_LGKM0(); BAR();
  if (b == 0 && t == 0) __hip_atomic_store((gu32*)(ws + WS_EPO), tag, __ATOMIC_RELAXED, __HIP_MEMORY_SCOPE_AGENT);
  STAMP(1, 6);
  u32x4 wq[7][3];
  if (j < 14) gemm1_phase(lds, ws, p, j, j >> 1, lane, w, tag, WS_FLP + (size_t)(p * 16 + j) * 16, wq SPASS);
  else {
    gemm2_prefetch(lds, ws, j >> 1, lane, w, 140, 8);
    gemm2_wq_issue(ws, j >> 1, lane, w, wq);
    gemm2_wq_wait(wq);
  }
  if (w == 4) await_flags(ws, WS_FLP + (size_t)p * 256, 14, lane, tag);
  BAR();
  STAMP(1, 10);
  gemm2_phase(lds, ws, out, p * 2 + (j & 1), j >> 1, lane, w, wq SPASS);
  STAMP(1, 13);
  STAMP_FLUSH(1);
}

extern "C" void kernel_launch(void* const* d_in, const int* in_sizes, int n_in, void* d_out, int out_size, void* d_ws, size_t ws_size, hipStream_t stream) {
  const u16* q = (const u16*)d_in[0]; const u16* k = (const u16*)d_in[1]; const u16* v = (const u16*)d_in[2];
  const u16* shk = (const u16*)d_in[3]; const u16* fk = (const u16*)d_in[4]; const u16* sD = (const u16*)d_in[5];
  const u16* wg = (const u16*)d_in[7]; const u16* wu = (const u16*)d_in[8]; const u16* wd = (const u16*)d_in[9];
  (void)hipFuncSetAttribute((const void*)kf_fused, hipFuncAttributeMaxDynamicSharedMemorySize, KF_LDS);
  kf_fused<<<256, 512, KF_LDS, stream>>>(q, k, v, shk, fk, sD, wg, wu, wd, (char*)d_ws, (u16*)d_out);
}
```

```cpp
#include <hip/hip_runtime.h>
#include <hip/hip_bf16.h>

typedef unsigned short u16;
typedef unsigned int u32;
typedef __attribute__((ext_vector_type(2))) unsigned int u32x2;
typedef __attribute__((ext_vector_type(4))) unsigned int u32x4;
typedef __attribute__((ext_vector_type(2))) __bf16 bf16x2;
typedef __attribute__((ext_vector_type(2))) float f32x2;
typedef __attribute__((ext_vector_type(4))) float f32x4;
typedef __attribute__((ext_vector_type(4))) unsigned short u16x4;
#define LAS __attribute__((address_space(3)))

static constexpr int S = 2048, D = 640, F = 1728;
static constexpr int NKS1 = D / 32;
static constexpr int NKS2 = F / 32;

static constexpr size_t WS_UR1 = 0;
static constexpr size_t WS_HB  = WS_UR1 + (size_t)D * S * 2;
static constexpr size_t WS_ST1 = WS_HB  + (size_t)D * S * 2;
static constexpr size_t WS_ST2 = WS_ST1 + (size_t)S * 8;
static constexpr size_t WS_W1T = WS_ST2 + (size_t)16 * 1024;
static constexpr int NKS2P = 56;
static constexpr size_t WS_W2T = WS_W1T + (size_t)14 * 8 * NKS1 * 2048;
static constexpr size_t WS_HF  = WS_W2T + (size_t)8 * NKS2P * 5 * 1024;
static constexpr size_t WS_FLP = WS_HF  + (size_t)128 * NKS2 * 1024;

__device__ __forceinline__ float bflo(u32 w) { return __uint_as_float(w << 16); }
__device__ __forceinline__ float bfhi(u32 w) { return __uint_as_float(w & 0xffff0000u); }
__device__ __forceinline__ u32 pack2(float lo, float hi) { f32x2 v = {lo, hi}; bf16x2 b = __builtin_convertvector(v, bf16x2); return __builtin_bit_cast(u32, b); }
__device__ __forceinline__ float relu1(float x) { float r; asm("v_max_f32 %0, 0, %1" : "=v"(r) : "v"(x)); return r; }
__device__ __forceinline__ f32x4 relu4(f32x4 x) { return (f32x4){relu1(x[0]), relu1(x[1]), relu1(x[2]), relu1(x[3])}; }
__device__ __forceinline__ float rbf(float x) { return bflo(pack2(x, 0.f)); }
__device__ __forceinline__ float dot2(u32 a, u32 b, float c) { return __builtin_amdgcn_fdot2_f32_bf16(__builtin_bit_cast(bf16x2, a), __builtin_bit_cast(bf16x2, b), c, false); }
__device__ __forceinline__ void dma16(const void* g, LAS void* l) { __builtin_amdgcn_global_load_lds((const u32*)g, (LAS u32*)l, 16, 0, 0); }
#define WAIT_VM(n) asm volatile("s_waitcnt vmcnt(%0)" ::"n"(n) : "memory")
#define WAIT_LGKM0() asm volatile("s_waitcnt lgkmcnt(0)" ::: "memory")
#define BAR() do { asm volatile("" ::: "memory"); __builtin_amdgcn_s_barrier(); asm volatile("" ::: "memory"); } while (0)
#define MAKE_RSRC(p) __builtin_amdgcn_make_buffer_rsrc((void*)(p), 0, 0x7fffffff, 0x00020000)
#define ST16(rs, off, v) __builtin_amdgcn_raw_buffer_store_b128((v), (rs), (int)(off), 0, 16)

template <int CTRL, int RM> __device__ __forceinline__ float dpp_add(float v) { return v + __builtin_bit_cast(float, __builtin_amdgcn_update_dpp(0, __builtin_bit_cast(int, v), CTRL, RM, 0xf, true)); }
__device__ __forceinline__ float wave_sum(float v) {
  v = dpp_add<0x111, 0xf>(v); v = dpp_add<0x112, 0xf>(v); v = dpp_add<0x114, 0xf>(v); v = dpp_add<0x118, 0xf>(v);
  v = dpp_add<0x142, 0xa>(v); v = dpp_add<0x143, 0xc>(v);
  return __builtin_bit_cast(float, __builtin_amdgcn_readlane(__builtin_bit_cast(int, v), 63));
}

__device__ __forceinline__ u16x4 tr_read(u32 addr) { u16x4 r; asm volatile("ds_read_b64_tr_b16 %0, %1" : "=v"(r) : "v"(addr) : "memory"); return r; }
#define DS_B64(dst, addr, off) asm volatile("ds_read_b64 %0, %1 offset:%2" : "=v"(dst) : "v"(addr), "n"(off))
#define DS_B128(dst, addr, off) asm volatile("ds_read_b128 %0, %1 offset:%2" : "=v"(dst) : "v"(addr), "n"(off))
#define MFMA_IP(acc, a, b) asm volatile("v_mfma_f32_16x16x32_bf16 %0, %1, %2, %0" : "+v"(acc) : "v"(a), "v"(b))
#define MFMA_Z(acc, a, b) asm volatile("v_mfma_f32_16x16x32_bf16 %0, %1, %2, 0" : "=&v"(acc) : "v"(a), "v"(b))
#define GLD16(dst, voff, sbase, off) asm volatile("global_load_dwordx4 %0, %1, %2 offset:%3" : "=v"(dst) : "v"(voff), "s"(sbase), "n"(off) : "memory")

static constexpr int K0_TILES = 32 * 10, K0_STATS = 128, K0_NPF = 8;
static constexpr int K0_RAW = 17 * 1024, K0_LDS = K0_RAW + 64 * 144;
static constexpr int CODE_PF1 = 9500;
static constexpr int CODE_PF2 = 28000;
__global__ __launch_bounds__(256) void k0_prep(const u16* __restrict__ x, const u16* __restrict__ k, const u16* __restrict__ v,
                                               const u16* __restrict__ shk, char* __restrict__ ws) {
  extern __shared__ __attribute__((aligned(16))) char shm[];
  LAS char* lds = (LAS char*)shm;
  const int t = threadIdx.x, lane = t & 63, w = __builtin_amdgcn_readfirstlane(t >> 6), b = blockIdx.x;
  const auto rs = MAKE_RSRC(ws);
  if (b < K0_TILES) {
    const int st = b / 10, ct = b % 10, s0 = st * 64, c0 = ct * 64;
#pragma unroll
    for (int i = 0; i < 5; ++i) {
      const int pc = w + 4 * i;
      if (pc < 17) {
        const u16* src = pc < 9 ? k : v;
        int row = (pc < 9 ? s0 - 8 + 8 * pc : s0 + 8 * (pc - 9)) + (lane >> 3);
        row = row < 0 ? 0 : row;
        dma16(src + (size_t)row * D + c0 + (lane & 7) * 8, lds + pc * 1024);
      }
    }
    const int cp = t & 31, rg = t >> 5, ch = c0 + 2 * cp;
    const u32x4 sh = *(const u32x4*)(shk + ch * 4);
    WAIT_VM(0); __syncthreads();
    u32 kk[11], vv[8];
    LAS const char* rk = lds + (8 * rg + 5) * 128 + cp * 4;
#pragma unroll
    for (int i = 0; i < 11; ++i) kk[i] = *(LAS const u32*)(rk + i * 128);
#pragma unroll
    for (int i = 0; i < 8; ++i) vv[i] = *(LAS const u32*)(lds + 9 * 1024 + (8 * rg + i) * 128 + cp * 4);
    if (st == 0 && rg == 0) { kk[0] = kk[1] = kk[2] = 0u; }
    const float a0 = bflo(sh.x), a1 = bfhi(sh.x), a2 = bflo(sh.y), a3 = bfhi(sh.y);
    const float b0 = bflo(sh.z), b1 = bfhi(sh.z), b2 = bflo(sh.w), b3 = bfhi(sh.w);
    float ka[11], kb[11];
#pragma unroll
    for (int i = 0; i < 11; ++i) { ka[i] = bflo(kk[i]); kb[i] = bfhi(kk[i]); }
    float ua[8], ub[8];
#pragma unroll
    for (int i = 0; i < 8; ++i) {
      ua[i] = (ka[i] * a3 + ka[i + 1] * a2 + ka[i + 2] * a1 + ka[i + 3] * a0) * bflo(vv[i]);
      ub[i] = (kb[i] * b3 + kb[i + 1] * b2 + kb[i + 2] * b1 + kb[i + 3] * b0) * bfhi(vv[i]);
    }
    LAS char* L1 = lds + K0_RAW;
    const int o = (7 - rg) * 16;
    *(LAS u32x4*)(L1 + (2 * cp) * 144 + o) = (u32x4){pack2(ua[7], ua[6]), pack2(ua[5], ua[4]), pack2(ua[3], ua[2]), pack2(ua[1], ua[0])};
    *(LAS u32x4*)(L1 + (2 * cp + 1) * 144 + o) = (u32x4){pack2(ub[7], ub[6]), pack2(ub[5], ub[4]), pack2(ub[3], ub[2]), pack2(ub[1], ub[0])};
    __syncthreads();
    u32x4 o1[2];
#pragma unroll
    for (int i = 0; i < 2; ++i) o1[i] = *(LAS const u32x4*)(L1 + ((t >> 3) + 32 * i) * 144 + (t & 7) * 16);
#pragma unroll
    for (int i = 0; i < 2; ++i) ST16(rs, WS_UR1 + ((size_t)(c0 + (t >> 3) + 32 * i) * S + (1984 - s0)) * 2 + (t & 7) * 16, o1[i]);
  } else if (b >= K0_TILES + K0_STATS) {
    const int i = (b - K0_TILES - K0_STATS) >> 3;
    unsigned long long pc;
    asm volatile("s_getpc_b64 s[40:41]\n\ts_add_u32 s40, s40, _Z7k1_convPKtS0_S0_S0_S0_S0_Pc@rel32@lo+4\n\ts_addc_u32 s41, s41, _Z7k1_convPKtS0_S0_S0_S0_S0_Pc@rel32@hi+12\n\ts_mov_b64 %0, s[40:41]" : "=s"(pc) :: "scc", "s40", "s41");
    int o = (i * 4 + w) * 8192 + lane * 128; o = o < CODE_PF1 - 4 ? o : CODE_PF1 - 4;
    const char* p = (const char*)pc + (o & ~3);
    u32 sink;
    asm volatile("global_load_dword %0, %1, off\n\ts_waitcnt vmcnt(0)" : "=&v"(sink) : "v"(p) : "memory");
  } else {
    const int rbase = (b - K0_TILES) * 16 + w * 4;
    u32x4 xa[4], xb[4];
#pragma unroll
    for (int r = 0; r < 4; ++r) {
      xa[r] = *(const u32x4*)(x + (size_t)(rbase + r) * D + lane * 8);
      xb[r] = *(const u32x4*)(x + (size_t)(rbase + r) * D + 512 + (lane & 15) * 8);
    }
    const float mk = lane < 16 ? 1.f : 0.f;
    float mean[4], q[4];
#pragma unroll
    for (int r = 0; r < 4; ++r) {
      float s = 0.f, s2 = 0.f;
#pragma unroll
      for (int j = 0; j < 4; ++j) { s += bflo(xa[r][j]) + bfhi(xa[r][j]); s2 += bflo(xb[r][j]) + bfhi(xb[r][j]); }
      mean[r] = wave_sum(s + mk * s2) * (1.0f / D);
    }
#pragma unroll
    for (int r = 0; r < 4; ++r) {
      float s = 0.f, s2 = 0.f;
#pragma unroll
      for (int j = 0; j < 4; ++j) {
        float a = bflo(xa[r][j]) - mean[r], c = bfhi(xa[r][j]) - mean[r]; s += a * a + c * c;
        a = bflo(xb[r][j]) - mean[r]; c = bfhi(xb[r][j]) - mean[r]; s2 += a * a + c * c;
      }
      q[r] = wave_sum(s + mk * s2) * (1.0f / D);
    }
    if (lane < 2) {
      const float m0 = lane ? mean[2] : mean[0], m1 = lane ? mean[3] : mean[1], v0 = lane ? q[2] : q[0], v1 = lane ? q[3] : q[1];
      const float sd0 = rbf(sqrtf(rbf(rbf(v0) + 1e-5f))), sd1 = rbf(sqrtf(rbf(rbf(v1) + 1e-5f)));
      const f32x4 o = {rbf(m0), 1.0f / sd0, rbf(m1), 1.0f / sd1};
      ST16(rs, WS_ST1 + (size_t)(rbase + 2 * lane) * 8, __builtin_bit_cast(u32x4, o));
    }
  }
  if (b == K0_TILES + K0_STATS - 1) ST16(rs, WS_FLP + t * 16, ((u32x4){0u, 0u, 0u, 0u}));
}


typedef __attribute__((address_space(1))) unsigned gu32;
__device__ __forceinline__ void await_flags(char* ws, size_t off, int n, int lane) {
  const unsigned long long t0 = __builtin_amdgcn_s_memrealtime();
  for (;;) {
    bool ok = true;
#pragma unroll
    for (int i = 0; i < 4; ++i) {
      const int idx = lane + 64 * i;
      if (64 * i < n) { const unsigned v = __hip_atomic_load((gu32*)(ws + off + (size_t)(idx < n ? idx : 0) * 16), __ATOMIC_RELAXED, __HIP_MEMORY_SCOPE_AGENT); ok &= v == 1u; }
    }
    if (__all(ok)) break;
    if (__builtin_amdgcn_s_memrealtime() - t0 > 400000ull) break;
    __builtin_amdgcn_s_sleep(1);
  }
  asm volatile("" ::: "memory");
}

static constexpr int CH_BYTES = 13568, CH_F = 0, CH_U1 = 4224, CH_U0 = 4224 + 4608 + 32;
static constexpr int K1_YL = 4 * CH_BYTES;
static constexpr int K1_YP = 8192 + 64;
static constexpr unsigned long long K1_DELAY = 80;
static constexpr int K1_CONV = 160, K1_W1 = 54, K1_W2 = 30;

template <bool W1>
__device__ __forceinline__ void retile_job(LAS char* lds, const u16* __restrict__ src, int pitch, int cg, int mat, int ks0, char* ws, int w, int lane) {
  constexpr int NU = W1 ? 10 : 9;
  const auto rs = MAKE_RSRC(ws);
#pragma unroll
  for (int i = 0; i < NU; ++i) {
    const int pc = w + 8 * i, row = 8 * pc + (lane >> 3);
    dma16(src + (size_t)(ks0 * 32 + row) * pitch + (((lane & 7) ^ (row & 7)) * 8), lds + pc * 1024);
  }
  WAIT_VM(0); BAR();
  const int g = lane >> 4, q = (lane & 15) >> 2, p = lane & 3, fb = w & 3;
  const u32 l0 = (u32)(size_t)lds;
  u16x4 lo[NU], hi[NU];
#pragma unroll
  for (int i = 0; i < NU; ++i) {
    const int ks = (w >> 2) + 2 * i, row0 = 32 * ks + 8 * g + q, row1 = row0 + 4, chk = 2 * fb + (p >> 1);
    lo[i] = tr_read(l0 + row0 * 128 + ((chk ^ (row0 & 7)) * 16) + 8 * (p & 1));
    hi[i] = tr_read(l0 + row1 * 128 + ((chk ^ (row1 & 7)) * 16) + 8 * (p & 1));
  }
#pragma unroll
  for (int i = 0; i < NU; ++i) asm volatile("s_waitcnt lgkmcnt(0)" : "+v"(lo[i]), "+v"(hi[i])::"memory");
  float m = 0.f;
  if (W1) {
    float cs = 0.f;
#pragma unroll
    for (int i = 0; i < NU; ++i) {
      const u32x2 l2 = __builtin_bit_cast(u32x2, lo[i]), h2 = __builtin_bit_cast(u32x2, hi[i]);
      cs = dot2(l2.x, 0x3f803f80u, dot2(l2.y, 0x3f803f80u, dot2(h2.x, 0x3f803f80u, dot2(h2.y, 0x3f803f80u, cs))));
    }
    cs += __shfl_xor(cs, 16); cs += __shfl_xor(cs, 32);
    LAS float* red = (LAS float*)(lds + 80 * 1024);
    if (lane < 16) red[w * 16 + lane] = cs;
    WAIT_LGKM0(); BAR();
    m = (red[fb * 16 + (lane & 15)] + red[(fb + 4) * 16 + (lane & 15)]) * (1.0f / D);
  }
#pragma unroll
  for (int i = 0; i < NU; ++i) {
    const int ks = (w >> 2) + 2 * i;
    const u32x2 l2 = __builtin_bit_cast(u32x2, lo[i]), h2 = __builtin_bit_cast(u32x2, hi[i]);
    if (W1) {
      const u32x4 o = {pack2(bflo(l2.x) - m, bfhi(l2.x) - m), pack2(bflo(l2.y) - m, bfhi(l2.y) - m), pack2(bflo(h2.x) - m, bfhi(h2.x) - m), pack2(bflo(h2.y) - m, bfhi(h2.y) - m)};
      const int blk = cg * 4 + fb;
      ST16(rs, WS_W1T + ((size_t)(blk * NKS1 + ks) * 2 + mat) * 1024 + lane * 16, o);
    } else {
      const int df = cg * 4 + fb, dt = df / 5, j = df % 5;
      ST16(rs, WS_W2T + ((size_t)(dt * NKS2P + ks0 + ks) * 5 + j) * 1024 + lane * 16, ((u32x4){l2.x, l2.y, h2.x, h2.y}));
    }
  }
  if (!W1 && ks0 == 36 && cg < 8) {
#pragma unroll
    for (int i = 0; i < 2; ++i) { const int pc = w + 8 * i; if (pc < 10) ST16(rs, WS_W2T + ((size_t)(cg * NKS2P + NKS2) * 5 + pc) * 1024 + lane * 16, ((u32x4){0u, 0u, 0u, 0u})); }
  }
  if (W1 && cg == 26 && mat == 0) {
#pragma unroll
    for (int i = 0; i < 3; ++i) {
      const int ks = w + 8 * i;
      if (ks < NKS1) { ST16(rs, WS_W1T + ((size_t)(108 * NKS1 + ks) * 2) * 1024 + lane * 16, ((u32x4){0x3f803f80u, 0x3f803f80u, 0x3f803f80u, 0x3f803f80u})); ST16(rs, WS_W1T + ((size_t)(108 * NKS1 + ks) * 2 + 1) * 1024 + lane * 16, ((u32x4){0u, 0u, 0u, 0u})); }
    }
  }
}


__device__ __forceinline__ void conv_phase(LAS char* lds, const u16* __restrict__ x, const u16* __restrict__ fk, const u16* __restrict__ sD, char* __restrict__ ws, int b, int t, int lane, int w) {
  const auto rs = MAKE_RSRC(ws);
  const int cgp = (b & 7) * 20 + (b >> 3);
  const int c = w & 3, hf = w >> 2, d = cgp * 4 + c;
  LAS char* cb = lds + c * CH_BYTES;
  {
    const char* gF = (const char*)fk + (size_t)d * S * 2 + lane * 16;
    const char* g1 = ws + WS_UR1 + (size_t)d * S * 2 + lane * 16;
#pragma unroll
    for (int i = 0; i < 2; ++i) {
      const int pc = hf * 2 + i;
      dma16(g1 + pc * 1024, cb + CH_U1 + pc * 1024);
      dma16(gF + pc * 1024, cb + CH_F + 64 + pc * 1024);
    }
    if (hf == 0) { if (lane < 32) *(LAS u32x4*)(cb + CH_U1 + 4096 + lane * 16) = (u32x4){0, 0, 0, 0}; if (lane < 4) *(LAS u32x4*)(cb + CH_F + lane * 16) = (u32x4){0, 0, 0, 0}; }
    else { if (lane < 32) *(LAS u32x4*)(cb + CH_U0 + 4096 + lane * 16) = (u32x4){0, 0, 0, 0}; }
  }
  const u32x2 dw = *(const u32x2*)(sD + cgp * 4);
  WAIT_VM(0); WAIT_LGKM0(); BAR();
#pragma unroll
  for (int i = 0; i < 2; ++i) {
    const int p0 = 1024 * hf + 512 * i + 8 * lane;
    const u32x4 in = *(LAS const u32x4*)(cb + CH_U1 + p0 * 2); const u32 nx = *(LAS const u32*)(cb + CH_U1 + p0 * 2 + 16);
    *(LAS u32x4*)(cb + CH_U0 + p0 * 2) = (u32x4){__builtin_amdgcn_alignbit(in.y, in.x, 16), __builtin_amdgcn_alignbit(in.z, in.y, 16), __builtin_amdgcn_alignbit(in.w, in.z, 16), __builtin_amdgcn_alignbit(nx, in.w, 16)};
  }
  WAIT_LGKM0(); BAR();
  u32x2 xr[4]; u32x4 stv[2];
  const char* gx = (const char*)x + (size_t)(2 * t) * D * 2 + cgp * 8;
#define C_LDX(i) do { \
      if ((i) == 0) asm volatile("global_load_dwordx2 %0, %1, off" : "=v"(xr[0]) : "v"(gx) : "memory"); \
      if ((i) == 1) asm volatile("global_load_dwordx2 %0, %1, off offset:1280" : "=v"(xr[1]) : "v"(gx) : "memory"); \
      if ((i) == 2) asm volatile("global_load_dwordx2 %0, %1, off" : "=v"(xr[2]) : "v"(gx + (size_t)1024 * D * 2) : "memory"); \
      if ((i) == 3) asm volatile("global_load_dwordx2 %0, %1, off offset:1280" : "=v"(xr[3]) : "v"(gx + (size_t)1024 * D * 2) : "memory"); \
      if ((i) == 4) GLD16(stv[0], t * 16, ws + WS_ST1, 0); \
      if ((i) == 5) GLD16(stv[1], t * 16, ws + WS_ST1 + 8192, 0); } while (0)
#define C_XWAIT() asm volatile("s_waitcnt vmcnt(0) lgkmcnt(0)" : "+v"(xr[0]), "+v"(xr[1]), "+v"(xr[2]), "+v"(xr[3]), "+v"(stv[0]), "+v"(stv[1])::"memory")
  if (hf == 0) {
    const int r = lane & 15, g = lane >> 4;
    f32x4 acc[8];
    const int a = r >> 1, e = r & 1;
    const u32 ub = (u32)(size_t)(cb + (e ? CH_U1 : CH_U0) + 448 - 64 * a + 16 * g);
    u32 fbp[4];
#pragma unroll
    for (int i = 0; i < 4; ++i) fbp[i] = (u32)(size_t)(cb + CH_F + 16 * g + 4 * r + 4) + 1024 * i;
    u32x4 U[2][8]; u32x2 Fr[2][8][2];
#define C_RDU(buf, i, cc) DS_B128(U[buf][i], ub, 512 * (7 - (i)) + 64 * (cc))
#define C_RDF(buf, i, cc, hh) asm volatile("ds_read2_b32 %0, %1 offset0:%2 offset1:%3" : "=v"(Fr[buf][i][hh]) : "v"(fbp[(i) >> 1]), "n"(128 * ((i) & 1) + 16 * (cc) + 2 * (hh)), "n"(128 * ((i) & 1) + 16 * (cc) + 2 * (hh) + 1))
#define C_F(buf, i) ((u32x4){Fr[buf][i][0].x, Fr[buf][i][0].y, Fr[buf][i][1].x, Fr[buf][i][1].y})
#define C_WAIT(buf) do { \
      asm volatile("s_waitcnt lgkmcnt(0)" : "+v"(U[buf][0]), "+v"(U[buf][1]), "+v"(U[buf][2]), "+v"(U[buf][3]), "+v"(U[buf][4]), "+v"(U[buf][5]), "+v"(U[buf][6]), "+v"(U[buf][7])::"memory"); \
      asm volatile("" : "+v"(Fr[buf][0][0]), "+v"(Fr[buf][0][1]), "+v"(Fr[buf][1][0]), "+v"(Fr[buf][1][1]), "+v"(Fr[buf][2][0]), "+v"(Fr[buf][2][1]), "+v"(Fr[buf][3][0]), "+v"(Fr[buf][3][1]), \
                        "+v"(Fr[buf][4][0]), "+v"(Fr[buf][4][1]), "+v"(Fr[buf][5][0]), "+v"(Fr[buf][5][1]), "+v"(Fr[buf][6][0]), "+v"(Fr[buf][6][1]), "+v"(Fr[buf][7][0]), "+v"(Fr[buf][7][1])); } while (0)
#pragma unroll
    for (int i = 0; i < 8; ++i) { C_RDF(0, i, 0, 0); C_RDF(0, i, 0, 1); C_RDU(0, i, 0); }
    C_WAIT(0);
#pragma unroll
    for (int cc = 0; cc < 8; ++cc) {
      const int cur = cc & 1, nxt = cur ^ 1;
      int n = 0;
#pragma unroll
      for (int c1 = 0; c1 < 8; ++c1)
#pragma unroll
        for (int i = c1; i < 8; ++i) {
          if (cc == 0 && c1 == 0) MFMA_Z(acc[i], U[cur][i - c1], C_F(cur, c1)); else MFMA_IP(acc[i], U[cur][i - c1], C_F(cur, c1));
          if (cc < 7 && n < 24) { const int j = n / 3, kk = n % 3; if (kk == 0) C_RDU(nxt, j, cc + 1); else C_RDF(nxt, j, cc + 1, kk - 1); }
          if (cc < 6 && n == 28) C_LDX(cc);
          ++n;
        }
      if (cc < 7) C_WAIT(nxt);
    }
    asm volatile("s_nop 15\n\ts_nop 15" ::: "memory");
    LAS char* yl = lds + K1_YL + c * K1_YP;
#pragma unroll
    for (int j = 0; j < 8; ++j)
#pragma unroll
      for (int hh = 0; hh < 2; ++hh) *(LAS f32x2*)(yl + (256 * j + 32 * (2 * g + hh) + 2 * r) * 4) = (f32x2){acc[j][2 * hh], acc[j][2 * hh + 1]};
    C_XWAIT();
  } else {
    C_LDX(0); C_LDX(1); C_LDX(2); C_LDX(3); C_LDX(4); C_LDX(5);
    C_XWAIT();
  }
  BAR();
#pragma unroll
  for (int i = 0; i < 2; ++i) {
    const int s = 2 * t + 1024 * i;
    f32x2 y2[4];
    const float Dd[4] = {bflo(dw.x), bfhi(dw.x), bflo(dw.y), bfhi(dw.y)};
#pragma unroll
    for (int cc = 0; cc < 4; ++cc) {
      const f32x2 y = *(LAS const f32x2*)(lds + K1_YL + cc * K1_YP + s * 4);
      const u32 uw = *(LAS const u32*)(lds + cc * CH_BYTES + CH_U1 + (2046 - s) * 2);
      y2[cc] = (f32x2){y[0] + bfhi(uw) * Dd[cc], y[1] + bflo(uw) * Dd[cc]};
    }
    const f32x4 st = __builtin_bit_cast(f32x4, stv[i]);
    const u32x2 x0 = xr[2 * i], x1 = xr[2 * i + 1];
    const float xa[4] = {bflo(x0.x), bfhi(x0.x), bflo(x0.y), bfhi(x0.y)}, xb[4] = {bflo(x1.x), bfhi(x1.x), bflo(x1.y), bfhi(x1.y)};
    float ha[4], hb[4];
#pragma unroll
    for (int cc = 0; cc < 4; ++cc) { ha[cc] = xa[cc] + (xa[cc] - st[0]) * st[1] * y2[cc][0]; hb[cc] = xb[cc] + (xb[cc] - st[2]) * st[3] * y2[cc][1]; }
    ST16(rs, WS_HB + ((size_t)cgp * S + s) * 8, ((u32x4){pack2(ha[0], ha[1]), pack2(ha[2], ha[3]), pack2(hb[0], hb[1]), pack2(hb[2], hb[3])}));
  }
}

#define DMA_PL(m0v, voffv, sbase, off) asm volatile("s_mov_b32 m0, %0\n\ts_nop 0\n\tglobal_load_lds_dwordx4 %1, %2 offset:%3" :: "s"(m0v), "v"(voffv), "s"(sbase), "n"(off) : "memory")
#define GLD16C(dst, voff, sbase, off) asm volatile("global_load_dwordx4 %0, %1, %2 offset:%3 sc1" : "=v"(dst) : "v"(voff), "s"(sbase), "n"(off) : "memory")
static constexpr int G1_NS = 12, G1_SLOT = 8192, G1_WR = 4, G1_RING = 64 * 1024;
static constexpr int KF_CNT = 60 * 1024;
__device__ __forceinline__ void gemm2_prefetch(LAS char* lds, char* __restrict__ ws, int dt, int lane, int first, int last, int step) {
  const u32 lds0 = (u32)(size_t)lds, voff = lane * 16;
  const char* wb = ws + WS_W2T + (size_t)dt * NKS2P * 5 * 1024;
  for (int pc = first; pc < last; pc += step) {
    int kq, o, dst;
    if (pc < 60) { kq = pc / 15; o = (pc - 15 * kq) * 1024; dst = kq * 15360 + o; }
    else { const int q = pc - 60; kq = q / 20; o = 15360 + (q - 20 * kq) * 1024; dst = G1_RING + kq * 20480 + (q - 20 * kq) * 1024; }
    const u32 m0v = __builtin_amdgcn_readfirstlane(lds0 + dst);
    const char* sb = wb + kq * 14 * 5120 + o;
    DMA_PL(m0v, voff, sb, 0);
  }
}
__device__ __forceinline__ void gemm2_wq_issue(char* __restrict__ ws, int dt, int lane, int w, u32x4 (&wq)[7][3]) {
  const char* wsb = ws + WS_W2T + ((size_t)dt * NKS2P + (w >> 1) * 14 + 7) * 5120 + (w & 1) * 3072;
  const u32 voff = lane * 16;
  const int sh = __builtin_amdgcn_readfirstlane(w & 1);
#pragma unroll
  for (int i = 0; i < 7; ++i) {
    GLD16(wq[i][0], voff, wsb + i * 5120, 0); GLD16(wq[i][1], voff, wsb + i * 5120, 1024);
    asm volatile("s_cmp_lg_u32 %3, 0\n\ts_cbranch_scc1 1f\n\tglobal_load_dwordx4 %0, %1, %2 offset:2048\n1:" : "=&v"(wq[i][2]) : "v"(voff), "s"(wsb + i * 5120), "s"(sh) : "scc", "memory");
  }
}
__device__ __forceinline__ void gemm2_wq_wait(u32x4 (&wq)[7][3]) {
  asm volatile("s_waitcnt vmcnt(0)" : "+v"(wq[0][0]), "+v"(wq[0][1]), "+v"(wq[0][2]), "+v"(wq[1][0]), "+v"(wq[1][1]), "+v"(wq[1][2]), "+v"(wq[2][0]), "+v"(wq[2][1]), "+v"(wq[2][2]),
                                      "+v"(wq[3][0]), "+v"(wq[3][1]), "+v"(wq[3][2])::"memory");
  asm volatile("" : "+v"(wq[4][0]), "+v"(wq[4][1]), "+v"(wq[4][2]), "+v"(wq[5][0]), "+v"(wq[5][1]), "+v"(wq[5][2]), "+v"(wq[6][0]), "+v"(wq[6][1]), "+v"(wq[6][2]));
}

__device__ __forceinline__ void gemm1_phase(LAS char* lds, char* __restrict__ ws, int rt, int ct, int dt2, int lane, int w, unsigned tag, size_t flag_off, u32x4 (&wq)[7][3]) {
  const u32 lds0 = (u32)(size_t)lds + G1_RING;
  const int s0 = rt * 128;
  const u32 voff = lane * 16;
  const auto rs = MAKE_RSRC(ws);
  if (w >= 4) {
    const int j = w - 4;
    const u32 hv = voff ^ ((j & 1) * 128);
    const char* hsb = ws + WS_HB + ((size_t)(2 * j) * S + s0) * 8;
    const u32 ldsw = lds0 + 2 * j * 1024;
#define G1_SLAB(ks) do { const char* hp_ = hsb + (size_t)(ks) * 8 * S * 8; DMA_PL(ldsw + ((ks) % G1_NS) * G1_SLOT, hv, hp_, 0); DMA_PL(ldsw + ((ks) % G1_NS) * G1_SLOT + 1024, hv, hp_ + S * 8, 0); } while (0)
    G1_SLAB(0); G1_SLAB(1); G1_SLAB(2); G1_SLAB(3); G1_SLAB(4);
    WAIT_VM(8); BAR();
    G1_SLAB(5); G1_SLAB(6); G1_SLAB(7); G1_SLAB(8);
    WAIT_VM(8); BAR();
    gemm2_prefetch(lds, ws, dt2, lane, j, 20, 4);
    G1_SLAB(9); G1_SLAB(10); G1_SLAB(11);
    WAIT_VM(6); BAR();
    gemm2_prefetch(lds, ws, dt2, lane, 20 + j, 40, 4);
    G1_SLAB(12); G1_SLAB(13); G1_SLAB(14); G1_SLAB(15);
    WAIT_VM(6); BAR();
    gemm2_prefetch(lds, ws, dt2, lane, 40 + j, 60, 4);
    G1_SLAB(16); G1_SLAB(17); G1_SLAB(18); G1_SLAB(19);
    WAIT_VM(6); BAR();
    gemm2_wq_issue(ws, dt2, lane, w, wq);
    WAIT_VM(14); BAR();
    BAR();
    gemm2_prefetch(lds, ws, dt2, lane, 60 + j, 140, 4);
    gemm2_wq_wait(wq);
  } else {
    const int blk0 = ct * 8 + 2 * w;
    const char* wsA = ws + WS_W1T + (size_t)(blk0 > 108 ? 108 : blk0) * NKS1 * 2048;
    const char* wsB = ws + WS_W1T + (size_t)(blk0 + 1 > 108 ? 108 : blk0 + 1) * NKS1 * 2048;
    const int g = lane >> 4;
    const u32 hbase = lds0 + g * 2048 + (lane & 15) * 8;
    const u32 hoffE = hbase + (g & 1) * 128, hoffO = hbase + (1 - (g & 1)) * 128 - 128;
    const u32 hoffE2 = hoffE + 65536, hoffO2 = hoffO + 65536;
    f32x4 acc[8][4];
    const int stat = __builtin_amdgcn_readfirstlane(blk0 == 108 ? 1 : 0);
#define G1_LDW(ks, i) do { if ((i) == 0) GLD16(wr[(ks) % G1_WR][0], voff, wsA + (ks) * 2048, 0); if ((i) == 1) GLD16(wr[(ks) % G1_WR][1], voff, wsA + (ks) * 2048, 1024); \
      if ((i) == 2) GLD16(wr[(ks) % G1_WR][2], voff, wsB + (ks) * 2048, 0); if ((i) == 3) GLD16(wr[(ks) % G1_WR][3], voff, wsB + (ks) * 2048, 1024); } while (0)
#define G1_RD(sf, hh, ks) DS_B64(hf[sf][hh], ((ks) % G1_NS) >= 8 ? (((sf) & 1) ? hoffO2 : hoffE2) : (((sf) & 1) ? hoffO : hoffE), (((ks) % G1_NS) & 7) * G1_SLOT + (hh) * 1024 + (sf) * 128)
#define G1_H(sf) ((u32x4){hf[sf][0].x, hf[sf][0].y, hf[sf][1].x, hf[sf][1].y})
#define G1_MM(ks, sf, i, STAT) do { \
      if (STAT) { if ((i) == 0) { if ((ks) == 0) MFMA_Z(acc[sf][0], wr[0][0], G1_H(sf)); else MFMA_IP(acc[sf][0], wr[(ks) % G1_WR][0], G1_H(sf)); } \
                  if ((i) == 2) { if ((ks) == 0) MFMA_Z(acc[sf][2], G1_H(sf), G1_H(sf)); else MFMA_IP(acc[sf][2], G1_H(sf), G1_H(sf)); } } \
      else { if ((ks) == 0) MFMA_Z(acc[sf][i], wr[0][i], G1_H(sf)); else MFMA_IP(acc[sf][i], wr[(ks) % G1_WR][i], G1_H(sf)); } } while (0)
#define G1_BODY(STAT) { \
      u32x4 wr[G1_WR][4]; u32x2 hf[8][2]; \
      _Pragma("unroll") for (int ks = 0; ks < 3; ++ks) { G1_LDW(ks, 0); G1_LDW(ks, 1); G1_LDW(ks, 2); G1_LDW(ks, 3); } \
      BAR();                                                   \
      _Pragma("unroll") for (int sf = 0; sf < 7; ++sf) { G1_RD(sf, 0, 0); G1_RD(sf, 1, 0); } \
      _Pragma("unroll") for (int ks = 0; ks < NKS1; ++ks) { \
        if ((ks & 3) == 0) BAR(); \
          \
        if (ks + 2 < NKS1) WAIT_VM(8); else if (ks + 1 < NKS1) WAIT_VM(4); else WAIT_VM(0); \
        asm volatile("" : "+v"(wr[ks % G1_WR][0]), "+v"(wr[ks % G1_WR][1]), "+v"(wr[ks % G1_WR][2]), "+v"(wr[ks % G1_WR][3])); \
        _Pragma("unroll") for (int sf = 0; sf < 8; ++sf) { \
            \
          if (ks + 1 < NKS1 || sf == 0) asm volatile("s_waitcnt lgkmcnt(12)" : "+v"(hf[sf][0]), "+v"(hf[sf][1])::"memory"); \
          else if (sf < 7) asm volatile("s_waitcnt lgkmcnt(%2)" : "+v"(hf[sf][0]), "+v"(hf[sf][1]) : "n"(14 - 2 * sf) : "memory"); \
          else asm volatile("s_waitcnt lgkmcnt(0)" : "+v"(hf[sf][0]), "+v"(hf[sf][1])::"memory"); \
          G1_MM(ks, sf, 0, STAT); \
          if (sf == 0) G1_RD(7, 0, ks); else if (ks + 1 < NKS1) G1_RD(sf - 1, 0, ks + 1); \
          G1_MM(ks, sf, 1, STAT); \
          if (sf == 0) G1_RD(7, 1, ks); else if (ks + 1 < NKS1) G1_RD(sf - 1, 1, ks + 1); \
          G1_MM(ks, sf, 2, STAT); \
          if ((sf & 1) && ks + 3 < NKS1) G1_LDW(ks + 3, sf >> 1); \
          G1_MM(ks, sf, 3, STAT); \
        } } }
    if (stat) G1_BODY(true) else G1_BODY(false)
    BAR();
    asm volatile("s_nop 15\n\ts_nop 15" ::: "memory");
    if (stat) {
      const int sx = lane & 15;
#pragma unroll
      for (int sp = 0; sp < 2; ++sp) {
        float r2[4];
#pragma unroll
        for (int q = 0; q < 4; ++q) {
          const int sf = 4 * sp + q;
          const float dg = (sx & 2) ? ((sx & 1) ? acc[sf][2][3] : acc[sf][2][2]) : ((sx & 1) ? acc[sf][2][1] : acc[sf][2][0]);
          const float ssq = __shfl(dg, (sx >> 2) * 16 + sx);
          const float mean = acc[sf][0][0] * (1.0f / D);
          const float var = fmaxf(ssq * (1.0f / D) - mean * mean, 0.f);
          const float r = 1.0f / rbf(sqrtf(rbf(rbf(var) + 1e-5f)));
          r2[q] = r * r;
        }
        const float mine = g == 0 ? r2[0] : (g == 1 ? r2[1] : (g == 2 ? r2[2] : r2[3]));
        const int b4 = lane & ~3;
        const f32x4 o = {__shfl(mine, b4), __shfl(mine, b4 + 1), __shfl(mine, b4 + 2), __shfl(mine, b4 + 3)};
        if ((lane & 3) == 0) ST16(rs, WS_ST2 + (size_t)(s0 + 64 * sp + lane) * 4, __builtin_bit_cast(u32x4, o));
      }
    } else {
#pragma unroll
      for (int bi = 0; bi < 2; ++bi) {
        const int blk = blk0 + bi;
        if (blk < 108) {
          const u32 obase = (u32)(WS_HF + ((size_t)(rt * 8 + (lane >> 5)) * NKS2 + (blk >> 1)) * 1024) + (blk & 1) * 512 + (lane & 31) * 16;
#pragma unroll
          for (int pr = 0; pr < 4; ++pr) {
            const f32x4 hE = relu4(acc[2 * pr][2 * bi]) * acc[2 * pr][2 * bi + 1], hO = relu4(acc[2 * pr + 1][2 * bi]) * acc[2 * pr + 1][2 * bi + 1];
            u32 e0 = pack2(hE[0], hE[1]), e1 = pack2(hE[2], hE[3]), o0 = pack2(hO[0], hO[1]), o1 = pack2(hO[2], hO[3]);
            auto r0 = __builtin_amdgcn_permlane32_swap(e0, o0, false, false); auto r0b = __builtin_amdgcn_permlane16_swap(r0[0], r0[1], false, false);
            auto r1 = __builtin_amdgcn_permlane32_swap(e1, o1, false, false); auto r1b = __builtin_amdgcn_permlane16_swap(r1[0], r1[1], false, false);
            ST16(rs, obase + pr * 2 * NKS2 * 1024, ((u32x4){r0b[0], r1b[0], r0b[1], r1b[1]}));
          }
        }
      }
    }
    WAIT_VM(0);
    unsigned old = 0;
    if (lane == 0) old = __hip_atomic_fetch_add((LAS unsigned*)(lds + KF_CNT), 1u, __ATOMIC_RELAXED, __HIP_MEMORY_SCOPE_WORKGROUP);
    if (lane == 0 && old == 3u) __hip_atomic_store((gu32*)(ws + flag_off), tag, __ATOMIC_RELAXED, __HIP_MEMORY_SCOPE_AGENT);
    gemm2_wq_issue(ws, dt2, lane, w, wq);
    gemm2_wq_wait(wq);
  }
}

__device__ __forceinline__ void gemm2_phase(LAS char* lds, char* __restrict__ ws, u16* __restrict__ out, int rt, int dt, int lane, int w, u32x4 (&wq)[7][3]) {
  const int sh = w & 1, kq = w >> 1, g = lane >> 4;
  const u32 voff = lane * 16, lds0 = (u32)(size_t)lds;
  const char* hA = ws + WS_HF + ((size_t)(rt * 4 + 2 * sh) * NKS2 + kq * 14) * 1024;
  const char* hB = hA + (size_t)NKS2 * 1024;
  const int hclamp = kq == 3 ? 1 : 0;
  u32x4 hq[6][2], wf[2][5];
#define G2_LH(n) do { const int o_ = ((n) >= 12 && hclamp) ? 11 * 1024 : (n) * 1024; GLD16C(hq[(n) % 6][0], voff, hA + o_, 0); GLD16C(hq[(n) % 6][1], voff, hB + o_, 0); } while (0)
  const u32 rlo = lds0 + kq * 15360 + lane * 16, rhi = lds0 + G1_RING + kq * 20480 - 3 * 5120 + lane * 16;
#define G2_SB(n) ((((n) % 7) < 3 ? rlo : rhi))
#define G2_READ(n) do { DS_B128(wf[(n) & 1][0], G2_SB(n), ((n) % 7) * 5120); DS_B128(wf[(n) & 1][1], G2_SB(n), ((n) % 7) * 5120 + 1024); DS_B128(wf[(n) & 1][2], G2_SB(n), ((n) % 7) * 5120 + 2048); \
    DS_B128(wf[(n) & 1][3], G2_SB(n), ((n) % 7) * 5120 + 3072); DS_B128(wf[(n) & 1][4], G2_SB(n), ((n) % 7) * 5120 + 4096); } while (0)
  const int shs = __builtin_amdgcn_readfirstlane(sh);
#define G2_WST(n, i) do { if ((i) < 2) asm volatile("ds_write_b128 %0, %1 offset:%2" :: "v"(G2_SB(n) + sh * 3072), "v"(wq[(n) - 7][i]), "n"(((n) % 7) * 5120 + (i) * 1024) : "memory"); \
    else asm volatile("s_cmp_lg_u32 %3, 0\n\ts_cbranch_scc1 1f\n\tds_write_b128 %0, %1 offset:%2\n1:" :: "v"(G2_SB(n)), "v"(wq[(n) - 7][2]), "n"(((n) % 7) * 5120 + 2048), "s"(shs) : "scc", "memory"); } while (0)
  G2_LH(0); G2_LH(1);
  float r2[3]; u32x2 res[3];
#pragma unroll
  for (int j = 0; j < 3; ++j) {
    const int f = kq + 4 * j < 10 ? kq + 4 * j : kq, srow = rt * 64 + (2 * sh + f / 5) * 16 + (lane & 15);
    asm volatile("global_load_dword %0, %1, off sc1" : "=v"(r2[j]) : "v"(ws + WS_ST2 + (size_t)srow * 4) : "memory");
    asm volatile("global_load_dwordx2 %0, %1, off" : "=v"(res[j]) : "v"(ws + WS_HB + ((size_t)(dt * 20 + (f % 5) * 4 + g) * S + srow) * 8) : "memory");
  }
  G2_LH(2); G2_LH(3); G2_LH(4);
  f32x4 acc[10];
  G2_READ(0);
  WAIT_VM(14);
#define G2_MM(n, m) do { if ((n) == 0) MFMA_Z(acc[m], wf[0][(m) % 5], hq[0][(m) / 5]); else MFMA_IP(acc[m], wf[(n) & 1][(m) % 5], hq[(n) % 6][(m) / 5]); } while (0)
#define G2_STEP(n) do { \
    if ((n) == 1) WAIT_VM(14); else if ((n) > 1) { constexpr int iss_ = 5 + (n) < 14 ? 5 + (n) : 14; WAIT_VM(2 * (iss_ - ((n) + 1))); } \
    asm volatile("s_waitcnt lgkmcnt(0)" : "+v"(wf[(n) & 1][0]), "+v"(wf[(n) & 1][1]), "+v"(wf[(n) & 1][2]), "+v"(wf[(n) & 1][3]), "+v"(wf[(n) & 1][4]), "+v"(hq[(n) % 6][0]), "+v"(hq[(n) % 6][1])::"memory"); \
    if (((n) & 1) && (n) <= 9) BAR(); \
    if ((n) + 1 < 14) G2_READ(((n) + 1 < 14 ? (n) + 1 : 0)); \
    G2_MM(n, 0); G2_MM(n, 1); if ((n) + 5 < 14) G2_LH(((n) + 5 < 14 ? (n) + 5 : 0)); \
    G2_MM(n, 2); G2_MM(n, 3); if ((n) >= 1 && (n) <= 7) G2_WST(((n) >= 1 && (n) <= 7 ? (n) + 6 : 7), 0); \
    G2_MM(n, 4); G2_MM(n, 5); G2_MM(n, 6); if ((n) >= 1 && (n) <= 7) G2_WST(((n) >= 1 && (n) <= 7 ? (n) + 6 : 7), 1); \
    G2_MM(n, 7); G2_MM(n, 8); if ((n) >= 1 && (n) <= 7) G2_WST(((n) >= 1 && (n) <= 7 ? (n) + 6 : 7), 2); \
    G2_MM(n, 9); } while (0)
  G2_STEP(0); G2_STEP(1); G2_STEP(2); G2_STEP(3); G2_STEP(4); G2_STEP(5); G2_STEP(6);
  G2_STEP(7); G2_STEP(8); G2_STEP(9); G2_STEP(10); G2_STEP(11); G2_STEP(12); G2_STEP(13);
  asm volatile("s_nop 15\n\ts_nop 15" ::: "memory");
  asm volatile("" : "+v"(r2[0]), "+v"(r2[1]), "+v"(r2[2]), "+v"(res[0]), "+v"(res[1]), "+v"(res[2]));
  BAR();
  LAS char* xb = lds + sh * 40960 + lane * 16;
#pragma unroll
  for (int f = 0; f < 10; ++f) *(LAS f32x4*)(xb + kq * 1024 + f * 4096) = acc[f];
  WAIT_LGKM0(); BAR();
  u32 pk[3][2];
#pragma unroll
  for (int j = 0; j < 3; ++j) {
    LAS const char* sl = xb + (kq + 4 * j < 10 ? kq + 4 * j : kq) * 4096;
    const f32x4 a = (*(LAS const f32x4*)(sl) + *(LAS const f32x4*)(sl + 1024)) + (*(LAS const f32x4*)(sl + 2048) + *(LAS const f32x4*)(sl + 3072));
    pk[j][0] = pack2(bflo(res[j].x) + a[0] * r2[j], bfhi(res[j].x) + a[1] * r2[j]);
    pk[j][1] = pack2(bflo(res[j].y) + a[2] * r2[j], bfhi(res[j].y) + a[3] * r2[j]);
  }
  const auto ro = MAKE_RSRC(out);
  {
    auto r0 = __builtin_amdgcn_permlane32_swap(pk[0][0], pk[1][0], false, false); auto r0b = __builtin_amdgcn_permlane16_swap(r0[0], r0[1], false, false);
    auto r1 = __builtin_amdgcn_permlane32_swap(pk[0][1], pk[1][1], false, false); auto r1b = __builtin_amdgcn_permlane16_swap(r1[0], r1[1], false, false);
    const int f = kq + 4 * (lane >> 5);
    ST16(ro, ((size_t)(rt * 64 + (2 * sh + f / 5) * 16 + (lane & 15)) * D + dt * 80 + (f % 5) * 16 + ((lane >> 4) & 1) * 8) * 2, ((u32x4){r0b[0], r1b[0], r0b[1], r1b[1]}));
  }
  if (kq < 2) {
    auto r0 = __builtin_amdgcn_permlane32_swap(pk[2][0], pk[2][0], false, false); auto r0b = __builtin_amdgcn_permlane16_swap(r0[0], r0[1], false, false);
    auto r1 = __builtin_amdgcn_permlane32_swap(pk[2][1], pk[2][1], false, false); auto r1b = __builtin_amdgcn_permlane16_swap(r1[0], r1[1], false, false);
    const int f = kq + 8;
    if (lane < 32) ST16(ro, ((size_t)(rt * 64 + (2 * sh + f / 5) * 16 + (lane & 15)) * D + dt * 80 + (f % 5) * 16 + ((lane >> 4) & 1) * 8) * 2, ((u32x4){r0b[0], r1b[0], r0b[1], r1b[1]}));
  }
}

static constexpr int KF_LDS = 160 * 1024, K1_LDS = 96 * 1024;
__global__ __launch_bounds__(512, 2) void k1_conv(const u16* __restrict__ x, const u16* __restrict__ fk, const u16* __restrict__ sD, const u16* __restrict__ wg,
                                                  const u16* __restrict__ wu, const u16* __restrict__ wd, char* __restrict__ ws) {
  extern __shared__ __attribute__((aligned(16))) char shm[];
  LAS char* lds = (LAS char*)shm;
  const int t = threadIdx.x, lane = t & 63, w = __builtin_amdgcn_readfirstlane(t >> 6), b = blockIdx.x;
  if (b < K1_CONV) conv_phase(lds, x, fk, sD, ws, b, t, lane, w);
  else if (b < K1_CONV + K1_W1 + K1_W2) {
    { const unsigned long long t0 = __builtin_amdgcn_s_memrealtime(); while (__builtin_amdgcn_s_memrealtime() - t0 < K1_DELAY) __builtin_amdgcn_s_sleep(4); }
    const int jb = b - K1_CONV;
    if (jb < K1_W1) retile_job<true>(lds, ((jb & 1) ? wu : wg) + (jb >> 1) * 64, F, jb >> 1, jb & 1, 0, ws, w, lane);
    else { const int j2 = jb - K1_W1; retile_job<false>(lds, wd + (j2 / 3) * 64, D, j2 / 3, 0, (j2 % 3) * 18, ws, w, lane); }
  } else if (w * 8192 < CODE_PF2) {
    unsigned long long pc;
    asm volatile("s_getpc_b64 s[40:41]\n\ts_add_u32 s40, s40, _Z6k2_ffnPcPt@rel32@lo+4\n\ts_addc_u32 s41, s41, _Z6k2_ffnPcPt@rel32@hi+12\n\ts_mov_b64 %0, s[40:41]" : "=s"(pc) :: "scc", "s40", "s41");
    int o = w * 8192 + lane * 128; o = o < CODE_PF2 - 4 ? o : CODE_PF2 - 4;
    u32 sink;
    asm volatile("global_load_dword %0, %1, off\n\ts_waitcnt vmcnt(0)" : "=&v"(sink) : "v"((const char*)pc + (o & ~3)) : "memory");
  }
}

__global__ __launch_bounds__(512, 2) void k2_ffn(char* __restrict__ ws, u16* __restrict__ out) {
  extern __shared__ __attribute__((aligned(16))) char shm[];
  LAS char* lds = (LAS char*)shm;
  const int t = threadIdx.x, lane = t & 63, w = __builtin_amdgcn_readfirstlane(t >> 6), b = blockIdx.x;
  const int xg = b & 7, ix = b >> 3;
  const int p = (xg & 3) * 4 + (ix & 3), j = (xg >> 2) * 8 + (ix >> 2);
  if (t == 0) *(LAS unsigned*)(lds + KF_CNT) = 0u;
  u32x4 wq[7][3];
  if (j < 14) gemm1_phase(lds, ws, p, j, j >> 1, lane, w, 1u, WS_FLP + (size_t)(p * 16 + j) * 16, wq);
  else {
    gemm2_prefetch(lds, ws, j >> 1, lane, w, 140, 8);
    gemm2_wq_issue(ws, j >> 1, lane, w, wq);
    gemm2_wq_wait(wq);
  }
  if (w == 4) await_flags(ws, WS_FLP + (size_t)p * 256, 14, lane);
  BAR();
  gemm2_phase(lds, ws, out, p * 2 + (j & 1), j >> 1, lane, w, wq);
}

extern "C" void kernel_launch(void* const* d_in, const int* in_sizes, int n_in, void* d_out, int out_size, void* d_ws, size_t ws_size, hipStream_t stream) {
  const u16* q = (const u16*)d_in[0]; const u16* k = (const u16*)d_in[1]; const u16* v = (const u16*)d_in[2];
  const u16* shk = (const u16*)d_in[3]; const u16* fk = (const u16*)d_in[4]; const u16* sD = (const u16*)d_in[5];
  const u16* wg = (const u16*)d_in[7]; const u16* wu = (const u16*)d_in[8]; const u16* wd = (const u16*)d_in[9];
  char* ws = (char*)d_ws;
  (void)hipFuncSetAttribute((const void*)k0_prep, hipFuncAttributeMaxDynamicSharedMemorySize, K0_LDS);
  (void)hipFuncSetAttribute((const void*)k1_conv, hipFuncAttributeMaxDynamicSharedMemorySize, K1_LDS);
  (void)hipFuncSetAttribute((const void*)k2_ffn, hipFuncAttributeMaxDynamicSharedMemorySize, KF_LDS);
  k0_prep<<<K0_TILES + K0_STATS + K0_NPF, 256, K0_LDS, stream>>>(q, k, v, shk, ws);
  k1_conv<<<K1_CONV + K1_W1 + K1_W2 + 12, 512, K1_LDS, stream>>>(q, fk, sD, wg, wu, wd, ws);
  k2_ffn<<<256, 512, KF_LDS, stream>>>(ws, (u16*)d_out);
}
```

```cpp
#include <hip/hip_runtime.h>
#include <hip/hip_bf16.h>

typedef unsigned short u16;
typedef unsigned int u32;
typedef __attribute__((ext_vector_type(2))) unsigned int u32x2;
typedef __attribute__((ext_vector_type(4))) unsigned int u32x4;
typedef __attribute__((ext_vector_type(8))) short bf16x8;
typedef __attribute__((ext_vector_type(2))) __bf16 bf16x2;
typedef __attribute__((ext_vector_type(2))) float f32x2;
typedef __attribute__((ext_vector_type(4))) float f32x4;
typedef __attribute__((ext_vector_type(4))) unsigned short u16x4;
#define LAS __attribute__((address_space(3)))

#define PRIO 0
static constexpr int S = 2048, D = 640, F = 1728;
static constexpr int NKS1 = D / 32;
static constexpr int NKS2 = F / 32;

static constexpr size_t WS_UR1 = 0;
static constexpr size_t WS_HB  = WS_UR1 + (size_t)D * S * 2;
static constexpr size_t WS_ST1 = WS_HB  + (size_t)D * S * 2;
static constexpr size_t WS_ST2 = WS_ST1 + (size_t)S * 8;
static constexpr size_t WS_W1T = WS_ST2 + (size_t)16 * 1024;
static constexpr int NKS2P = 56;
static constexpr size_t WS_W2T = WS_W1T + (size_t)14 * 8 * NKS1 * 2048;
static constexpr size_t WS_HF  = WS_W2T + (size_t)8 * NKS2P * 5 * 1024;
static constexpr size_t WS_FLG = WS_HF  + (size_t)128 * NKS2 * 1024;
static constexpr size_t WS_FLP = WS_FLG + 4096;
static constexpr size_t WS_END = WS_FLP + 4096;

__device__ __forceinline__ float bflo(u32 w) { return __uint_as_float(w << 16); }
__device__ __forceinline__ float bfhi(u32 w) { return __uint_as_float(w & 0xffff0000u); }
__device__ __forceinline__ u32 pack2(float lo, float hi) { f32x2 v = {lo, hi}; bf16x2 b = __builtin_convertvector(v, bf16x2); return __builtin_bit_cast(u32, b); }
__device__ __forceinline__ float relu1(float x) { float r; asm("v_max_f32 %0, 0, %1" : "=v"(r) : "v"(x)); return r; }
__device__ __forceinline__ f32x4 relu4(f32x4 x) { return (f32x4){relu1(x[0]), relu1(x[1]), relu1(x[2]), relu1(x[3])}; }
__device__ __forceinline__ float rbf(float x) { return bflo(pack2(x, 0.f)); }
__device__ __forceinline__ float dot2(u32 a, u32 b, float c) { return __builtin_amdgcn_fdot2_f32_bf16(__builtin_bit_cast(bf16x2, a), __builtin_bit_cast(bf16x2, b), c, false); }
__device__ __forceinline__ f32x4 mfma16(u32x4 a, u32x4 b, f32x4 c) { return __builtin_amdgcn_mfma_f32_16x16x32_bf16(__builtin_bit_cast(bf16x8, a), __builtin_bit_cast(bf16x8, b), c, 0, 0, 0); }
__device__ __forceinline__ void dma16(const void* g, LAS void* l) { __builtin_amdgcn_global_load_lds((const u32*)g, (LAS u32*)l, 16, 0, 0); }
#define WAIT_VM(n) asm volatile("s_waitcnt vmcnt(%0)" ::"n"(n) : "memory")
#define WAIT_LGKM0() asm volatile("s_waitcnt lgkmcnt(0)" ::: "memory")
#define BAR() do { asm volatile("" ::: "memory"); __builtin_amdgcn_s_barrier(); asm volatile("" ::: "memory"); } while (0)
#define SB() __builtin_amdgcn_sched_barrier(0)
#define MEMFENCE() asm volatile("" ::: "memory")
#define MAKE_RSRC(p) __builtin_amdgcn_make_buffer_rsrc((void*)(p), 0, 0x7fffffff, 0x00020000)
#define ST16(rs, off, v) __builtin_amdgcn_raw_buffer_store_b128((v), (rs), (int)(off), 0, 16)

template <int CTRL, int RM> __device__ __forceinline__ float dpp_add(float v) { return v + __builtin_bit_cast(float, __builtin_amdgcn_update_dpp(0, __builtin_bit_cast(int, v), CTRL, RM, 0xf, true)); }
__device__ __forceinline__ float wave_sum(float v) {
  v = dpp_add<0x111, 0xf>(v); v = dpp_add<0x112, 0xf>(v); v = dpp_add<0x114, 0xf>(v); v = dpp_add<0x118, 0xf>(v);
  v = dpp_add<0x142, 0xa>(v); v = dpp_add<0x143, 0xc>(v);
  return __builtin_bit_cast(float, __builtin_amdgcn_readlane(__builtin_bit_cast(int, v), 63));
}

#define STAMP(kid, i) do {} while (0)
#define STAMP_DECL do {} while (0)
#define STAMP_FLUSH(kid) do {} while (0)

typedef __attribute__((ext_vector_type(4))) short s16x4;
__device__ __forceinline__ u16x4 tr_read(u32 addr) { u16x4 r; asm volatile("ds_read_b64_tr_b16 %0, %1" : "=v"(r) : "v"(addr) : "memory"); return r; }
#define DS_TR(dst, addr, off) asm volatile("ds_read_b64_tr_b16 %0, %1 offset:%2" : "=v"(dst) : "v"(addr), "n"(off))
#define DS_B64(dst, addr, off) asm volatile("ds_read_b64 %0, %1 offset:%2" : "=v"(dst) : "v"(addr), "n"(off))
#define DS_B128(dst, addr, off) asm volatile("ds_read_b128 %0, %1 offset:%2" : "=v"(dst) : "v"(addr), "n"(off))
#define MFMA_IP(acc, a, b) asm volatile("v_mfma_f32_16x16x32_bf16 %0, %1, %2, %0" : "+v"(acc) : "v"(a), "v"(b))
#define MFMA_Z(acc, a, b) asm volatile("v_mfma_f32_16x16x32_bf16 %0, %1, %2, 0" : "=&v"(acc) : "v"(a), "v"(b))
#define GLD16(dst, voff, sbase, off) asm volatile("global_load_dwordx4 %0, %1, %2 offset:%3" : "=v"(dst) : "v"(voff), "s"(sbase), "n"(off) : "memory")
#define WAIT_LGKM(n) asm volatile("s_waitcnt lgkmcnt(%0)" ::"n"(n) : "memory")

static constexpr int K0_TILES = 32 * 10, K0_STATS = 128, K0_PF = 148;
#define K0_TOUCH 1
#define K0_NPF (8 * (2 + F_PF))
static constexpr int K0_RAW = 17 * 1024, K0_LDS = K0_RAW + 64 * 144;
#define KD_PF 1
#define F_DELAY 140
#define F_PF 10
#define CODE_PF 10000
__global__ __launch_bounds__(256) void k0_prep(const u16* __restrict__ x, const u16* __restrict__ k, const u16* __restrict__ v,
                                               const u16* __restrict__ shk, const u16* __restrict__ fk, const u16* __restrict__ wg, const u16* __restrict__ wu,
                                               const u16* __restrict__ wd, const u16* __restrict__ outp, char* __restrict__ ws) {
  extern __shared__ __attribute__((aligned(16))) char shm[];
  LAS char* lds = (LAS char*)shm;
  const int t = threadIdx.x, lane = t & 63, w = __builtin_amdgcn_readfirstlane(t >> 6), b = blockIdx.x;
  STAMP_DECL; STAMP(0, 0);
  const auto rs = MAKE_RSRC(ws);
  if (b < K0_TILES) {
    const int st = b / 10, ct = b % 10, s0 = st * 64, c0 = ct * 64;
#pragma unroll
    for (int i = 0; i < 5; ++i) {
      const int pc = w + 4 * i;
      if (pc < 17) {
        const u16* src = pc < 9 ? k : v;
        int row = (pc < 9 ? s0 - 8 + 8 * pc : s0 + 8 * (pc - 9)) + (lane >> 3);
        row = row < 0 ? 0 : row;
        dma16(src + (size_t)row * D + c0 + (lane & 7) * 8, lds + pc * 1024);
      }
    }
    const int cp = t & 31, rg = t >> 5, ch = c0 + 2 * cp;
    const u32x4 sh = *(const u32x4*)(shk + ch * 4);
    WAIT_VM(0); __syncthreads();
    STAMP(0, 1);
    u32 kk[11], vv[8];
    LAS const char* rk = lds + (8 * rg + 5) * 128 + cp * 4;
#pragma unroll
    for (int i = 0; i < 11; ++i) kk[i] = *(LAS const u32*)(rk + i * 128);
#pragma unroll
    for (int i = 0; i < 8; ++i) vv[i] = *(LAS const u32*)(lds + 9 * 1024 + (8 * rg + i) * 128 + cp * 4);
    if (st == 0 && rg == 0) { kk[0] = kk[1] = kk[2] = 0u; }
    const float a0 = bflo(sh.x), a1 = bfhi(sh.x), a2 = bflo(sh.y), a3 = bfhi(sh.y);
    const float b0 = bflo(sh.z), b1 = bfhi(sh.z), b2 = bflo(sh.w), b3 = bfhi(sh.w);
    float ka[11], kb[11];
#pragma unroll
    for (int i = 0; i < 11; ++i) { ka[i] = bflo(kk[i]); kb[i] = bfhi(kk[i]); }
    float ua[8], ub[8];
#pragma unroll
    for (int i = 0; i < 8; ++i) {
      ua[i] = (ka[i] * a3 + ka[i + 1] * a2 + ka[i + 2] * a1 + ka[i + 3] * a0) * bflo(vv[i]);
      ub[i] = (kb[i] * b3 + kb[i + 1] * b2 + kb[i + 2] * b1 + kb[i + 3] * b0) * bfhi(vv[i]);
    }
    LAS char* L1 = lds + K0_RAW;
    const int o = (7 - rg) * 16;
    *(LAS u32x4*)(L1 + (2 * cp) * 144 + o) = (u32x4){pack2(ua[7], ua[6]), pack2(ua[5], ua[4]), pack2(ua[3], ua[2]), pack2(ua[1], ua[0])};
    *(LAS u32x4*)(L1 + (2 * cp + 1) * 144 + o) = (u32x4){pack2(ub[7], ub[6]), pack2(ub[5], ub[4]), pack2(ub[3], ub[2]), pack2(ub[1], ub[0])};
    __syncthreads();
    u32x4 o1[2];
#pragma unroll
    for (int i = 0; i < 2; ++i) o1[i] = *(LAS const u32x4*)(L1 + ((t >> 3) + 32 * i) * 144 + (t & 7) * 16);
#pragma unroll
    for (int i = 0; i < 2; ++i) ST16(rs, WS_UR1 + ((size_t)(c0 + (t >> 3) + 32 * i) * S + (1984 - s0)) * 2 + (t & 7) * 16, o1[i]);
    STAMP(0, 2);
  } else if (b >= K0_TILES + K0_STATS) {
    const int xq = b & 7, i = (b - K0_TILES - K0_STATS) >> 3;
    const char* p;
    if (i < 2) {
      unsigned long long pc;
      asm volatile("s_getpc_b64 s[40:41]\n\ts_add_u32 s40, s40, _Z7k1_convPKtS0_S0_S0_S0_S0_Pc@rel32@lo+4\n\ts_addc_u32 s41, s41, _Z7k1_convPKtS0_S0_S0_S0_S0_Pc@rel32@hi+12\n\ts_mov_b64 %0, s[40:41]" : "=s"(pc) :: "scc", "s40", "s41");
      int o = (i * 4 + w) * 8192 + lane * 128; o = o < CODE_PF - 4 ? o : CODE_PF - 4;
      p = (const char*)pc + (o & ~3);
    } else {
      { const unsigned long long t0 = __builtin_amdgcn_s_memrealtime(); while (__builtin_amdgcn_s_memrealtime() - t0 < F_DELAY) __builtin_amdgcn_s_sleep(4); }
      const int q = (i - 2) * 4 + w;
      p = (const char*)fk + (size_t)xq * 80 * S * 2 + (size_t)q * 8192 + lane * 128;
    }
    u32 sink;
    asm volatile("global_load_dword %0, %1, off\n\ts_waitcnt vmcnt(0)" : "=&v"(sink) : "v"(p) : "memory");
  } else {
    const int rbase = (b - K0_TILES) * 16 + w * 4;
    u32x4 xa[4], xb[4];
#pragma unroll
    for (int r = 0; r < 4; ++r) {
      xa[r] = *(const u32x4*)(x + (size_t)(rbase + r) * D + lane * 8);
      xb[r] = *(const u32x4*)(x + (size_t)(rbase + r) * D + 512 + (lane & 15) * 8);
    }
    const float mk = lane < 16 ? 1.f : 0.f;
    float mean[4], q[4];
#pragma unroll
    for (int r = 0; r < 4; ++r) {
      float s = 0.f, s2 = 0.f;
#pragma unroll
      for (int j = 0; j < 4; ++j) { s += bflo(xa[r][j]) + bfhi(xa[r][j]); s2 += bflo(xb[r][j]) + bfhi(xb[r][j]); }
      mean[r] = wave_sum(s + mk * s2) * (1.0f / D);
    }
#pragma unroll
    for (int r = 0; r < 4; ++r) {
      float s = 0.f, s2 = 0.f;
#pragma unroll
      for (int j = 0; j < 4; ++j) {
        float a = bflo(xa[r][j]) - mean[r], c = bfhi(xa[r][j]) - mean[r]; s += a * a + c * c;
        a = bflo(xb[r][j]) - mean[r]; c = bfhi(xb[r][j]) - mean[r]; s2 += a * a + c * c;
      }
      q[r] = wave_sum(s + mk * s2) * (1.0f / D);
    }
    STAMP(0, 1);
    if (lane < 2) {
      const float m0 = lane ? mean[2] : mean[0], m1 = lane ? mean[3] : mean[1], v0 = lane ? q[2] : q[0], v1 = lane ? q[3] : q[1];
      const float sd0 = rbf(sqrtf(rbf(rbf(v0) + 1e-5f))), sd1 = rbf(sqrtf(rbf(rbf(v1) + 1e-5f)));
      const f32x4 o = {rbf(m0), 1.0f / sd0, rbf(m1), 1.0f / sd1};
      ST16(rs, WS_ST1 + (size_t)(rbase + 2 * lane) * 8, __builtin_bit_cast(u32x4, o));
    }
    STAMP(0, 2);
  }
  if (b == K0_TILES + K0_STATS - 1) { ST16(rs, WS_FLG + t * 16, ((u32x4){0u, 0u, 0u, 0u})); ST16(rs, WS_FLP + t * 16, ((u32x4){0u, 0u, 0u, 0u})); }
  STAMP_FLUSH(0);
}

#define POLL_SLEEP 1
typedef __attribute__((address_space(1))) unsigned gu32;
__device__ __forceinline__ void publish(char* ws, size_t off) {
  WAIT_VM(0); BAR();
  if (threadIdx.x == 0) __hip_atomic_store((gu32*)(ws + off), 1u, __ATOMIC_RELAXED, __HIP_MEMORY_SCOPE_AGENT);
}
__device__ __forceinline__ void await_flags(char* ws, size_t off, int n, int lane) {
  const unsigned long long t0 = __builtin_amdgcn_s_memrealtime();
  for (;;) {
    bool ok = true;
#pragma unroll
    for (int i = 0; i < 4; ++i) {
      const int idx = lane + 64 * i;
      if (64 * i < n) { const unsigned v = __hip_atomic_load((gu32*)(ws + off + (size_t)(idx < n ? idx : 0) * 16), __ATOMIC_RELAXED, __HIP_MEMORY_SCOPE_AGENT); ok &= v == 1u; }
    }
    if (__all(ok)) break;
    if (__builtin_amdgcn_s_memrealtime() - t0 > 400000ull) break;
    __builtin_amdgcn_s_sleep(POLL_SLEEP);
  }
  asm volatile("" ::: "memory");
}

static constexpr int CH_BYTES = 13568, CH_F = 0, CH_U1 = 4224, CH_U0 = 4224 + 4608 + 32;
static constexpr int K1_YL = 4 * CH_BYTES;
static constexpr int K1_YP = 8192 + 64;
#define K1_DELAY 80
static constexpr int K1_CONV = 160, K1_W1 = 54, K1_W2 = 30;

template <bool W1>
__device__ __forceinline__ void retile_job(LAS char* lds, const u16* __restrict__ src, int pitch, int cg, int mat, int ks0, char* ws, int w, int lane) {
  constexpr int NU = W1 ? 10 : 9;
  const auto rs = MAKE_RSRC(ws);
#pragma unroll
  for (int i = 0; i < NU; ++i) {
    const int pc = w + 8 * i, row = 8 * pc + (lane >> 3);
    dma16(src + (size_t)(ks0 * 32 + row) * pitch + (((lane & 7) ^ (row & 7)) * 8), lds + pc * 1024);
  }
  WAIT_VM(0); BAR();
  const int g = lane >> 4, q = (lane & 15) >> 2, p = lane & 3, fb = w & 3;
  const u32 l0 = (u32)(size_t)lds;
  u16x4 lo[NU], hi[NU];
#pragma unroll
  for (int i = 0; i < NU; ++i) {
    const int ks = (w >> 2) + 2 * i, row0 = 32 * ks + 8 * g + q, row1 = row0 + 4, chk = 2 * fb + (p >> 1);
    lo[i] = tr_read(l0 + row0 * 128 + ((chk ^ (row0 & 7)) * 16) + 8 * (p & 1));
    hi[i] = tr_read(l0 + row1 * 128 + ((chk ^ (row1 & 7)) * 16) + 8 * (p & 1));
  }
#pragma unroll
  for (int i = 0; i < NU; ++i) asm volatile("s_waitcnt lgkmcnt(0)" : "+v"(lo[i]), "+v"(hi[i])::"memory");
  float m = 0.f;
  if (W1) {
    float cs = 0.f;
#pragma unroll
    for (int i = 0; i < NU; ++i) {
      const u32x2 l2 = __builtin_bit_cast(u32x2, lo[i]), h2 = __builtin_bit_cast(u32x2, hi[i]);
      cs = dot2(l2.x, 0x3f803f80u, dot2(l2.y, 0x3f803f80u, dot2(h2.x, 0x3f803f80u, dot2(h2.y, 0x3f803f80u, cs))));
    }
    cs += __shfl_xor(cs, 16); cs += __shfl_xor(cs, 32);
    LAS float* red = (LAS float*)(lds + 80 * 1024);
    if (lane < 16) red[w * 16 + lane] = cs;
    WAIT_LGKM0(); BAR();
    m = (red[fb * 16 + (lane & 15)] + red[(fb + 4) * 16 + (lane & 15)]) * (1.0f / D);
  }
#pragma unroll
  for (int i = 0; i < NU; ++i) {
    const int ks = (w >> 2) + 2 * i;
    const u32x2 l2 = __builtin_bit_cast(u32x2, lo[i]), h2 = __builtin_bit_cast(u32x2, hi[i]);
    if (W1) {
      const u32x4 o = {pack2(bflo(l2.x) - m, bfhi(l2.x) - m), pack2(bflo(l2.y) - m, bfhi(l2.y) - m), pack2(bflo(h2.x) - m, bfhi(h2.x) - m), pack2(bflo(h2.y) - m, bfhi(h2.y) - m)};
      const int blk = cg * 4 + fb;
      ST16(rs, WS_W1T + ((size_t)(blk * NKS1 + ks) * 2 + mat) * 1024 + lane * 16, o);
    } else {
      const int df = cg * 4 + fb, dt = df / 5, j = df % 5;
      ST16(rs, WS_W2T + ((size_t)(dt * NKS2P + ks0 + ks) * 5 + j) * 1024 + lane * 16, ((u32x4){l2.x, l2.y, h2.x, h2.y}));
    }
  }
  if (!W1 && ks0 == 36 && cg < 8) {
#pragma unroll
    for (int i = 0; i < 2; ++i) { const int pc = w + 8 * i; if (pc < 10) ST16(rs, WS_W2T + ((size_t)(cg * NKS2P + NKS2) * 5 + pc) * 1024 + lane * 16, ((u32x4){0u, 0u, 0u, 0u})); }
  }
  if (W1 && cg == 26 && mat == 0) {
#pragma unroll
    for (int i = 0; i < 3; ++i) {
      const int ks = w + 8 * i;
      if (ks < NKS1) { ST16(rs, WS_W1T + ((size_t)(108 * NKS1 + ks) * 2) * 1024 + lane * 16, ((u32x4){0x3f803f80u, 0x3f803f80u, 0x3f803f80u, 0x3f803f80u})); ST16(rs, WS_W1T + ((size_t)(108 * NKS1 + ks) * 2 + 1) * 1024 + lane * 16, ((u32x4){0u, 0u, 0u, 0u})); }
    }
  }
}

#define SARG
#define SPASS

__device__ __forceinline__ void conv_phase(LAS char* lds, const u16* __restrict__ x, const u16* __restrict__ fk, const u16* __restrict__ sD, char* __restrict__ ws, int b, int t, int lane, int w SARG) {
  const auto rs = MAKE_RSRC(ws);
  const int cgp = (b & 7) * 20 + (b >> 3);
  const int c = w & 3, hf = w >> 2, d = cgp * 4 + c;
  LAS char* cb = lds + c * CH_BYTES;
  {
    const char* gF = (const char*)fk + (size_t)d * S * 2 + lane * 16;
    const char* g1 = ws + WS_UR1 + (size_t)d * S * 2 + lane * 16;
#pragma unroll
    for (int i = 0; i < 2; ++i) {
      const int pc = hf * 2 + i;
      dma16(g1 + pc * 1024, cb + CH_U1 + pc * 1024);
      dma16(gF + pc * 1024, cb + CH_F + 64 + pc * 1024);
    }
    if (hf == 0) { if (lane < 32) *(LAS u32x4*)(cb + CH_U1 + 4096 + lane * 16) = (u32x4){0, 0, 0, 0}; if (lane < 4) *(LAS u32x4*)(cb + CH_F + lane * 16) = (u32x4){0, 0, 0, 0}; }
    else { if (lane < 32) *(LAS u32x4*)(cb + CH_U0 + 4096 + lane * 16) = (u32x4){0, 0, 0, 0}; }
  }
  const u32x2 dw = *(const u32x2*)(sD + cgp * 4);
  WAIT_VM(0); WAIT_LGKM0(); BAR();
#pragma unroll
  for (int i = 0; i < 2; ++i) {
    const int p0 = 1024 * hf + 512 * i + 8 * lane;
    const u32x4 in = *(LAS const u32x4*)(cb + CH_U1 + p0 * 2); const u32 nx = *(LAS const u32*)(cb + CH_U1 + p0 * 2 + 16);
    *(LAS u32x4*)(cb + CH_U0 + p0 * 2) = (u32x4){__builtin_amdgcn_alignbit(in.y, in.x, 16), __builtin_amdgcn_alignbit(in.z, in.y, 16), __builtin_amdgcn_alignbit(in.w, in.z, 16), __builtin_amdgcn_alignbit(nx, in.w, 16)};
  }
  WAIT_LGKM0(); BAR();
  STAMP(1, 1);
  u32x2 xr[4]; u32x4 stv[2];
  const char* gx = (const char*)x + (size_t)(2 * t) * D * 2 + cgp * 8;
#define C_LDX(i) do { \
      if ((i) == 0) asm volatile("global_load_dwordx2 %0, %1, off" : "=v"(xr[0]) : "v"(gx) : "memory"); \
      if ((i) == 1) asm volatile("global_load_dwordx2 %0, %1, off offset:1280" : "=v"(xr[1]) : "v"(gx) : "memory"); \
      if ((i) == 2) asm volatile("global_load_dwordx2 %0, %1, off" : "=v"(xr[2]) : "v"(gx + (size_t)1024 * D * 2) : "memory"); \
      if ((i) == 3) asm volatile("global_load_dwordx2 %0, %1, off offset:1280" : "=v"(xr[3]) : "v"(gx + (size_t)1024 * D * 2) : "memory"); \
      if ((i) == 4) GLD16(stv[0], t * 16, ws + WS_ST1, 0); \
      if ((i) == 5) GLD16(stv[1], t * 16, ws + WS_ST1 + 8192, 0); } while (0)
#define C_XWAIT() asm volatile("s_waitcnt vmcnt(0) lgkmcnt(0)" : "+v"(xr[0]), "+v"(xr[1]), "+v"(xr[2]), "+v"(xr[3]), "+v"(stv[0]), "+v"(stv[1])::"memory")
  if (hf == 0) {
    const int r = lane & 15, g = lane >> 4;
    f32x4 acc[8];
    const int a = r >> 1, e = r & 1;
    const u32 ub = (u32)(size_t)(cb + (e ? CH_U1 : CH_U0) + 448 - 64 * a + 16 * g);
    u32 fbp[4];
#pragma unroll
    for (int i = 0; i < 4; ++i) fbp[i] = (u32)(size_t)(cb + CH_F + 16 * g + 4 * r + 4) + 1024 * i;
    u32x4 U[2][8]; u32x2 Fr[2][8][2];
#define C_RDU(buf, i, cc) DS_B128(U[buf][i], ub, 512 * (7 - (i)) + 64 * (cc))
#define C_RDF(buf, i, cc, hh) asm volatile("ds_read2_b32 %0, %1 offset0:%2 offset1:%3" : "=v"(Fr[buf][i][hh]) : "v"(fbp[(i) >> 1]), "n"(128 * ((i) & 1) + 16 * (cc) + 2 * (hh)), "n"(128 * ((i) & 1) + 16 * (cc) + 2 * (hh) + 1))
#define C_F(buf, i) ((u32x4){Fr[buf][i][0].x, Fr[buf][i][0].y, Fr[buf][i][1].x, Fr[buf][i][1].y})
#define C_WAIT(buf) do { \
      asm volatile("s_waitcnt lgkmcnt(0)" : "+v"(U[buf][0]), "+v"(U[buf][1]), "+v"(U[buf][2]), "+v"(U[buf][3]), "+v"(U[buf][4]), "+v"(U[buf][5]), "+v"(U[buf][6]), "+v"(U[buf][7])::"memory"); \
      asm volatile("" : "+v"(Fr[buf][0][0]), "+v"(Fr[buf][0][1]), "+v"(Fr[buf][1][0]), "+v"(Fr[buf][1][1]), "+v"(Fr[buf][2][0]), "+v"(Fr[buf][2][1]), "+v"(Fr[buf][3][0]), "+v"(Fr[buf][3][1]), \
                        "+v"(Fr[buf][4][0]), "+v"(Fr[buf][4][1]), "+v"(Fr[buf][5][0]), "+v"(Fr[buf][5][1]), "+v"(Fr[buf][6][0]), "+v"(Fr[buf][6][1]), "+v"(Fr[buf][7][0]), "+v"(Fr[buf][7][1])); } while (0)
    if (PRIO) __builtin_amdgcn_s_setprio(3);
#pragma unroll
    for (int i = 0; i < 8; ++i) { C_RDF(0, i, 0, 0); C_RDF(0, i, 0, 1); C_RDU(0, i, 0); }
    C_WAIT(0);
#pragma unroll
    for (int cc = 0; cc < 8; ++cc) {
      const int cur = cc & 1, nxt = cur ^ 1;
      int n = 0;
#pragma unroll
      for (int c1 = 0; c1 < 8; ++c1)
#pragma unroll
        for (int i = c1; i < 8; ++i) {
          if (cc == 0 && c1 == 0) MFMA_Z(acc[i], U[cur][i - c1], C_F(cur, c1)); else MFMA_IP(acc[i], U[cur][i - c1], C_F(cur, c1));
          if (cc < 7 && n < 24) { const int j = n / 3, kk = n % 3; if (kk == 0) C_RDU(nxt, j, cc + 1); else C_RDF(nxt, j, cc + 1, kk - 1); }
          if (cc < 6 && n == 28) C_LDX(cc);
          ++n;
        }
      if (cc < 7) C_WAIT(nxt);
    }
    if (PRIO) __builtin_amdgcn_s_setprio(0);
    asm volatile("s_nop 15\n\ts_nop 15" ::: "memory");
    STAMP(1, 2);
    LAS char* yl = lds + K1_YL + c * K1_YP;
#pragma unroll
    for (int j = 0; j < 8; ++j)
#pragma unroll
      for (int hh = 0; hh < 2; ++hh) *(LAS f32x2*)(yl + (256 * j + 32 * (2 * g + hh) + 2 * r) * 4) = (f32x2){acc[j][2 * hh], acc[j][2 * hh + 1]};
    C_XWAIT();
  } else {
    C_LDX(0); C_LDX(1); C_LDX(2); C_LDX(3); C_LDX(4); C_LDX(5);
    C_XWAIT();
  }
  BAR();
  STAMP(1, 3);
#pragma unroll
  for (int i = 0; i < 2; ++i) {
    const int s = 2 * t + 1024 * i;
    f32x2 y2[4];
    const float Dd[4] = {bflo(dw.x), bfhi(dw.x), bflo(dw.y), bfhi(dw.y)};
#pragma unroll
    for (int cc = 0; cc < 4; ++cc) {
      const f32x2 y = *(LAS const f32x2*)(lds + K1_YL + cc * K1_YP + s * 4);
      const u32 uw = *(LAS const u32*)(lds + cc * CH_BYTES + CH_U1 + (2046 - s) * 2);
      y2[cc] = (f32x2){y[0] + bfhi(uw) * Dd[cc], y[1] + bflo(uw) * Dd[cc]};
    }
    const f32x4 st = __builtin_bit_cast(f32x4, stv[i]);
    const u32x2 x0 = xr[2 * i], x1 = xr[2 * i + 1];
    const float xa[4] = {bflo(x0.x), bfhi(x0.x), bflo(x0.y), bfhi(x0.y)}, xb[4] = {bflo(x1.x), bfhi(x1.x), bflo(x1.y), bfhi(x1.y)};
    float ha[4], hb[4];
#pragma unroll
    for (int cc = 0; cc < 4; ++cc) { ha[cc] = xa[cc] + (xa[cc] - st[0]) * st[1] * y2[cc][0]; hb[cc] = xb[cc] + (xb[cc] - st[2]) * st[3] * y2[cc][1]; }
    ST16(rs, WS_HB + ((size_t)cgp * S + s) * 8, ((u32x4){pack2(ha[0], ha[1]), pack2(ha[2], ha[3]), pack2(hb[0], hb[1]), pack2(hb[2], hb[3])}));
  }
  STAMP(1, 4);
}

#define DMA_SC1(m0v, voffv, sbase, off) asm volatile("s_mov_b32 m0, %0\n\ts_nop 0\n\tglobal_load_lds_dwordx4 %1, %2 offset:%3 sc1" :: "s"(m0v), "v"(voffv), "s"(sbase), "n"(off) : "memory")
#define DMA_PL(m0v, voffv, sbase, off) asm volatile("s_mov_b32 m0, %0\n\ts_nop 0\n\tglobal_load_lds_dwordx4 %1, %2 offset:%3" :: "s"(m0v), "v"(voffv), "s"(sbase), "n"(off) : "memory")
#define GLD16C(dst, voff, sbase, off) asm volatile("global_load_dwordx4 %0, %1, %2 offset:%3 sc1" : "=v"(dst) : "v"(voff), "s"(sbase), "n"(off) : "memory")
static constexpr int G1_NS = 12, G1_SLOT = 8192, G1_WR = 4, G1_RING = 64 * 1024;
#define G1_ROT 0
#define WQ_EARLY 1
#define G1_EARLY 1
#define G1_PACE 0
static constexpr int KF_CNT = 60 * 1024;
__host__ __device__ constexpr int g2_slot(int kq, int s) { return s < 3 ? kq * 15360 + s * 5120 : G1_RING + kq * 20480 + (s - 3) * 5120; }
template <int PACE>
__device__ __forceinline__ void gemm2_prefetch(LAS char* lds, char* __restrict__ ws, int dt, int lane, int first, int last, int step) {
  const u32 lds0 = (u32)(size_t)lds, voff = lane * 16;
  const char* wb = ws + WS_W2T + (size_t)dt * NKS2P * 5 * 1024;
  for (int pc = first; pc < last; pc += step) {
    int kq, o, dst;
    if (pc < 60) { kq = pc / 15; o = (pc - 15 * kq) * 1024; dst = kq * 15360 + o; }
    else { const int q = pc - 60; kq = q / 20; o = 15360 + (q - 20 * kq) * 1024; dst = G1_RING + kq * 20480 + (q - 20 * kq) * 1024; }
    const u32 m0v = __builtin_amdgcn_readfirstlane(lds0 + dst);
    const char* sb = wb + kq * 14 * 5120 + o;
    DMA_PL(m0v, voff, sb, 0);
    if (PACE) __builtin_amdgcn_s_sleep(PACE);
  }
}
template <int PACE>
__device__ __forceinline__ void gemm2_wq_issue(char* __restrict__ ws, int dt, int lane, int w, u32x4 (&wq)[7][3]) {
  const char* wsb = ws + WS_W2T + ((size_t)dt * NKS2P + (w >> 1) * 14 + 7) * 5120 + (w & 1) * 3072;
  const u32 voff = lane * 16;
  const int sh = __builtin_amdgcn_readfirstlane(w & 1);
#pragma unroll
  for (int i = 0; i < 7; ++i) {
    GLD16(wq[i][0], voff, wsb + i * 5120, 0); GLD16(wq[i][1], voff, wsb + i * 5120, 1024);
    asm volatile("s_cmp_lg_u32 %3, 0\n\ts_cbranch_scc1 1f\n\tglobal_load_dwordx4 %0, %1, %2 offset:2048\n1:" : "=&v"(wq[i][2]) : "v"(voff), "s"(wsb + i * 5120), "s"(sh) : "scc", "memory");
    if (PACE) __builtin_amdgcn_s_sleep(PACE);
  }
}
__device__ __forceinline__ void gemm2_wq_wait(u32x4 (&wq)[7][3]) {
  asm volatile("s_waitcnt vmcnt(0)" : "+v"(wq[0][0]), "+v"(wq[0][1]), "+v"(wq[0][2]), "+v"(wq[1][0]), "+v"(wq[1][1]), "+v"(wq[1][2]), "+v"(wq[2][0]), "+v"(wq[2][1]), "+v"(wq[2][2]),
                                      "+v"(wq[3][0]), "+v"(wq[3][1]), "+v"(wq[3][2])::"memory");
  asm volatile("" : "+v"(wq[4][0]), "+v"(wq[4][1]), "+v"(wq[4][2]), "+v"(wq[5][0]), "+v"(wq[5][1]), "+v"(wq[5][2]), "+v"(wq[6][0]), "+v"(wq[6][1]), "+v"(wq[6][2]));
}

__device__ __forceinline__ void gemm1_phase(LAS char* lds, char* __restrict__ ws, int rt, int ct, int dt2, int lane, int w, unsigned tag, size_t flag_off, u32x4 (&wq)[7][3] SARG) {
  const u32 lds0 = (u32)(size_t)lds + G1_RING;
  const int s0 = rt * 128;
  const u32 voff = lane * 16;
  const auto rs = MAKE_RSRC(ws);
  const int krot = G1_ROT ? 4 * ((rt + ct) % 5) : 0;
#define G1_KS(ks) ((ks) + krot >= NKS1 ? (ks) + krot - NKS1 : (ks) + krot)
  if (w >= 4) {
    const int j = w - 4;
    const u32 hv = voff ^ ((j & 1) * 128);
    const char* hsb = ws + WS_HB + ((size_t)(2 * j) * S + s0) * 8;
    const u32 ldsw = lds0 + 2 * j * 1024;
#define G1_SLAB(ks) do { const char* hp_ = hsb + (size_t)G1_KS(ks) * 8 * S * 8; DMA_PL(ldsw + ((ks) % G1_NS) * G1_SLOT, hv, hp_, 0); DMA_PL(ldsw + ((ks) % G1_NS) * G1_SLOT + 1024, hv, hp_ + S * 8, 0); if ((ks) > 8 && G1_PACE) __builtin_amdgcn_s_sleep(G1_PACE); } while (0)
    G1_SLAB(0); G1_SLAB(1); G1_SLAB(2); G1_SLAB(3); G1_SLAB(4);
    WAIT_VM(8); BAR();
    G1_SLAB(5); G1_SLAB(6); G1_SLAB(7); G1_SLAB(8);
    WAIT_VM(8); BAR();
    if (G1_EARLY) gemm2_prefetch<G1_PACE>(lds, ws, dt2, lane, j, 20, 4);
    G1_SLAB(9); G1_SLAB(10); G1_SLAB(11);
    WAIT_VM(6); BAR();
    if (G1_EARLY) gemm2_prefetch<G1_PACE>(lds, ws, dt2, lane, 20 + j, 40, 4);
    G1_SLAB(12); G1_SLAB(13); G1_SLAB(14); G1_SLAB(15);
    WAIT_VM(6); BAR();
    if (G1_EARLY) gemm2_prefetch<G1_PACE>(lds, ws, dt2, lane, 40 + j, 60, 4);
    G1_SLAB(16); G1_SLAB(17); G1_SLAB(18); G1_SLAB(19);
    WAIT_VM(6); BAR();
    if (WQ_EARLY) { gemm2_wq_issue<G1_PACE>(ws, dt2, lane, w, wq); WAIT_VM(14); } else WAIT_VM(0);
    BAR();
    BAR();
    gemm2_prefetch<0>(lds, ws, dt2, lane, (G1_EARLY ? 60 : 0) + j, 140, 4);
    if (!WQ_EARLY) gemm2_wq_issue<0>(ws, dt2, lane, w, wq);
    gemm2_wq_wait(wq);
  } else {
    const int blk0 = ct * 8 + 2 * w;
    const char* wsA = ws + WS_W1T + (size_t)(blk0 > 108 ? 108 : blk0) * NKS1 * 2048;
    const char* wsB = ws + WS_W1T + (size_t)(blk0 + 1 > 108 ? 108 : blk0 + 1) * NKS1 * 2048;
    const int g = lane >> 4;
    const u32 hbase = lds0 + g * 2048 + (lane & 15) * 8;
    const u32 hoffE = hbase + (g & 1) * 128, hoffO = hbase + (1 - (g & 1)) * 128 - 128;
    const u32 hoffE2 = hoffE + 65536, hoffO2 = hoffO + 65536;
    u32 g1d[20], g1b[20];
    f32x4 acc[8][4];
    const int stat = __builtin_amdgcn_readfirstlane(blk0 == 108 ? 1 : 0);
#define G1_LDW(ks, i) do { if ((i) == 0) GLD16(wr[(ks) % G1_WR][0], voff, wsA + G1_KS(ks) * 2048, 0); if ((i) == 1) GLD16(wr[(ks) % G1_WR][1], voff, wsA + G1_KS(ks) * 2048, 1024); \
      if ((i) == 2) GLD16(wr[(ks) % G1_WR][2], voff, wsB + G1_KS(ks) * 2048, 0); if ((i) == 3) GLD16(wr[(ks) % G1_WR][3], voff, wsB + G1_KS(ks) * 2048, 1024); } while (0)
#define G1_RD(sf, hh, ks) DS_B64(hf[sf][hh], ((ks) % G1_NS) >= 8 ? (((sf) & 1) ? hoffO2 : hoffE2) : (((sf) & 1) ? hoffO : hoffE), (((ks) % G1_NS) & 7) * G1_SLOT + (hh) * 1024 + (sf) * 128)
#define G1_H(sf) ((u32x4){hf[sf][0].x, hf[sf][0].y, hf[sf][1].x, hf[sf][1].y})
#define G1_MM(ks, sf, i, STAT) do { \
      if (STAT) { if ((i) == 0) { if ((ks) == 0) MFMA_Z(acc[sf][0], wr[0][0], G1_H(sf)); else MFMA_IP(acc[sf][0], wr[(ks) % G1_WR][0], G1_H(sf)); } \
                  if ((i) == 2) { if ((ks) == 0) MFMA_Z(acc[sf][2], G1_H(sf), G1_H(sf)); else MFMA_IP(acc[sf][2], G1_H(sf), G1_H(sf)); } } \
      else { if ((ks) == 0) MFMA_Z(acc[sf][i], wr[0][i], G1_H(sf)); else MFMA_IP(acc[sf][i], wr[(ks) % G1_WR][i], G1_H(sf)); } } while (0)
#define G1D(x) do {} while (0)
#define G1ACC(ks) do {} while (0)
#define G1_BODY(STAT) { \
      u32x4 wr[G1_WR][4]; u32x2 hf[8][2]; \
      _Pragma("unroll") for (int ks = 0; ks < 3; ++ks) { G1_LDW(ks, 0); G1_LDW(ks, 1); G1_LDW(ks, 2); G1_LDW(ks, 3); } \
      BAR();                                                   \
      STAMP(1, 7); \
      _Pragma("unroll") for (int sf = 0; sf < 7; ++sf) { G1_RD(sf, 0, 0); G1_RD(sf, 1, 0); } \
      _Pragma("unroll") for (int ks = 0; ks < NKS1; ++ks) { \
        G1D(ta); \
        if ((ks & 3) == 0) BAR(); \
        G1D(tb); \
          \
        if (ks + 2 < NKS1) WAIT_VM(8); else if (ks + 1 < NKS1) WAIT_VM(4); else WAIT_VM(0); \
        G1D(tc); G1ACC(ks); \
        asm volatile("" : "+v"(wr[ks % G1_WR][0]), "+v"(wr[ks % G1_WR][1]), "+v"(wr[ks % G1_WR][2]), "+v"(wr[ks % G1_WR][3])); \
        _Pragma("unroll") for (int sf = 0; sf < 8; ++sf) { \
            \
          if (ks + 1 < NKS1 || sf == 0) asm volatile("s_waitcnt lgkmcnt(12)" : "+v"(hf[sf][0]), "+v"(hf[sf][1])::"memory"); \
          else if (sf < 7) asm volatile("s_waitcnt lgkmcnt(%2)" : "+v"(hf[sf][0]), "+v"(hf[sf][1]) : "n"(14 - 2 * sf) : "memory"); \
          else asm volatile("s_waitcnt lgkmcnt(0)" : "+v"(hf[sf][0]), "+v"(hf[sf][1])::"memory"); \
          G1_MM(ks, sf, 0, STAT); \
          if (sf == 0) G1_RD(7, 0, ks); else if (ks + 1 < NKS1) G1_RD(sf - 1, 0, ks + 1); \
          G1_MM(ks, sf, 1, STAT); \
          if (sf == 0) G1_RD(7, 1, ks); else if (ks + 1 < NKS1) G1_RD(sf - 1, 1, ks + 1); \
          G1_MM(ks, sf, 2, STAT); \
          if ((sf & 1) && ks + 3 < NKS1) G1_LDW(ks + 3, sf >> 1); \
          G1_MM(ks, sf, 3, STAT); \
        } } }
    if (PRIO) __builtin_amdgcn_s_setprio(3);
    if (stat) G1_BODY(true) else G1_BODY(false)
    if (PRIO) __builtin_amdgcn_s_setprio(0);
    BAR();
    asm volatile("s_nop 15\n\ts_nop 15" ::: "memory");
    STAMP(1, 8);
    if (stat) {
      const int sx = lane & 15;
#pragma unroll
      for (int sp = 0; sp < 2; ++sp) {
        float r2[4];
#pragma unroll
        for (int q = 0; q < 4; ++q) {
          const int sf = 4 * sp + q;
          const float dg = (sx & 2) ? ((sx & 1) ? acc[sf][2][3] : acc[sf][2][2]) : ((sx & 1) ? acc[sf][2][1] : acc[sf][2][0]);
          const float ssq = __shfl(dg, (sx >> 2) * 16 + sx);
          const float mean = acc[sf][0][0] * (1.0f / D);
          const float var = fmaxf(ssq * (1.0f / D) - mean * mean, 0.f);
          const float r = 1.0f / rbf(sqrtf(rbf(rbf(var) + 1e-5f)));
          r2[q] = r * r;
        }
        const float mine = g == 0 ? r2[0] : (g == 1 ? r2[1] : (g == 2 ? r2[2] : r2[3]));
        const int b4 = lane & ~3;
        const f32x4 o = {__shfl(mine, b4), __shfl(mine, b4 + 1), __shfl(mine, b4 + 2), __shfl(mine, b4 + 3)};
        if ((lane & 3) == 0) ST16(rs, WS_ST2 + (size_t)(s0 + 64 * sp + lane) * 4, __builtin_bit_cast(u32x4, o));
      }
    } else {
#pragma unroll
      for (int bi = 0; bi < 2; ++bi) {
        const int blk = blk0 + bi;
        if (blk < 108) {
          const u32 obase = (u32)(WS_HF + ((size_t)(rt * 8 + (lane >> 5)) * NKS2 + (blk >> 1)) * 1024) + (blk & 1) * 512 + (lane & 31) * 16;
#pragma unroll
          for (int pr = 0; pr < 4; ++pr) {
            const f32x4 hE = relu4(acc[2 * pr][2 * bi]) * acc[2 * pr][2 * bi + 1], hO = relu4(acc[2 * pr + 1][2 * bi]) * acc[2 * pr + 1][2 * bi + 1];
            u32 e0 = pack2(hE[0], hE[1]), e1 = pack2(hE[2], hE[3]), o0 = pack2(hO[0], hO[1]), o1 = pack2(hO[2], hO[3]);
            auto r0 = __builtin_amdgcn_permlane32_swap(e0, o0, false, false); auto r0b = __builtin_amdgcn_permlane16_swap(r0[0], r0[1], false, false);
            auto r1 = __builtin_amdgcn_permlane32_swap(e1, o1, false, false); auto r1b = __builtin_amdgcn_permlane16_swap(r1[0], r1[1], false, false);
            ST16(rs, obase + pr * 2 * NKS2 * 1024, ((u32x4){r0b[0], r1b[0], r0b[1], r1b[1]}));
          }
        }
      }
    }
    WAIT_VM(0);
    unsigned old = 0;
    if (lane == 0) old = __hip_atomic_fetch_add((LAS unsigned*)(lds + KF_CNT), 1u, __ATOMIC_RELAXED, __HIP_MEMORY_SCOPE_WORKGROUP);
    if (lane == 0 && old == 3u) __hip_atomic_store((gu32*)(ws + flag_off), tag, __ATOMIC_RELAXED, __HIP_MEMORY_SCOPE_AGENT);
    STAMP(1, 9);
    gemm2_wq_issue<0>(ws, dt2, lane, w, wq);
    gemm2_wq_wait(wq);
  }
}

__device__ __forceinline__ void gemm2_phase(LAS char* lds, char* __restrict__ ws, u16* __restrict__ out, int rt, int dt, int lane, int w, u32x4 (&wq)[7][3] SARG) {
  const int sh = w & 1, kq = w >> 1, g = lane >> 4;
  const u32 voff = lane * 16, lds0 = (u32)(size_t)lds;
  const char* hA = ws + WS_HF + ((size_t)(rt * 4 + 2 * sh) * NKS2 + kq * 14) * 1024;
  const char* hB = hA + (size_t)NKS2 * 1024;
  const int hclamp = kq == 3 ? 1 : 0;
  u32x4 hq[6][2], wf[2][5];
#define G2_LH(n) do { const int o_ = ((n) >= 12 && hclamp) ? 11 * 1024 : (n) * 1024; GLD16C(hq[(n) % 6][0], voff, hA + o_, 0); GLD16C(hq[(n) % 6][1], voff, hB + o_, 0); } while (0)
  const u32 rlo = lds0 + kq * 15360 + lane * 16, rhi = lds0 + G1_RING + kq * 20480 - 3 * 5120 + lane * 16;
#define G2_SB(n) ((((n) % 7) < 3 ? rlo : rhi))
#define G2_READ(n) do { DS_B128(wf[(n) & 1][0], G2_SB(n), ((n) % 7) * 5120); DS_B128(wf[(n) & 1][1], G2_SB(n), ((n) % 7) * 5120 + 1024); DS_B128(wf[(n) & 1][2], G2_SB(n), ((n) % 7) * 5120 + 2048); \
    DS_B128(wf[(n) & 1][3], G2_SB(n), ((n) % 7) * 5120 + 3072); DS_B128(wf[(n) & 1][4], G2_SB(n), ((n) % 7) * 5120 + 4096); } while (0)
  const int shs = __builtin_amdgcn_readfirstlane(sh);
#define G2_WST(n, i) do { if ((i) < 2) asm volatile("ds_write_b128 %0, %1 offset:%2" :: "v"(G2_SB(n) + sh * 3072), "v"(wq[(n) - 7][i]), "n"(((n) % 7) * 5120 + (i) * 1024) : "memory"); \
    else asm volatile("s_cmp_lg_u32 %3, 0\n\ts_cbranch_scc1 1f\n\tds_write_b128 %0, %1 offset:%2\n1:" :: "v"(G2_SB(n)), "v"(wq[(n) - 7][2]), "n"(((n) % 7) * 5120 + 2048), "s"(shs) : "scc", "memory"); } while (0)
  G2_LH(0); G2_LH(1);
  float r2[3]; u32x2 res[3];
#pragma unroll
  for (int j = 0; j < 3; ++j) {
    const int f = kq + 4 * j < 10 ? kq + 4 * j : kq, srow = rt * 64 + (2 * sh + f / 5) * 16 + (lane & 15);
    asm volatile("global_load_dword %0, %1, off sc1" : "=v"(r2[j]) : "v"(ws + WS_ST2 + (size_t)srow * 4) : "memory");
    asm volatile("global_load_dwordx2 %0, %1, off" : "=v"(res[j]) : "v"(ws + WS_HB + ((size_t)(dt * 20 + (f % 5) * 4 + g) * S + srow) * 8) : "memory");
  }
  G2_LH(2); G2_LH(3); G2_LH(4);
  f32x4 acc[10];
  G2_READ(0);
  WAIT_VM(14);
  STAMP(1, 11);
#define G2_MM(n, m) do { if ((n) == 0) MFMA_Z(acc[m], wf[0][(m) % 5], hq[0][(m) / 5]); else MFMA_IP(acc[m], wf[(n) & 1][(m) % 5], hq[(n) % 6][(m) / 5]); } while (0)
#define G2D(k) do {} while (0)
  u32 g2d[30];
#define G2_STEP(n) do { G2D(2 * (n)); \
    if ((n) == 1) WAIT_VM(14); else if ((n) > 1) { constexpr int iss_ = 5 + (n) < 14 ? 5 + (n) : 14; WAIT_VM(2 * (iss_ - ((n) + 1))); } \
    asm volatile("s_waitcnt lgkmcnt(0)" : "+v"(wf[(n) & 1][0]), "+v"(wf[(n) & 1][1]), "+v"(wf[(n) & 1][2]), "+v"(wf[(n) & 1][3]), "+v"(wf[(n) & 1][4]), "+v"(hq[(n) % 6][0]), "+v"(hq[(n) % 6][1])::"memory"); \
    if (((n) & 1) && (n) <= 9) BAR(); \
    G2D(2 * (n) + 1); \
    if ((n) + 1 < 14) G2_READ(((n) + 1 < 14 ? (n) + 1 : 0)); \
    G2_MM(n, 0); G2_MM(n, 1); if ((n) + 5 < 14) G2_LH(((n) + 5 < 14 ? (n) + 5 : 0)); \
    G2_MM(n, 2); G2_MM(n, 3); if ((n) >= 1 && (n) <= 7) G2_WST(((n) >= 1 && (n) <= 7 ? (n) + 6 : 7), 0); \
    G2_MM(n, 4); G2_MM(n, 5); G2_MM(n, 6); if ((n) >= 1 && (n) <= 7) G2_WST(((n) >= 1 && (n) <= 7 ? (n) + 6 : 7), 1); \
    G2_MM(n, 7); G2_MM(n, 8); if ((n) >= 1 && (n) <= 7) G2_WST(((n) >= 1 && (n) <= 7 ? (n) + 6 : 7), 2); \
    G2_MM(n, 9); } while (0)
  G2_STEP(0); G2_STEP(1); G2_STEP(2); G2_STEP(3); G2_STEP(4); G2_STEP(5); G2_STEP(6);
  G2_STEP(7); G2_STEP(8); G2_STEP(9); G2_STEP(10); G2_STEP(11); G2_STEP(12); G2_STEP(13);
  asm volatile("s_nop 15\n\ts_nop 15" ::: "memory");
  asm volatile("" : "+v"(r2[0]), "+v"(r2[1]), "+v"(r2[2]), "+v"(res[0]), "+v"(res[1]), "+v"(res[2]));
  STAMP(1, 12);
  BAR();
  LAS char* xb = lds + sh * 40960 + lane * 16;
#pragma unroll
  for (int f = 0; f < 10; ++f) *(LAS f32x4*)(xb + kq * 1024 + f * 4096) = acc[f];
  WAIT_LGKM0(); BAR();
  u32 pk[3][2];
#pragma unroll
  for (int j = 0; j < 3; ++j) {
    LAS const char* sl = xb + (kq + 4 * j < 10 ? kq + 4 * j : kq) * 4096;
    const f32x4 a = (*(LAS const f32x4*)(sl) + *(LAS const f32x4*)(sl + 1024)) + (*(LAS const f32x4*)(sl + 2048) + *(LAS const f32x4*)(sl + 3072));
    pk[j][0] = pack2(bflo(res[j].x) + a[0] * r2[j], bfhi(res[j].x) + a[1] * r2[j]);
    pk[j][1] = pack2(bflo(res[j].y) + a[2] * r2[j], bfhi(res[j].y) + a[3] * r2[j]);
  }
  const auto ro = MAKE_RSRC(out);
  {
    auto r0 = __builtin_amdgcn_permlane32_swap(pk[0][0], pk[1][0], false, false); auto r0b = __builtin_amdgcn_permlane16_swap(r0[0], r0[1], false, false);
    auto r1 = __builtin_amdgcn_permlane32_swap(pk[0][1], pk[1][1], false, false); auto r1b = __builtin_amdgcn_permlane16_swap(r1[0], r1[1], false, false);
    const int f = kq + 4 * (lane >> 5);
    ST16(ro, ((size_t)(rt * 64 + (2 * sh + f / 5) * 16 + (lane & 15)) * D + dt * 80 + (f % 5) * 16 + ((lane >> 4) & 1) * 8) * 2, ((u32x4){r0b[0], r1b[0], r0b[1], r1b[1]}));
  }
  if (kq < 2) {
    auto r0 = __builtin_amdgcn_permlane32_swap(pk[2][0], pk[2][0], false, false); auto r0b = __builtin_amdgcn_permlane16_swap(r0[0], r0[1], false, false);
    auto r1 = __builtin_amdgcn_permlane32_swap(pk[2][1], pk[2][1], false, false); auto r1b = __builtin_amdgcn_permlane16_swap(r1[0], r1[1], false, false);
    const int f = kq + 8;
    if (lane < 32) ST16(ro, ((size_t)(rt * 64 + (2 * sh + f / 5) * 16 + (lane & 15)) * D + dt * 80 + (f % 5) * 16 + ((lane >> 4) & 1) * 8) * 2, ((u32x4){r0b[0], r1b[0], r0b[1], r1b[1]}));
  }
}

static constexpr int KF_LDS = 160 * 1024, K1_LDS = 96 * 1024;
#define CODE_PF2 29000
__global__ __launch_bounds__(512, 2) void k1_conv(const u16* __restrict__ x, const u16* __restrict__ fk, const u16* __restrict__ sD, const u16* __restrict__ wg,
                                                  const u16* __restrict__ wu, const u16* __restrict__ wd, char* __restrict__ ws) {
  extern __shared__ __attribute__((aligned(16))) char shm[];
  LAS char* lds = (LAS char*)shm;
  const int t = threadIdx.x, lane = t & 63, w = __builtin_amdgcn_readfirstlane(t >> 6), b = blockIdx.x;
  STAMP_DECL; STAMP(1, 0);
  if (b < K1_CONV) conv_phase(lds, x, fk, sD, ws, b, t, lane, w SPASS);
  else if (b < K1_CONV + K1_W1 + K1_W2) {
    { const unsigned long long t0 = __builtin_amdgcn_s_memrealtime(); while (__builtin_amdgcn_s_memrealtime() - t0 < K1_DELAY) __builtin_amdgcn_s_sleep(4); }
    const int jb = b - K1_CONV;
    if (jb < K1_W1) retile_job<true>(lds, ((jb & 1) ? wu : wg) + (jb >> 1) * 64, F, jb >> 1, jb & 1, 0, ws, w, lane);
    else { const int j2 = jb - K1_W1; retile_job<false>(lds, wd + (j2 / 3) * 64, D, j2 / 3, 0, (j2 % 3) * 18, ws, w, lane); }
  } else if (CODE_PF2 > 0 && w * 8192 < CODE_PF2) {
    unsigned long long pc;
    asm volatile("s_getpc_b64 s[40:41]\n\ts_add_u32 s40, s40, _Z6k2_ffnPcPt@rel32@lo+4\n\ts_addc_u32 s41, s41, _Z6k2_ffnPcPt@rel32@hi+12\n\ts_mov_b64 %0, s[40:41]" : "=s"(pc) :: "scc", "s40", "s41");
    int o = w * 8192 + lane * 128; o = o < CODE_PF2 - 4 ? o : CODE_PF2 - 4;
    u32 sink;
    asm volatile("global_load_dword %0, %1, off\n\ts_waitcnt vmcnt(0)" : "=&v"(sink) : "v"((const char*)pc + (o & ~3)) : "memory");
  }
  STAMP(1, 5);
  STAMP_FLUSH(1);
}

__global__ __launch_bounds__(512, 2) void k2_ffn(char* __restrict__ ws, u16* __restrict__ out) {
  extern __shared__ __attribute__((aligned(16))) char shm[];
  LAS char* lds = (LAS char*)shm;
  const int t = threadIdx.x, lane = t & 63, w = __builtin_amdgcn_readfirstlane(t >> 6), b = blockIdx.x;
  STAMP_DECL; STAMP(2, 0);
  const int xg = b & 7, ix = b >> 3;
  const int p = (xg & 3) * 4 + (ix & 3), j = (xg >> 2) * 8 + (ix >> 2);
  if (t == 0) *(LAS unsigned*)(lds + KF_CNT) = 0u;
  u32x4 wq[7][3];
  if (j < 14) gemm1_phase(lds, ws, p, j, j >> 1, lane, w, 1u, WS_FLP + (size_t)(p * 16 + j) * 16, wq SPASS);
  else {
    gemm2_prefetch<0>(lds, ws, j >> 1, lane, w, 140, 8);
    gemm2_wq_issue<0>(ws, j >> 1, lane, w, wq);
    gemm2_wq_wait(wq);
  }
  if (w == 4) await_flags(ws, WS_FLP + (size_t)p * 256, 14, lane);
  BAR();
  STAMP(2, 10);
  gemm2_phase(lds, ws, out, p * 2 + (j & 1), j >> 1, lane, w, wq SPASS);
  STAMP(2, 13);
  STAMP_FLUSH(2);
}

extern "C" void kernel_launch(void* const* d_in, const int* in_sizes, int n_in, void* d_out, int out_size, void* d_ws, size_t ws_size, hipStream_t stream) {
  const u16* q = (const u16*)d_in[0]; const u16* k = (const u16*)d_in[1]; const u16* v = (const u16*)d_in[2];
  const u16* shk = (const u16*)d_in[3]; const u16* fk = (const u16*)d_in[4]; const u16* sD = (const u16*)d_in[5];
  const u16* wg = (const u16*)d_in[7]; const u16* wu = (const u16*)d_in[8]; const u16* wd = (const u16*)d_in[9];
  char* ws = (char*)d_ws;
  (void)hipFuncSetAttribute((const void*)k0_prep, hipFuncAttributeMaxDynamicSharedMemorySize, K0_LDS);
  (void)hipFuncSetAttribute((const void*)k1_conv, hipFuncAttributeMaxDynamicSharedMemorySize, K1_LDS);
  (void)hipFuncSetAttribute((const void*)k2_ffn, hipFuncAttributeMaxDynamicSharedMemorySize, KF_LDS);
  k0_prep<<<K0_TILES + K0_STATS + K0_NPF, 256, K0_LDS, stream>>>(q, k, v, shk, fk, wg, wu, wd, (const u16*)d_out, ws);
  k1_conv<<<K1_CONV + K1_W1 + K1_W2 + 12, 512, K1_LDS, stream>>>(q, fk, sD, wg, wu, wd, ws);
  k2_ffn<<<256, 512, KF_LDS, stream>>>(ws, (u16*)d_out);
}
```

```cpp
#include <hip/hip_runtime.h>
#include <hip/hip_bf16.h>

typedef unsigned short u16;
typedef unsigned int u32;
typedef __attribute__((ext_vector_type(2))) unsigned int u32x2;
typedef __attribute__((ext_vector_type(4))) unsigned int u32x4;
typedef __attribute__((ext_vector_type(2))) __bf16 bf16x2;
typedef __attribute__((ext_vector_type(2))) float f32x2;
typedef __attribute__((ext_vector_type(4))) float f32x4;
typedef __attribute__((ext_vector_type(4))) unsigned short u16x4;
#define LAS __attribute__((address_space(3)))

static constexpr int S = 2048, D = 640, F = 1728;
static constexpr int NKS1 = D / 32;
static constexpr int NKS2 = F / 32;

static constexpr size_t WS_UR1 = 0;
static constexpr size_t WS_HB  = WS_UR1 + (size_t)D * S * 2;
static constexpr size_t WS_ST1 = WS_HB  + (size_t)D * S * 2;
static constexpr size_t WS_ST2 = WS_ST1 + (size_t)S * 8;
static constexpr size_t WS_W1T = WS_ST2 + (size_t)16 * 1024;
static constexpr int NKS2P = 56;
static constexpr size_t WS_W2T = WS_W1T + (size_t)14 * 8 * NKS1 * 2048;
static constexpr size_t WS_HF  = WS_W2T + (size_t)8 * NKS2P * 5 * 1024;
static constexpr size_t WS_FLP = WS_HF  + (size_t)128 * NKS2 * 1024;

__device__ __forceinline__ float bflo(u32 w) { return __uint_as_float(w << 16); }
__device__ __forceinline__ float bfhi(u32 w) { return __uint_as_float(w & 0xffff0000u); }
__device__ __forceinline__ u32 pack2(float lo, float hi) { f32x2 v = {lo, hi}; bf16x2 b = __builtin_convertvector(v, bf16x2); return __builtin_bit_cast(u32, b); }
__device__ __forceinline__ float relu1(float x) { float r; asm("v_max_f32 %0, 0, %1" : "=v"(r) : "v"(x)); return r; }
__device__ __forceinline__ f32x4 relu4(f32x4 x) { return (f32x4){relu1(x[0]), relu1(x[1]), relu1(x[2]), relu1(x[3])}; }
__device__ __forceinline__ float rbf(float x) { return bflo(pack2(x, 0.f)); }
__device__ __forceinline__ float dot2(u32 a, u32 b, float c) { return __builtin_amdgcn_fdot2_f32_bf16(__builtin_bit_cast(bf16x2, a), __builtin_bit_cast(bf16x2, b), c, false); }
__device__ __forceinline__ void dma16(const void* g, LAS void* l) { __builtin_amdgcn_global_load_lds((const u32*)g, (LAS u32*)l, 16, 0, 0); }
#define WAIT_VM(n) asm volatile("s_waitcnt vmcnt(%0)" ::"n"(n) : "memory")
#define WAIT_LGKM0() asm volatile("s_waitcnt lgkmcnt(0)" ::: "memory")
#define BAR() do { asm volatile("" ::: "memory"); __builtin_amdgcn_s_barrier(); asm volatile("" ::: "memory"); } while (0)
#define MAKE_RSRC(p) __builtin_amdgcn_make_buffer_rsrc((void*)(p), 0, 0x7fffffff, 0x00020000)
#define ST16(rs, off, v) __builtin_amdgcn_raw_buffer_store_b128((v), (rs), (int)(off), 0, 16)

template <int CTRL, int RM> __device__ __forceinline__ float dpp_add(float v) { return v + __builtin_bit_cast(float, __builtin_amdgcn_update_dpp(0, __builtin_bit_cast(int, v), CTRL, RM, 0xf, true)); }
__device__ __forceinline__ float wave_sum(float v) {
  v = dpp_add<0x111, 0xf>(v); v = dpp_add<0x112, 0xf>(v); v = dpp_add<0x114, 0xf>(v); v = dpp_add<0x118, 0xf>(v);
  v = dpp_add<0x142, 0xa>(v); v = dpp_add<0x143, 0xc>(v);
  return __builtin_bit_cast(float, __builtin_amdgcn_readlane(__builtin_bit_cast(int, v), 63));
}

__device__ __forceinline__ u16x4 tr_read(u32 addr) { u16x4 r; asm volatile("ds_read_b64_tr_b16 %0, %1" : "=v"(r) : "v"(addr) : "memory"); return r; }
#define DS_B64(dst, addr, off) asm volatile("ds_read_b64 %0, %1 offset:%2" : "=v"(dst) : "v"(addr), "n"(off))
#define DS_B128(dst, addr, off) asm volatile("ds_read_b128 %0, %1 offset:%2" : "=v"(dst) : "v"(addr), "n"(off))
#define MFMA_IP(acc, a, b) asm volatile("v_mfma_f32_16x16x32_bf16 %0, %1, %2, %0" : "+v"(acc) : "v"(a), "v"(b))
#define MFMA_Z(acc, a, b) asm volatile("v_mfma_f32_16x16x32_bf16 %0, %1, %2, 0" : "=&v"(acc) : "v"(a), "v"(b))
#define GLD16(dst, voff, sbase, off) asm volatile("global_load_dwordx4 %0, %1, %2 offset:%3" : "=v"(dst) : "v"(voff), "s"(sbase), "n"(off) : "memory")

static constexpr int K0_TILES = 32 * 10, K0_STATS = 128, K0_NPF = 8 * 11;
static constexpr int K0_RAW = 17 * 1024, K0_LDS = K0_RAW + 64 * 144;
static constexpr int CODE_PF1 = 10300;
static constexpr int CODE_PF2 = 29000;
__global__ __launch_bounds__(256) void k0_prep(const u16* __restrict__ x, const u16* __restrict__ k, const u16* __restrict__ v,
                                               const u16* __restrict__ shk, const u16* __restrict__ fk, char* __restrict__ ws) {
  extern __shared__ __attribute__((aligned(16))) char shm[];
  LAS char* lds = (LAS char*)shm;
  const int t = threadIdx.x, lane = t & 63, w = __builtin_amdgcn_readfirstlane(t >> 6), b = blockIdx.x;
  const auto rs = MAKE_RSRC(ws);
  if (b < K0_TILES) {
    const int st = b / 10, ct = b % 10, s0 = st * 64, c0 = ct * 64;
#pragma unroll
    for (int i = 0; i < 5; ++i) {
      const int pc = w + 4 * i;
      if (pc < 17) {
        const u16* src = pc < 9 ? k : v;
        int row = (pc < 9 ? s0 - 8 + 8 * pc : s0 + 8 * (pc - 9)) + (lane >> 3);
        row = row < 0 ? 0 : row;
        dma16(src + (size_t)row * D + c0 + (lane & 7) * 8, lds + pc * 1024);
      }
    }
    const int cp = t & 31, rg = t >> 5, ch = c0 + 2 * cp;
    const u32x4 sh = *(const u32x4*)(shk + ch * 4);
    WAIT_VM(0); __syncthreads();
    u32 kk[11], vv[8];
    LAS const char* rk = lds + (8 * rg + 5) * 128 + cp * 4;
#pragma unroll
    for (int i = 0; i < 11; ++i) kk[i] = *(LAS const u32*)(rk + i * 128);
#pragma unroll
    for (int i = 0; i < 8; ++i) vv[i] = *(LAS const u32*)(lds + 9 * 1024 + (8 * rg + i) * 128 + cp * 4);
    if (st == 0 && rg == 0) { kk[0] = kk[1] = kk[2] = 0u; }
    const float a0 = bflo(sh.x), a1 = bfhi(sh.x), a2 = bflo(sh.y), a3 = bfhi(sh.y);
    const float b0 = bflo(sh.z), b1 = bfhi(sh.z), b2 = bflo(sh.w), b3 = bfhi(sh.w);
    float ka[11], kb[11];
#pragma unroll
    for (int i = 0; i < 11; ++i) { ka[i] = bflo(kk[i]); kb[i] = bfhi(kk[i]); }
    float ua[8], ub[8];
#pragma unroll
    for (int i = 0; i < 8; ++i) {
      ua[i] = (ka[i] * a3 + ka[i + 1] * a2 + ka[i + 2] * a1 + ka[i + 3] * a0) * bflo(vv[i]);
      ub[i] = (kb[i] * b3 + kb[i + 1] * b2 + kb[i + 2] * b1 + kb[i + 3] * b0) * bfhi(vv[i]);
    }
    LAS char* L1 = lds + K0_RAW;
    const int o = (7 - rg) * 16;
    *(LAS u32x4*)(L1 + (2 * cp) * 144 + o) = (u32x4){pack2(ua[7], ua[6]), pack2(ua[5], ua[4]), pack2(ua[3], ua[2]), pack2(ua[1], ua[0])};
    *(LAS u32x4*)(L1 + (2 * cp + 1) * 144 + o) = (u32x4){pack2(ub[7], ub[6]), pack2(ub[5], ub[4]), pack2(ub[3], ub[2]), pack2(ub[1], ub[0])};
    __syncthreads();
    u32x4 o1[2];
#pragma unroll
    for (int i = 0; i < 2; ++i) o1[i] = *(LAS const u32x4*)(L1 + ((t >> 3) + 32 * i) * 144 + (t & 7) * 16);
#pragma unroll
    for (int i = 0; i < 2; ++i) ST16(rs, WS_UR1 + ((size_t)(c0 + (t >> 3) + 32 * i) * S + (1984 - s0)) * 2 + (t & 7) * 16, o1[i]);
  } else if (b >= K0_TILES + K0_STATS) {
    const int i = (b - K0_TILES - K0_STATS) >> 3;
    const char* p;
    if (i == 0) {
      unsigned long long pc;
      asm volatile("s_getpc_b64 s[40:41]\n\ts_add_u32 s40, s40, _Z7k1_convPKtS0_S0_S0_S0_S0_Pc@rel32@lo+4\n\ts_addc_u32 s41, s41, _Z7k1_convPKtS0_S0_S0_S0_S0_Pc@rel32@hi+12\n\ts_mov_b64 %0, s[40:41]" : "=s"(pc) :: "scc", "s40", "s41");
      int o = w * 8192 + lane * 128; o = o < CODE_PF1 - 4 ? o : CODE_PF1 - 4;
      p = (const char*)pc + (o & ~3);
    } else {
      { const unsigned long long t0 = __builtin_amdgcn_s_memrealtime(); while (__builtin_amdgcn_s_memrealtime() - t0 < 140ull) __builtin_amdgcn_s_sleep(4); }
      p = (const char*)fk + (size_t)(b & 7) * 80 * S * 2 + (size_t)((i - 1) * 4 + w) * 8192 + lane * 128;
    }
    u32 sink;
    asm volatile("global_load_dword %0, %1, off\n\ts_waitcnt vmcnt(0)" : "=&v"(sink) : "v"(p) : "memory");
  } else {
    const int rbase = (b - K0_TILES) * 16 + w * 4;
    u32x4 xa[4], xb[4];
#pragma unroll
    for (int r = 0; r < 4; ++r) {
      xa[r] = *(const u32x4*)(x + (size_t)(rbase + r) * D + lane * 8);
      xb[r] = *(const u32x4*)(x + (size_t)(rbase + r) * D + 512 + (lane & 15) * 8);
    }
    const float mk = lane < 16 ? 1.f : 0.f;
    float mean[4], q[4];
#pragma unroll
    for (int r = 0; r < 4; ++r) {
      float s = 0.f, s2 = 0.f;
#pragma unroll
      for (int j = 0; j < 4; ++j) { s += bflo(xa[r][j]) + bfhi(xa[r][j]); s2 += bflo(xb[r][j]) + bfhi(xb[r][j]); }
      mean[r] = wave_sum(s + mk * s2) * (1.0f / D);
    }
#pragma unroll
    for (int r = 0; r < 4; ++r) {
      float s = 0.f, s2 = 0.f;
#pragma unroll
      for (int j = 0; j < 4; ++j) {
        float a = bflo(xa[r][j]) - mean[r], c = bfhi(xa[r][j]) - mean[r]; s += a * a + c * c;
        a = bflo(xb[r][j]) - mean[r]; c = bfhi(xb[r][j]) - mean[r]; s2 += a * a + c * c;
      }
      q[r] = wave_sum(s + mk * s2) * (1.0f / D);
    }
    if (lane < 2) {
      const float m0 = lane ? mean[2] : mean[0], m1 = lane ? mean[3] : mean[1], v0 = lane ? q[2] : q[0], v1 = lane ? q[3] : q[1];
      const float sd0 = rbf(sqrtf(rbf(rbf(v0) + 1e-5f))), sd1 = rbf(sqrtf(rbf(rbf(v1) + 1e-5f)));
      const f32x4 o = {rbf(m0), 1.0f / sd0, rbf(m1), 1.0f / sd1};
      ST16(rs, WS_ST1 + (size_t)(rbase + 2 * lane) * 8, __builtin_bit_cast(u32x4, o));
    }
  }
  if (b == K0_TILES + K0_STATS - 1) ST16(rs, WS_FLP + t * 16, ((u32x4){0u, 0u, 0u, 0u}));
}


typedef __attribute__((address_space(1))) unsigned gu32;
__device__ __forceinline__ void await_flags(char* ws, size_t off, int n, int lane) {
  const unsigned long long t0 = __builtin_amdgcn_s_memrealtime();
  for (;;) {
    bool ok = true;
#pragma unroll
    for (int i = 0; i < 4; ++i) {
      const int idx = lane + 64 * i;
      if (64 * i < n) { const unsigned v = __hip_atomic_load((gu32*)(ws + off + (size_t)(idx < n ? idx : 0) * 16), __ATOMIC_RELAXED, __HIP_MEMORY_SCOPE_AGENT); ok &= v == 1u; }
    }
    if (__all(ok)) break;
    if (__builtin_amdgcn_s_memrealtime() - t0 > 400000ull) break;
    __builtin_amdgcn_s_sleep(1);
  }
  asm volatile("" ::: "memory");
}

static constexpr int CH_BYTES = 13568, CH_F = 0, CH_U1 = 4224, CH_U0 = 4224 + 4608 + 32;
static constexpr int K1_YL = 4 * CH_BYTES;
static constexpr int K1_YP = 8192 + 64;
static constexpr unsigned long long K1_DELAY = 80;
static constexpr int K1_CONV = 160, K1_W1 = 54, K1_W2 = 30;

template <bool W1>
__device__ __forceinline__ void retile_job(LAS char* lds, const u16* __restrict__ src, int pitch, int cg, int mat, int ks0, char* ws, int w, int lane) {
  constexpr int NU = W1 ? 10 : 9;
  const auto rs = MAKE_RSRC(ws);
#pragma unroll
  for (int i = 0; i < NU; ++i) {
    const int pc = w + 8 * i, row = 8 * pc + (lane >> 3);
    dma16(src + (size_t)(ks0 * 32 + row) * pitch + (((lane & 7) ^ (row & 7)) * 8), lds + pc * 1024);
  }
  WAIT_VM(0); BAR();
  const int g = lane >> 4, q = (lane & 15) >> 2, p = lane & 3, fb = w & 3;
  const u32 l0 = (u32)(size_t)lds;
  u16x4 lo[NU], hi[NU];
#pragma unroll
  for (int i = 0; i < NU; ++i) {
    const int ks = (w >> 2) + 2 * i, row0 = 32 * ks + 8 * g + q, row1 = row0 + 4, chk = 2 * fb + (p >> 1);
    lo[i] = tr_read(l0 + row0 * 128 + ((chk ^ (row0 & 7)) * 16) + 8 * (p & 1));
    hi[i] = tr_read(l0 + row1 * 128 + ((chk ^ (row1 & 7)) * 16) + 8 * (p & 1));
  }
#pragma unroll
  for (int i = 0; i < NU; ++i) asm volatile("s_waitcnt lgkmcnt(0)" : "+v"(lo[i]), "+v"(hi[i])::"memory");
  float m = 0.f;
  if (W1) {
    float cs = 0.f;
#pragma unroll
    for (int i = 0; i < NU; ++i) {
      const u32x2 l2 = __builtin_bit_cast(u32x2, lo[i]), h2 = __builtin_bit_cast(u32x2, hi[i]);
      cs = dot2(l2.x, 0x3f803f80u, dot2(l2.y, 0x3f803f80u, dot2(h2.x, 0x3f803f80u, dot2(h2.y, 0x3f803f80u, cs))));
    }
    cs += __shfl_xor(cs, 16); cs += __shfl_xor(cs, 32);
    LAS float* red = (LAS float*)(lds + 80 * 1024);
    if (lane < 16) red[w * 16 + lane] = cs;
    WAIT_LGKM0(); BAR();
    m = (red[fb * 16 + (lane & 15)] + red[(fb + 4) * 16 + (lane & 15)]) * (1.0f / D);
  }
#pragma unroll
  for (int i = 0; i < NU; ++i) {
    const int ks = (w >> 2) + 2 * i;
    const u32x2 l2 = __builtin_bit_cast(u32x2, lo[i]), h2 = __builtin_bit_cast(u32x2, hi[i]);
    if (W1) {
      const u32x4 o = {pack2(bflo(l2.x) - m, bfhi(l2.x) - m), pack2(bflo(l2.y) - m, bfhi(l2.y) - m), pack2(bflo(h2.x) - m, bfhi(h2.x) - m), pack2(bflo(h2.y) - m, bfhi(h2.y) - m)};
      const int blk = cg * 4 + fb;
      ST16(rs, WS_W1T + ((size_t)(blk * NKS1 + ks) * 2 + mat) * 1024 + lane * 16, o);
    } else {
      const int df = cg * 4 + fb, dt = df / 5, j = df % 5;
      ST16(rs, WS_W2T + ((size_t)(dt * NKS2P + ks0 + ks) * 5 + j) * 1024 + lane * 16, ((u32x4){l2.x, l2.y, h2.x, h2.y}));
    }
  }
  if (!W1 && ks0 == 36 && cg < 8) {
#pragma unroll
    for (int i = 0; i < 2; ++i) { const int pc = w + 8 * i; if (pc < 10) ST16(rs, WS_W2T + ((size_t)(cg * NKS2P + NKS2) * 5 + pc) * 1024 + lane * 16, ((u32x4){0u, 0u, 0u, 0u})); }
  }
  if (W1 && cg == 26 && mat == 0) {
#pragma unroll
    for (int i = 0; i < 3; ++i) {
      const int ks = w + 8 * i;
      if (ks < NKS1) { ST16(rs, WS_W1T + ((size_t)(108 * NKS1 + ks) * 2) * 1024 + lane * 16, ((u32x4){0x3f803f80u, 0x3f803f80u, 0x3f803f80u, 0x3f803f80u})); ST16(rs, WS_W1T + ((size_t)(108 * NKS1 + ks) * 2 + 1) * 1024 + lane * 16, ((u32x4){0u, 0u, 0u, 0u})); }
    }
  }
}


__device__ __forceinline__ void conv_phase(LAS char* lds, const u16* __restrict__ x, const u16* __restrict__ fk, const u16* __restrict__ sD, char* __restrict__ ws, int b, int t, int lane, int w) {
  const auto rs = MAKE_RSRC(ws);
  const int cgp = (b & 7) * 20 + (b >> 3);
  const int c = w & 3, hf = w >> 2, d = cgp * 4 + c;
  LAS char* cb = lds + c * CH_BYTES;
  {
    const char* gF = (const char*)fk + (size_t)d * S * 2 + lane * 16;
    const char* g1 = ws + WS_UR1 + (size_t)d * S * 2 + lane * 16;
#pragma unroll
    for (int i = 0; i < 2; ++i) {
      const int pc = hf * 2 + i;
      dma16(g1 + pc * 1024, cb + CH_U1 + pc * 1024);
      dma16(gF + pc * 1024, cb + CH_F + 64 + pc * 1024);
    }
    if (hf == 0) { if (lane < 32) *(LAS u32x4*)(cb + CH_U1 + 4096 + lane * 16) = (u32x4){0, 0, 0, 0}; if (lane < 4) *(LAS u32x4*)(cb + CH_F + lane * 16) = (u32x4){0, 0, 0, 0}; }
    else { if (lane < 32) *(LAS u32x4*)(cb + CH_U0 + 4096 + lane * 16) = (u32x4){0, 0, 0, 0}; }
  }
  const u32x2 dw = *(const u32x2*)(sD + cgp * 4);
  WAIT_VM(0); WAIT_LGKM0(); BAR();
#pragma unroll
  for (int i = 0; i < 2; ++i) {
    const int p0 = 1024 * hf + 512 * i + 8 * lane;
    const u32x4 in = *(LAS const u32x4*)(cb + CH_U1 + p0 * 2); const u32 nx = *(LAS const u32*)(cb + CH_U1 + p0 * 2 + 16);
    *(LAS u32x4*)(cb + CH_U0 + p0 * 2) = (u32x4){__builtin_amdgcn_alignbit(in.y, in.x, 16), __builtin_amdgcn_alignbit(in.z, in.y, 16), __builtin_amdgcn_alignbit(in.w, in.z, 16), __builtin_amdgcn_alignbit(nx, in.w, 16)};
  }
  WAIT_LGKM0(); BAR();
  u32x2 xr[4]; u32x4 stv[2];
  const char* gx = (const char*)x + (size_t)(2 * t) * D * 2 + cgp * 8;
#define C_LDX(i) do { \
      if ((i) == 0) asm volatile("global_load_dwordx2 %0, %1, off" : "=v"(xr[0]) : "v"(gx) : "memory"); \
      if ((i) == 1) asm volatile("global_load_dwordx2 %0, %1, off offset:1280" : "=v"(xr[1]) : "v"(gx) : "memory"); \
      if ((i) == 2) asm volatile("global_load_dwordx2 %0, %1, off" : "=v"(xr[2]) : "v"(gx + (size_t)1024 * D * 2) : "memory"); \
      if ((i) == 3) asm volatile("global_load_dwordx2 %0, %1, off offset:1280" : "=v"(xr[3]) : "v"(gx + (size_t)1024 * D * 2) : "memory"); \
      if ((i) == 4) GLD16(stv[0], t * 16, ws + WS_ST1, 0); \
      if ((i) == 5) GLD16(stv[1], t * 16, ws + WS_ST1 + 8192, 0); } while (0)
#define C_XWAIT() asm volatile("s_waitcnt vmcnt(0) lgkmcnt(0)" : "+v"(xr[0]), "+v"(xr[1]), "+v"(xr[2]), "+v"(xr[3]), "+v"(stv[0]), "+v"(stv[1])::"memory")
  if (hf == 0) {
    const int r = lane & 15, g = lane >> 4;
    f32x4 acc[8];
    const int a = r >> 1, e = r & 1;
    const u32 ub = (u32)(size_t)(cb + (e ? CH_U1 : CH_U0) + 448 - 64 * a + 16 * g);
    u32 fbp[4];
#pragma unroll
    for (int i = 0; i < 4; ++i) fbp[i] = (u32)(size_t)(cb + CH_F + 16 * g + 4 * r + 4) + 1024 * i;
    u32x4 U[2][8]; u32x2 Fr[2][8][2];
#define C_RDU(buf, i, cc) DS_B128(U[buf][i], ub, 512 * (7 - (i)) + 64 * (cc))
#define C_RDF(buf, i, cc, hh) asm volatile("ds_read2_b32 %0, %1 offset0:%2 offset1:%3" : "=v"(Fr[buf][i][hh]) : "v"(fbp[(i) >> 1]), "n"(128 * ((i) & 1) + 16 * (cc) + 2 * (hh)), "n"(128 * ((i) & 1) + 16 * (cc) + 2 * (hh) + 1))
#define C_F(buf, i) ((u32x4){Fr[buf][i][0].x, Fr[buf][i][0].y, Fr[buf][i][1].x, Fr[buf][i][1].y})
#define C_WAIT(buf) do { \
      asm volatile("s_waitcnt lgkmcnt(0)" : "+v"(U[buf][0]), "+v"(U[buf][1]), "+v"(U[buf][2]), "+v"(U[buf][3]), "+v"(U[buf][4]), "+v"(U[buf][5]), "+v"(U[buf][6]), "+v"(U[buf][7])::"memory"); \
      asm volatile("" : "+v"(Fr[buf][0][0]), "+v"(Fr[buf][0][1]), "+v"(Fr[buf][1][0]), "+v"(Fr[buf][1][1]), "+v"(Fr[buf][2][0]), "+v"(Fr[buf][2][1]), "+v"(Fr[buf][3][0]), "+v"(Fr[buf][3][1]), \
                        "+v"(Fr[buf][4][0]), "+v"(Fr[buf][4][1]), "+v"(Fr[buf][5][0]), "+v"(Fr[buf][5][1]), "+v"(Fr[buf][6][0]), "+v"(Fr[buf][6][1]), "+v"(Fr[buf][7][0]), "+v"(Fr[buf][7][1])); } while (0)
#pragma unroll
    for (int i = 0; i < 8; ++i) { C_RDF(0, i, 0, 0); C_RDF(0, i, 0, 1); C_RDU(0, i, 0); }
    C_WAIT(0);
#pragma unroll
    for (int cc = 0; cc < 8; ++cc) {
      const int cur = cc & 1, nxt = cur ^ 1;
      int n = 0;
#pragma unroll
      for (int c1 = 0; c1 < 8; ++c1)
#pragma unroll
        for (int i = c1; i < 8; ++i) {
          if (cc == 0 && c1 == 0) MFMA_Z(acc[i], U[cur][i - c1], C_F(cur, c1)); else MFMA_IP(acc[i], U[cur][i - c1], C_F(cur, c1));
          if (cc < 7 && n < 24) { const int j = n / 3, kk = n % 3; if (kk == 0) C_RDU(nxt, j, cc + 1); else C_RDF(nxt, j, cc + 1, kk - 1); }
          if (cc < 6 && n == 28) C_LDX(cc);
          ++n;
        }
      if (cc < 7) C_WAIT(nxt);
    }
    asm volatile("s_nop 15\n\ts_nop 15" ::: "memory");
    asm volatile("" : "+v"(acc[0]), "+v"(acc[1]), "+v"(acc[2]), "+v"(acc[3]), "+v"(acc[4]), "+v"(acc[5]), "+v"(acc[6]), "+v"(acc[7]));
    LAS char* yl = lds + K1_YL + c * K1_YP;
#pragma unroll
    for (int j = 0; j < 8; ++j)
#pragma unroll
      for (int hh = 0; hh < 2; ++hh) *(LAS f32x2*)(yl + (256 * j + 32 * (2 * g + hh) + 2 * r) * 4) = (f32x2){acc[j][2 * hh], acc[j][2 * hh + 1]};
    C_XWAIT();
  } else {
    C_LDX(0); C_LDX(1); C_LDX(2); C_LDX(3); C_LDX(4); C_LDX(5);
    C_XWAIT();
  }
  BAR();
#pragma unroll
  for (int i = 0; i < 2; ++i) {
    const int s = 2 * t + 1024 * i;
    f32x2 y2[4];
    const float Dd[4] = {bflo(dw.x), bfhi(dw.x), bflo(dw.y), bfhi(dw.y)};
#pragma unroll
    for (int cc = 0; cc < 4; ++cc) {
      const f32x2 y = *(LAS const f32x2*)(lds + K1_YL + cc * K1_YP + s * 4);
      const u32 uw = *(LAS const u32*)(lds + cc * CH_BYTES + CH_U1 + (2046 - s) * 2);
      y2[cc] = (f32x2){y[0] + bfhi(uw) * Dd[cc], y[1] + bflo(uw) * Dd[cc]};
    }
    const f32x4 st = __builtin_bit_cast(f32x4, stv[i]);
    const u32x2 x0 = xr[2 * i], x1 = xr[2 * i + 1];
    const float xa[4] = {bflo(x0.x), bfhi(x0.x), bflo(x0.y), bfhi(x0.y)}, xb[4] = {bflo(x1.x), bfhi(x1.x), bflo(x1.y), bfhi(x1.y)};
    float ha[4], hb[4];
#pragma unroll
    for (int cc = 0; cc < 4; ++cc) { ha[cc] = xa[cc] + (xa[cc] - st[0]) * st[1] * y2[cc][0]; hb[cc] = xb[cc] + (xb[cc] - st[2]) * st[3] * y2[cc][1]; }
    ST16(rs, WS_HB + ((size_t)cgp * S + s) * 8, ((u32x4){pack2(ha[0], ha[1]), pack2(ha[2], ha[3]), pack2(hb[0], hb[1]), pack2(hb[2], hb[3])}));
  }
}

#define DMA_PL(m0v, voffv, sbase, off) asm volatile("s_mov_b32 m0, %0\n\ts_nop 0\n\tglobal_load_lds_dwordx4 %1, %2 offset:%3" :: "s"(m0v), "v"(voffv), "s"(sbase), "n"(off) : "memory")
#define GLD16C(dst, voff, sbase, off) asm volatile("global_load_dwordx4 %0, %1, %2 offset:%3 sc1" : "=v"(dst) : "v"(voff), "s"(sbase), "n"(off) : "memory")
static constexpr int G1_NS = 12, G1_SLOT = 8192, G1_WR = 4, G1_RING = 64 * 1024;
static constexpr int KF_CNT = 60 * 1024;
__device__ __forceinline__ void gemm2_prefetch(LAS char* lds, char* __restrict__ ws, int dt, int lane, int first, int last, int step) {
  const u32 lds0 = (u32)(size_t)lds, voff = lane * 16;
  const char* wb = ws + WS_W2T + (size_t)dt * NKS2P * 5 * 1024;
  for (int pc = first; pc < last; pc += step) {
    int kq, o, dst;
    if (pc < 60) { kq = pc / 15; o = (pc - 15 * kq) * 1024; dst = kq * 15360 + o; }
    else { const int q = pc - 60; kq = q / 20; o = 15360 + (q - 20 * kq) * 1024; dst = G1_RING + kq * 20480 + (q - 20 * kq) * 1024; }
    const u32 m0v = __builtin_amdgcn_readfirstlane(lds0 + dst);
    const char* sb = wb + kq * 14 * 5120 + o;
    DMA_PL(m0v, voff, sb, 0);
  }
}
__device__ __forceinline__ void gemm2_wq_issue(char* __restrict__ ws, int dt, int lane, int w, u32x4 (&wq)[7][3]) {
  const char* wsb = ws + WS_W2T + ((size_t)dt * NKS2P + (w >> 1) * 14 + 7) * 5120 + (w & 1) * 3072;
  const u32 voff = lane * 16;
  const int sh = __builtin_amdgcn_readfirstlane(w & 1);
#pragma unroll
  for (int i = 0; i < 7; ++i) {
    GLD16(wq[i][0], voff, wsb + i * 5120, 0); GLD16(wq[i][1], voff, wsb + i * 5120, 1024);
    asm volatile("s_cmp_lg_u32 %3, 0\n\ts_cbranch_scc1 1f\n\tglobal_load_dwordx4 %0, %1, %2 offset:2048\n1:" : "=&v"(wq[i][2]) : "v"(voff), "s"(wsb + i * 5120), "s"(sh) : "scc", "memory");
  }
}
__device__ __forceinline__ void gemm2_wq_wait(u32x4 (&wq)[7][3]) {
  asm volatile("s_waitcnt vmcnt(0)" : "+v"(wq[0][0]), "+v"(wq[0][1]), "+v"(wq[0][2]), "+v"(wq[1][0]), "+v"(wq[1][1]), "+v"(wq[1][2]), "+v"(wq[2][0]), "+v"(wq[2][1]), "+v"(wq[2][2]),
                                      "+v"(wq[3][0]), "+v"(wq[3][1]), "+v"(wq[3][2])::"memory");
  asm volatile("" : "+v"(wq[4][0]), "+v"(wq[4][1]), "+v"(wq[4][2]), "+v"(wq[5][0]), "+v"(wq[5][1]), "+v"(wq[5][2]), "+v"(wq[6][0]), "+v"(wq[6][1]), "+v"(wq[6][2]));
}

__device__ __forceinline__ void gemm1_phase(LAS char* lds, char* __restrict__ ws, int rt, int ct, int dt2, int lane, int w, unsigned tag, size_t flag_off, u32x4 (&wq)[7][3]) {
  const u32 lds0 = (u32)(size_t)lds + G1_RING;
  const int s0 = rt * 128;
  const u32 voff = lane * 16;
  const auto rs = MAKE_RSRC(ws);
  if (w >= 4) {
    const int j = w - 4;
    const u32 hv = voff ^ ((j & 1) * 128);
    const char* hsb = ws + WS_HB + ((size_t)(2 * j) * S + s0) * 8;
    const u32 ldsw = lds0 + 2 * j * 1024;
#define G1_SLAB(ks) do { const char* hp_ = hsb + (size_t)(ks) * 8 * S * 8; DMA_PL(ldsw + ((ks) % G1_NS) * G1_SLOT, hv, hp_, 0); DMA_PL(ldsw + ((ks) % G1_NS) * G1_SLOT + 1024, hv, hp_ + S * 8, 0); } while (0)
    G1_SLAB(0); G1_SLAB(1); G1_SLAB(2); G1_SLAB(3); G1_SLAB(4);
    WAIT_VM(8); BAR();
    G1_SLAB(5); G1_SLAB(6); G1_SLAB(7); G1_SLAB(8);
    WAIT_VM(8); BAR();
    gemm2_prefetch(lds, ws, dt2, lane, j, 20, 4);
    G1_SLAB(9); G1_SLAB(10); G1_SLAB(11);
    WAIT_VM(6); BAR();
    gemm2_prefetch(lds, ws, dt2, lane, 20 + j, 40, 4);
    G1_SLAB(12); G1_SLAB(13); G1_SLAB(14); G1_SLAB(15);
    WAIT_VM(6); BAR();
    gemm2_prefetch(lds, ws, dt2, lane, 40 + j, 60, 4);
    G1_SLAB(16); G1_SLAB(17); G1_SLAB(18); G1_SLAB(19);
    WAIT_VM(6); BAR();
    gemm2_wq_issue(ws, dt2, lane, w, wq);
    WAIT_VM(14); BAR();
    BAR();
    gemm2_prefetch(lds, ws, dt2, lane, 60 + j, 140, 4);
    gemm2_wq_wait(wq);
  } else {
    const int blk0 = ct * 8 + 2 * w;
    const char* wsA = ws + WS_W1T + (size_t)(blk0 > 108 ? 108 : blk0) * NKS1 * 2048;
    const char* wsB = ws + WS_W1T + (size_t)(blk0 + 1 > 108 ? 108 : blk0 + 1) * NKS1 * 2048;
    const int g = lane >> 4;
    const u32 hbase = lds0 + g * 2048 + (lane & 15) * 8;
    const u32 hoffE = hbase + (g & 1) * 128, hoffO = hbase + (1 - (g & 1)) * 128 - 128;
    const u32 hoffE2 = hoffE + 65536, hoffO2 = hoffO + 65536;
    f32x4 acc[8][4];
    const int stat = __builtin_amdgcn_readfirstlane(blk0 == 108 ? 1 : 0);
#define G1_LDW(ks, i) do { if ((i) == 0) GLD16(wr[(ks) % G1_WR][0], voff, wsA + (ks) * 2048, 0); if ((i) == 1) GLD16(wr[(ks) % G1_WR][1], voff, wsA + (ks) * 2048, 1024); \
      if ((i) == 2) GLD16(wr[(ks) % G1_WR][2], voff, wsB + (ks) * 2048, 0); if ((i) == 3) GLD16(wr[(ks) % G1_WR][3], voff, wsB + (ks) * 2048, 1024); } while (0)
#define G1_RD(sf, hh, ks) DS_B64(hf[sf][hh], ((ks) % G1_NS) >= 8 ? (((sf) & 1) ? hoffO2 : hoffE2) : (((sf) & 1) ? hoffO : hoffE), (((ks) % G1_NS) & 7) * G1_SLOT + (hh) * 1024 + (sf) * 128)
#define G1_H(sf) ((u32x4){hf[sf][0].x, hf[sf][0].y, hf[sf][1].x, hf[sf][1].y})
#define G1_MM(ks, sf, i, STAT) do { \
      if (STAT) { if ((i) == 0) { if ((ks) == 0) MFMA_Z(acc[sf][0], wr[0][0], G1_H(sf)); else MFMA_IP(acc[sf][0], wr[(ks) % G1_WR][0], G1_H(sf)); } \
                  if ((i) == 2) { if ((ks) == 0) MFMA_Z(acc[sf][2], G1_H(sf), G1_H(sf)); else MFMA_IP(acc[sf][2], G1_H(sf), G1_H(sf)); } } \
      else { if ((ks) == 0) MFMA_Z(acc[sf][i], wr[0][i], G1_H(sf)); else MFMA_IP(acc[sf][i], wr[(ks) % G1_WR][i], G1_H(sf)); } } while (0)
#define G1_BODY(STAT) { \
      u32x4 wr[G1_WR][4]; u32x2 hf[8][2]; \
      _Pragma("unroll") for (int ks = 0; ks < 3; ++ks) { G1_LDW(ks, 0); G1_LDW(ks, 1); G1_LDW(ks, 2); G1_LDW(ks, 3); } \
      BAR();                                                   \
      _Pragma("unroll") for (int sf = 0; sf < 7; ++sf) { G1_RD(sf, 0, 0); G1_RD(sf, 1, 0); } \
      _Pragma("unroll") for (int ks = 0; ks < NKS1; ++ks) { \
        if ((ks & 3) == 0) BAR(); \
          \
        if (ks + 2 < NKS1) WAIT_VM(8); else if (ks + 1 < NKS1) WAIT_VM(4); else WAIT_VM(0); \
        asm volatile("" : "+v"(wr[ks % G1_WR][0]), "+v"(wr[ks % G1_WR][1]), "+v"(wr[ks % G1_WR][2]), "+v"(wr[ks % G1_WR][3])); \
        _Pragma("unroll") for (int sf = 0; sf < 8; ++sf) { \
            \
          if (ks + 1 < NKS1 || sf == 0) asm volatile("s_waitcnt lgkmcnt(12)" : "+v"(hf[sf][0]), "+v"(hf[sf][1])::"memory"); \
          else if (sf < 7) asm volatile("s_waitcnt lgkmcnt(%2)" : "+v"(hf[sf][0]), "+v"(hf[sf][1]) : "n"(14 - 2 * sf) : "memory"); \
          else asm volatile("s_waitcnt lgkmcnt(0)" : "+v"(hf[sf][0]), "+v"(hf[sf][1])::"memory"); \
          G1_MM(ks, sf, 0, STAT); \
          if (sf == 0) G1_RD(7, 0, ks); else if (ks + 1 < NKS1) G1_RD(sf - 1, 0, ks + 1); \
          G1_MM(ks, sf, 1, STAT); \
          if (sf == 0) G1_RD(7, 1, ks); else if (ks + 1 < NKS1) G1_RD(sf - 1, 1, ks + 1); \
          G1_MM(ks, sf, 2, STAT); \
          if ((sf & 1) && ks + 3 < NKS1) G1_LDW(ks + 3, sf >> 1); \
          G1_MM(ks, sf, 3, STAT); \
        } } }
    if (stat) G1_BODY(true) else G1_BODY(false)
    BAR();
    asm volatile("s_nop 15\n\ts_nop 15" ::: "memory");
#pragma unroll
    for (int i = 0; i < 8; ++i) asm volatile("" : "+v"(acc[i][0]), "+v"(acc[i][1]), "+v"(acc[i][2]), "+v"(acc[i][3]));
    if (stat) {
      const int sx = lane & 15;
#pragma unroll
      for (int sp = 0; sp < 2; ++sp) {
        float r2[4];
#pragma unroll
        for (int q = 0; q < 4; ++q) {
          const int sf = 4 * sp + q;
          const float dg = (sx & 2) ? ((sx & 1) ? acc[sf][2][3] : acc[sf][2][2]) : ((sx & 1) ? acc[sf][2][1] : acc[sf][2][0]);
          const float ssq = __shfl(dg, (sx >> 2) * 16 + sx);
          const float mean = acc[sf][0][0] * (1.0f / D);
          const float var = fmaxf(ssq * (1.0f / D) - mean * mean, 0.f);
          const float r = 1.0f / rbf(sqrtf(rbf(rbf(var) + 1e-5f)));
          r2[q] = r * r;
        }
        const float mine = g == 0 ? r2[0] : (g == 1 ? r2[1] : (g == 2 ? r2[2] : r2[3]));
        const int b4 = lane & ~3;
        const f32x4 o = {__shfl(mine, b4), __shfl(mine, b4 + 1), __shfl(mine, b4 + 2), __shfl(mine, b4 + 3)};
        if ((lane & 3) == 0) ST16(rs, WS_ST2 + (size_t)(s0 + 64 * sp + lane) * 4, __builtin_bit_cast(u32x4, o));
      }
    } else {
#pragma unroll
      for (int bi = 0; bi < 2; ++bi) {
        const int blk = blk0 + bi;
        if (blk < 108) {
          const u32 obase = (u32)(WS_HF + ((size_t)(rt * 8 + (lane >> 5)) * NKS2 + (blk >> 1)) * 1024) + (blk & 1) * 512 + (lane & 31) * 16;
#pragma unroll
          for (int pr = 0; pr < 4; ++pr) {
            const f32x4 hE = relu4(acc[2 * pr][2 * bi]) * acc[2 * pr][2 * bi + 1], hO = relu4(acc[2 * pr + 1][2 * bi]) * acc[2 * pr + 1][2 * bi + 1];
            u32 e0 = pack2(hE[0], hE[1]), e1 = pack2(hE[2], hE[3]), o0 = pack2(hO[0], hO[1]), o1 = pack2(hO[2], hO[3]);
            auto r0 = __builtin_amdgcn_permlane32_swap(e0, o0, false, false); auto r0b = __builtin_amdgcn_permlane16_swap(r0[0], r0[1], false, false);
            auto r1 = __builtin_amdgcn_permlane32_swap(e1, o1, false, false); auto r1b = __builtin_amdgcn_permlane16_swap(r1[0], r1[1], false, false);
            ST16(rs, obase + pr * 2 * NKS2 * 1024, ((u32x4){r0b[0], r1b[0], r0b[1], r1b[1]}));
          }
        }
      }
    }
    WAIT_VM(0);
    unsigned old = 0;
    if (lane == 0) old = __hip_atomic_fetch_add((LAS unsigned*)(lds + KF_CNT), 1u, __ATOMIC_RELAXED, __HIP_MEMORY_SCOPE_WORKGROUP);
    if (lane == 0 && old == 3u) __hip_atomic_store((gu32*)(ws + flag_off), tag, __ATOMIC_RELAXED, __HIP_MEMORY_SCOPE_AGENT);
    gemm2_wq_issue(ws, dt2, lane, w, wq);
    gemm2_wq_wait(wq);
  }
}

__device__ __forceinline__ void gemm2_phase(LAS char* lds, char* __restrict__ ws, u16* __restrict__ out, int rt, int dt, int lane, int w, u32x4 (&wq)[7][3]) {
  const int sh = w & 1, kq = w >> 1, g = lane >> 4;
  const u32 voff = lane * 16, lds0 = (u32)(size_t)lds;
  const char* hA = ws + WS_HF + ((size_t)(rt * 4 + 2 * sh) * NKS2 + kq * 14) * 1024;
  const char* hB = hA + (size_t)NKS2 * 1024;
  const int hclamp = kq == 3 ? 1 : 0;
  u32x4 hq[6][2], wf[2][5];
#define G2_LH(n) do { const int o_ = ((n) >= 12 && hclamp) ? 11 * 1024 : (n) * 1024; GLD16C(hq[(n) % 6][0], voff, hA + o_, 0); GLD16C(hq[(n) % 6][1], voff, hB + o_, 0); } while (0)
  const u32 rlo = lds0 + kq * 15360 + lane * 16, rhi = lds0 + G1_RING + kq * 20480 - 3 * 5120 + lane * 16;
#define G2_SB(n) ((((n) % 7) < 3 ? rlo : rhi))
#define G2_READ(n) do { DS_B128(wf[(n) & 1][0], G2_SB(n), ((n) % 7) * 5120); DS_B128(wf[(n) & 1][1], G2_SB(n), ((n) % 7) * 5120 + 1024); DS_B128(wf[(n) & 1][2], G2_SB(n), ((n) % 7) * 5120 + 2048); \
    DS_B128(wf[(n) & 1][3], G2_SB(n), ((n) % 7) * 5120 + 3072); DS_B128(wf[(n) & 1][4], G2_SB(n), ((n) % 7) * 5120 + 4096); } while (0)
  const int shs = __builtin_amdgcn_readfirstlane(sh);
#define G2_WST(n, i) do { if ((i) < 2) asm volatile("ds_write_b128 %0, %1 offset:%2" :: "v"(G2_SB(n) + sh * 3072), "v"(wq[(n) - 7][i]), "n"(((n) % 7) * 5120 + (i) * 1024) : "memory"); \
    else asm volatile("s_cmp_lg_u32 %3, 0\n\ts_cbranch_scc1 1f\n\tds_write_b128 %0, %1 offset:%2\n1:" :: "v"(G2_SB(n)), "v"(wq[(n) - 7][2]), "n"(((n) % 7) * 5120 + 2048), "s"(shs) : "scc", "memory"); } while (0)
  G2_LH(0); G2_LH(1);
  float r2[3]; u32x2 res[3];
#pragma unroll
  for (int j = 0; j < 3; ++j) {
    const int f = kq + 4 * j < 10 ? kq + 4 * j : kq, srow = rt * 64 + (2 * sh + f / 5) * 16 + (lane & 15);
    asm volatile("global_load_dword %0, %1, off sc1" : "=v"(r2[j]) : "v"(ws + WS_ST2 + (size_t)srow * 4) : "memory");
    asm volatile("global_load_dwordx2 %0, %1, off" : "=v"(res[j]) : "v"(ws + WS_HB + ((size_t)(dt * 20 + (f % 5) * 4 + g) * S + srow) * 8) : "memory");
  }
  G2_LH(2); G2_LH(3); G2_LH(4);
  f32x4 acc[10];
  G2_READ(0);
  WAIT_VM(14);
#define G2_MM(n, m) do { if ((n) == 0) MFMA_Z(acc[m], wf[0][(m) % 5], hq[0][(m) / 5]); else MFMA_IP(acc[m], wf[(n) & 1][(m) % 5], hq[(n) % 6][(m) / 5]); } while (0)
#define G2_STEP(n) do { \
    if ((n) == 1) WAIT_VM(14); else if ((n) > 1) { constexpr int iss_ = 5 + (n) < 14 ? 5 + (n) : 14; WAIT_VM(2 * (iss_ - ((n) + 1))); } \
    asm volatile("s_waitcnt lgkmcnt(0)" : "+v"(wf[(n) & 1][0]), "+v"(wf[(n) & 1][1]), "+v"(wf[(n) & 1][2]), "+v"(wf[(n) & 1][3]), "+v"(wf[(n) & 1][4]), "+v"(hq[(n) % 6][0]), "+v"(hq[(n) % 6][1])::"memory"); \
    if (((n) & 1) && (n) <= 9) BAR(); \
    if ((n) + 1 < 14) G2_READ(((n) + 1 < 14 ? (n) + 1 : 0)); \
    G2_MM(n, 0); G2_MM(n, 1); if ((n) + 5 < 14) G2_LH(((n) + 5 < 14 ? (n) + 5 : 0)); \
    G2_MM(n, 2); G2_MM(n, 3); if ((n) >= 1 && (n) <= 7) G2_WST(((n) >= 1 && (n) <= 7 ? (n) + 6 : 7), 0); \
    G2_MM(n, 4); G2_MM(n, 5); G2_MM(n, 6); if ((n) >= 1 && (n) <= 7) G2_WST(((n) >= 1 && (n) <= 7 ? (n) + 6 : 7), 1); \
    G2_MM(n, 7); G2_MM(n, 8); if ((n) >= 1 && (n) <= 7) G2_WST(((n) >= 1 && (n) <= 7 ? (n) + 6 : 7), 2); \
    G2_MM(n, 9); } while (0)
  G2_STEP(0); G2_STEP(1); G2_STEP(2); G2_STEP(3); G2_STEP(4); G2_STEP(5); G2_STEP(6);
  G2_STEP(7); G2_STEP(8); G2_STEP(9); G2_STEP(10); G2_STEP(11); G2_STEP(12); G2_STEP(13);
  asm volatile("s_nop 15\n\ts_nop 15" ::: "memory");
  asm volatile("" : "+v"(acc[0]), "+v"(acc[1]), "+v"(acc[2]), "+v"(acc[3]), "+v"(acc[4]), "+v"(acc[5]), "+v"(acc[6]), "+v"(acc[7]), "+v"(acc[8]), "+v"(acc[9]));
  asm volatile("" : "+v"(r2[0]), "+v"(r2[1]), "+v"(r2[2]), "+v"(res[0]), "+v"(res[1]), "+v"(res[2]));
  BAR();
  LAS char* xb = lds + sh * 40960 + lane * 16;
#pragma unroll
  for (int f = 0; f < 10; ++f) *(LAS f32x4*)(xb + kq * 1024 + f * 4096) = acc[f];
  WAIT_LGKM0(); BAR();
  u32 pk[3][2];
#pragma unroll
  for (int j = 0; j < 3; ++j) {
    LAS const char* sl = xb + (kq + 4 * j < 10 ? kq + 4 * j : kq) * 4096;
    const f32x4 a = (*(LAS const f32x4*)(sl) + *(LAS const f32x4*)(sl + 1024)) + (*(LAS const f32x4*)(sl + 2048) + *(LAS const f32x4*)(sl + 3072));
    pk[j][0] = pack2(bflo(res[j].x) + a[0] * r2[j], bfhi(res[j].x) + a[1] * r2[j]);
    pk[j][1] = pack2(bflo(res[j].y) + a[2] * r2[j], bfhi(res[j].y) + a[3] * r2[j]);
  }
  const auto ro = MAKE_RSRC(out);
  {
    auto r0 = __builtin_amdgcn_permlane32_swap(pk[0][0], pk[1][0], false, false); auto r0b = __builtin_amdgcn_permlane16_swap(r0[0], r0[1], false, false);
    auto r1 = __builtin_amdgcn_permlane32_swap(pk[0][1], pk[1][1], false, false); auto r1b = __builtin_amdgcn_permlane16_swap(r1[0], r1[1], false, false);
    const int f = kq + 4 * (lane >> 5);
    ST16(ro, ((size_t)(rt * 64 + (2 * sh + f / 5) * 16 + (lane & 15)) * D + dt * 80 + (f % 5) * 16 + ((lane >> 4) & 1) * 8) * 2, ((u32x4){r0b[0], r1b[0], r0b[1], r1b[1]}));
  }
  if (kq < 2) {
    auto r0 = __builtin_amdgcn_permlane32_swap(pk[2][0], pk[2][0], false, false); auto r0b = __builtin_amdgcn_permlane16_swap(r0[0], r0[1], false, false);
    auto r1 = __builtin_amdgcn_permlane32_swap(pk[2][1], pk[2][1], false, false); auto r1b = __builtin_amdgcn_permlane16_swap(r1[0], r1[1], false, false);
    const int f = kq + 8;
    if (lane < 32) ST16(ro, ((size_t)(rt * 64 + (2 * sh + f / 5) * 16 + (lane & 15)) * D + dt * 80 + (f % 5) * 16 + ((lane >> 4) & 1) * 8) * 2, ((u32x4){r0b[0], r1b[0], r0b[1], r1b[1]}));
  }
}

static constexpr int KF_LDS = 160 * 1024, K1_LDS = 96 * 1024;
__global__ __launch_bounds__(512, 2) void k1_conv(const u16* __restrict__ x, const u16* __restrict__ fk, const u16* __restrict__ sD, const u16* __restrict__ wg,
                                                  const u16* __restrict__ wu, const u16* __restrict__ wd, char* __restrict__ ws) {
  extern __shared__ __attribute__((aligned(16))) char shm[];
  LAS char* lds = (LAS char*)shm;
  const int t = threadIdx.x, lane = t & 63, w = __builtin_amdgcn_readfirstlane(t >> 6), b = blockIdx.x;
  if (b < K1_CONV) conv_phase(lds, x, fk, sD, ws, b, t, lane, w);
  else if (b < K1_CONV + K1_W1 + K1_W2) {
    { const unsigned long long t0 = __builtin_amdgcn_s_memrealtime(); while (__builtin_amdgcn_s_memrealtime() - t0 < K1_DELAY) __builtin_amdgcn_s_sleep(4); }
    const int jb = b - K1_CONV;
    if (jb < K1_W1) retile_job<true>(lds, ((jb & 1) ? wu : wg) + (jb >> 1) * 64, F, jb >> 1, jb & 1, 0, ws, w, lane);
    else { const int j2 = jb - K1_W1; retile_job<false>(lds, wd + (j2 / 3) * 64, D, j2 / 3, 0, (j2 % 3) * 18, ws, w, lane); }
  } else if (w * 8192 < CODE_PF2) {
    unsigned long long pc;
    asm volatile("s_getpc_b64 s[40:41]\n\ts_add_u32 s40, s40, _Z6k2_ffnPcPt@rel32@lo+4\n\ts_addc_u32 s41, s41, _Z6k2_ffnPcPt@rel32@hi+12\n\ts_mov_b64 %0, s[40:41]" : "=s"(pc) :: "scc", "s40", "s41");
    int o = w * 8192 + lane * 128; o = o < CODE_PF2 - 4 ? o : CODE_PF2 - 4;
    u32 sink;
    asm volatile("global_load_dword %0, %1, off\n\ts_waitcnt vmcnt(0)" : "=&v"(sink) : "v"((const char*)pc + (o & ~3)) : "memory");
  }
}

__global__ __launch_bounds__(512, 2) void k2_ffn(char* __restrict__ ws, u16* __restrict__ out) {
  extern __shared__ __attribute__((aligned(16))) char shm[];
  LAS char* lds = (LAS char*)shm;
  const int t = threadIdx.x, lane = t & 63, w = __builtin_amdgcn_readfirstlane(t >> 6), b = blockIdx.x;
  const int xg = b & 7, ix = b >> 3;
  const int p = (xg & 3) * 4 + (ix & 3), j = (xg >> 2) * 8 + (ix >> 2);
  if (t == 0) *(LAS unsigned*)(lds + KF_CNT) = 0u;
  u32x4 wq[7][3];
  if (j < 14) gemm1_phase(lds, ws, p, j, j >> 1, lane, w, 1u, WS_FLP + (size_t)(p * 16 + j) * 16, wq);
  else {
    gemm2_prefetch(lds, ws, j >> 1, lane, w, 140, 8);
    gemm2_wq_issue(ws, j >> 1, lane, w, wq);
    gemm2_wq_wait(wq);
  }
  if (w == 4) await_flags(ws, WS_FLP + (size_t)p * 256, 14, lane);
  BAR();
  gemm2_phase(lds, ws, out, p * 2 + (j & 1), j >> 1, lane, w, wq);
}

extern "C" void kernel_launch(void* const* d_in, const int* in_sizes, int n_in, void* d_out, int out_size, void* d_ws, size_t ws_size, hipStream_t stream) {
  const u16* q = (const u16*)d_in[0]; const u16* k = (const u16*)d_in[1]; const u16* v = (const u16*)d_in[2];
  const u16* shk = (const u16*)d_in[3]; const u16* fk = (const u16*)d_in[4]; const u16* sD = (const u16*)d_in[5];
  const u16* wg = (const u16*)d_in[7]; const u16* wu = (const u16*)d_in[8]; const u16* wd = (const u16*)d_in[9];
  char* ws = (char*)d_ws;
  (void)hipFuncSetAttribute((const void*)k0_prep, hipFuncAttributeMaxDynamicSharedMemorySize, K0_LDS);
  (void)hipFuncSetAttribute((const void*)k1_conv, hipFuncAttributeMaxDynamicSharedMemorySize, K1_LDS);
  (void)hipFuncSetAttribute((const void*)k2_ffn, hipFuncAttributeMaxDynamicSharedMemorySize, KF_LDS);
  k0_prep<<<K0_TILES + K0_STATS + K0_NPF, 256, K0_LDS, stream>>>(q, k, v, shk, fk, ws);
  k1_conv<<<K1_CONV + K1_W1 + K1_W2 + 12, 512, K1_LDS, stream>>>(q, fk, sD, wg, wu, wd, ws);
  k2_ffn<<<256, 512, KF_LDS, stream>>>(ws, (u16*)d_out);
}
```
